```python
import torch

def _softmax(x, axis=-1):
    return torch.softmax(x, dim=axis)

def _gelu(x):
    return torch.nn.functional.gelu(x, approximate='tanh')

def _silu(x):
    return torch.nn.functional.silu(x)

def _sigmoid(x):
    return torch.sigmoid(x)

def _layernorm(x, axis=-1, eps=1e-05):
    m = x.mean(dim=axis, keepdim=True)
    v = x.var(dim=axis, keepdim=True, unbiased=False)
    return (x - m) / torch.sqrt(v + eps)

def _scatter_add(tgt, idx, src, axis=0):
    return tgt.clone().index_add_(axis, idx, src)

def _segment_sum(src, seg, n):
    out = torch.zeros((n,) + src.shape[1:], dtype=src.dtype)
    return out.index_add_(0, seg, src)

def _pad(x, pw):
    flat = tuple((v for pair in reversed(pw) for v in pair))
    return torch.nn.functional.pad(x, flat)
kernel_name = 'parallel_sum__linear_kernel_hedgehog__mixer_gmlp__prompt'

def setup_inputs():

    _w = lambda a: (lambda t: t.to(torch.bfloat16) if t.is_floating_point() else t)(torch.as_tensor(a))

    def _A_setup():
        B, S, D, H = (1, 2048, 576, 32)
        Dh = D // H
        return {'x': _w((lambda *s: torch.randn(*s, dtype=torch.bfloat16) * 0.05)(B, S, D)), 'wqkv': _w((lambda *s: torch.randn(*s, dtype=torch.bfloat16) * 0.4)(D, 3 * D)), 'w_phi': _w((lambda *s: torch.randn(*s, dtype=torch.bfloat16) * 0.4)(H, Dh, Dh) * Dh ** (-0.5)), 'wo': _w((lambda *s: torch.randn(*s, dtype=torch.bfloat16) * 0.4)(D, D))}
    _A_in = _A_setup()

    def _B_setup():
        B, S, D, F = (1, 2048, 576, 4608)
        Fh = F // 2
        return {'x': _w((lambda *s: torch.randn(*s, dtype=torch.bfloat16) * 0.05)(B, S, D)), 'w_in': _w((lambda *s: torch.randn(*s, dtype=torch.bfloat16) * 0.4)(D, 2 * Fh)), 'w_sp': _w(0.01 * (lambda *s: torch.randn(*s, dtype=torch.bfloat16) * 0.4)(S, S)), 'b_sp': _w(1.0 + 0.01 * (lambda *s: torch.randn(*s, dtype=torch.bfloat16) * 0.05)(S)), 'w_out': _w((lambda *s: torch.randn(*s, dtype=torch.bfloat16) * 0.05)(Fh, D))}
    _B_in = _B_setup()
    return {'A_x': _A_in['x'], 'A_wqkv': _A_in['wqkv'], 'A_w_phi': _A_in['w_phi'], 'A_wo': _A_in['wo'], 'B_x': _B_in['x'], 'B_w_in': _B_in['w_in'], 'B_w_sp': _B_in['w_sp'], 'B_b_sp': _B_in['b_sp'], 'B_w_out': _B_in['w_out']}

def reference(A_x, A_wqkv, A_w_phi, A_wo, B_x, B_w_in, B_w_sp, B_b_sp, B_w_out):

    def _A_ref(x, wqkv, w_phi, wo):
        B, S, D = x.shape
        H = 32
        Dh = D // H
        qkv = (lambda x, *d: x.permute(*d))((x @ wqkv).reshape(B, S, 3, H, Dh), 2, 0, 3, 1, 4)
        q, k, v = (qkv[0], qkv[1], qkv[2])
        pq = torch.einsum('bhsd,hdf->bhsf', q, w_phi)
        pk = torch.einsum('bhsd,hdf->bhsf', k, w_phi)
        phi_q = torch.concatenate([_softmax(pq, axis=-1), _softmax(-pq, axis=-1)], axis=-1)
        phi_k = torch.concatenate([_softmax(pk, axis=-1), _softmax(-pk, axis=-1)], axis=-1)
        kv = torch.einsum('bhsf,bhsd->bhfd', phi_k, v)
        z = torch.einsum('bhsf->bhf', phi_k)
        num = torch.einsum('bhqf,bhfd->bhqd', phi_q, kv)
        den = torch.einsum('bhqf,bhf->bhq', phi_q, z)[..., None] + 1e-06
        o = num / den
        return (lambda x, *d: x.permute(*d))(o, 0, 2, 1, 3).reshape(B, S, D) @ wo

    def _B_ref(x, w_in, w_sp, b_sp, w_out):
        B, S, D = x.shape
        F = w_in.shape[-1]
        Fh = F // 2
        z = _gelu(torch.einsum('bsd,df->bsf', x, w_in))
        z1 = z[..., :Fh].clone()
        z2 = _layernorm(z[..., Fh:], axis=-1)
        g = torch.einsum('ts,bsf->btf', w_sp, z2) + b_sp[None, :, None]
        return torch.einsum('bsf,fd->bsd', z1 * g, w_out)
    x = A_x
    xn = _layernorm(x, axis=-1)
    return x + _A_ref(xn, A_wqkv, A_w_phi, A_wo) + _B_ref(xn, B_w_in, B_w_sp, B_b_sp, B_w_out)
```

```cpp
#include <hip/hip_runtime.h>
#include <hip/hip_bf16.h>
typedef unsigned short u16;
__device__ __forceinline__ float b2f(u16 v){ return __uint_as_float(((unsigned)v)<<16); }
__device__ __forceinline__ u16 f2b(float f){ unsigned u=__float_as_uint(f); u += 0x7FFF + ((u>>16)&1); return (u16)(u>>16); }
constexpr int S=2048,D=576,H=32,DH=18,F=4608,FH=2304;

__global__ void k_ln(const u16* x, float* xn){
  int r=blockIdx.x; __shared__ float red[256]; float s=0;
  for(int c=threadIdx.x;c<D;c+=256) s+=b2f(x[r*D+c]);
  red[threadIdx.x]=s; __syncthreads();
  for(int o=128;o;o>>=1){ if(threadIdx.x<o) red[threadIdx.x]+=red[threadIdx.x+o]; __syncthreads(); }
  float m=red[0]/D; __syncthreads(); s=0;
  for(int c=threadIdx.x;c<D;c+=256){ float d=b2f(x[r*D+c])-m; s+=d*d; }
  red[threadIdx.x]=s; __syncthreads();
  for(int o=128;o;o>>=1){ if(threadIdx.x<o) red[threadIdx.x]+=red[threadIdx.x+o]; __syncthreads(); }
  float rs=1.0f/sqrtf(red[0]/D+1e-5f);
  for(int c=threadIdx.x;c<D;c+=256) xn[r*D+c]=(b2f(x[r*D+c])-m)*rs;
}
template<int MODE> __global__ void k_gemm(const float* A,int lda,const u16* B,int ldb,float* C,int ldc,int M,int N,int K){
  __shared__ float As[32][33], Bs[32][33];
  int tx=threadIdx.x&31, ty=threadIdx.x>>5; int row0=blockIdx.y*32,col0=blockIdx.x*32; float acc[4]={0,0,0,0};
  for(int k0=0;k0<K;k0+=32){
    for(int i=0;i<4;i++){ int r=ty+8*i; As[r][tx]=A[(size_t)(row0+r)*lda+k0+tx]; Bs[r][tx]=b2f(B[(size_t)(k0+r)*ldb+col0+tx]); }
    __syncthreads();
    for(int k=0;k<32;k++){ float b=Bs[k][tx]; for(int i=0;i<4;i++) acc[i]+=As[ty+8*i][k]*b; }
    __syncthreads();
  }
  for(int i=0;i<4;i++){ float v=acc[i]; if(MODE==1){ float u=0.7978845608028654f*(v+0.044715f*v*v*v); v=0.5f*v*(1.f+tanhf(u)); }
    C[(size_t)(row0+ty+8*i)*ldc+col0+tx]=v; }
}
__global__ void k_gemm2(const u16* A,int lda,const float* B,int ldb,const u16* bias,const float* z,float* C,int ldc,int K){
  __shared__ float As[32][33], Bs[32][33];
  int tx=threadIdx.x&31, ty=threadIdx.x>>5; int row0=blockIdx.y*32,col0=blockIdx.x*32; float acc[4]={0,0,0,0};
  for(int k0=0;k0<K;k0+=32){
    for(int i=0;i<4;i++){ int r=ty+8*i; As[r][tx]=b2f(A[(size_t)(row0+r)*lda+k0+tx]); Bs[r][tx]=B[(size_t)(k0+r)*ldb+col0+tx]; }
    __syncthreads();
    for(int k=0;k<32;k++){ float b=Bs[k][tx]; for(int i=0;i<4;i++) acc[i]+=As[ty+8*i][k]*b; }
    __syncthreads();
  }
  for(int i=0;i<4;i++){ int r=row0+ty+8*i; C[(size_t)r*ldc+col0+tx]=(acc[i]+b2f(bias[r]))*z[(size_t)r*F+col0+tx]; }
}
__global__ void k_ln2(const float* z, float* zn){ int r=blockIdx.x; __shared__ float red[256]; float s=0; const float* p=z+(size_t)r*F+FH;
  for(int c=threadIdx.x;c<FH;c+=256) s+=p[c];
  red[threadIdx.x]=s; __syncthreads();
  for(int o=128;o;o>>=1){ if(threadIdx.x<o) red[threadIdx.x]+=red[threadIdx.x+o]; __syncthreads(); }
  float m=red[0]/FH; __syncthreads(); s=0;
  for(int c=threadIdx.x;c<FH;c+=256){ float d=p[c]-m; s+=d*d; }
  red[threadIdx.x]=s; __syncthreads();
  for(int o=128;o;o>>=1){ if(threadIdx.x<o) red[threadIdx.x]+=red[threadIdx.x+o]; __syncthreads(); }
  float rs=1.0f/sqrtf(red[0]/FH+1e-5f);
  for(int c=threadIdx.x;c<FH;c+=256) zn[(size_t)r*FH+c]=(p[c]-m)*rs;
}
__global__ void k_phi(const float* qkv,const u16* wphi,float* phi){ int idx=blockIdx.x*256+threadIdx.x; int which=idx/(S*H), s=(idx/H)%S, h=idx%H;
  const float* q=qkv+(size_t)s*3*D+which*D+h*DH; float* o=phi+(size_t)idx*36; float mx=-1e30f,mn=1e30f;
  for(int f=0;f<DH;f++){ float a=0; for(int d=0;d<DH;d++) a+=q[d]*b2f(wphi[h*DH*DH+d*DH+f]); o[f]=a; mx=fmaxf(mx,a); mn=fminf(mn,a);}
  float s1=0,s2=0; for(int f=0;f<DH;f++){ float a=o[f]; float e1=expf(a-mx), e2=expf(mn-a); o[f]=e1;o[18+f]=e2; s1+=e1;s2+=e2; }
  for(int f=0;f<DH;f++){ o[f]/=s1; o[18+f]/=s2; }
}
__global__ void k_kv(const float* qkv,const float* phi,float* kv){ int h=blockIdx.x; int i=blockIdx.y*256+threadIdx.x; if(i>=36*19) return; int f=i/19,d=i%19; float a=0;
  const float* pk=phi+(size_t)S*H*36; for(int s=0;s<S;s++){ float v= d<18? qkv[(size_t)s*3*D+2*D+h*DH+d]:1.f; a+=pk[((size_t)s*H+h)*36+f]*v; } kv[h*36*19+i]=a; }
__global__ void k_o(const float* phi,const float* kv,float* o){ int idx=blockIdx.x*256+threadIdx.x; int s=idx/D,c=idx%D,h=c/DH,d=c%DH; const float* pq=phi+((size_t)s*H+h)*36; float n=0,dd=0;
  for(int f=0;f<36;f++){ n+=pq[f]*kv[h*36*19+f*19+d]; dd+=pq[f]*kv[h*36*19+f*19+18]; } o[idx]=n/(dd+1e-6f); }
__global__ void k_fin(const u16* x,const float* a,const float* b,u16* out){ int i=blockIdx.x*256+threadIdx.x; out[i]=f2b(b2f(x[i])+a[i]+b[i]); }

extern "C" void kernel_launch(void* const* d_in, const int* in_sizes, int n_in, void* d_out, int out_size, void* d_ws, size_t ws_size, hipStream_t stream) {
  const u16* x=(const u16*)d_in[0]; const u16* wqkv=(const u16*)d_in[1]; const u16* wphi=(const u16*)d_in[2]; const u16* wo=(const u16*)d_in[3];
  const u16* win=(const u16*)d_in[5]; const u16* wsp=(const u16*)d_in[6]; const u16* bsp=(const u16*)d_in[7]; const u16* wout=(const u16*)d_in[8];
  float* w=(float*)d_ws; float* xn=w; w+=S*D; float* qkv=w; w+=S*3*D; float* z=w; w+=(size_t)S*F; float* zn=w; w+=(size_t)S*FH; float* hh=w; w+=(size_t)S*FH;
  float* phi=w; w+=(size_t)2*S*H*36; float* kv=w; w+=H*36*19+64; float* o=w; w+=S*D; float* ra=w; w+=S*D; float* rb=w; w+=S*D;
  k_ln<<<S,256,0,stream>>>(x,xn);
  k_gemm<0><<<dim3(3*D/32,S/32),256,0,stream>>>(xn,D,wqkv,3*D,qkv,3*D,S,3*D,D);
  k_gemm<1><<<dim3(F/32,S/32),256,0,stream>>>(xn,D,win,F,z,F,S,F,D);
  k_ln2<<<S,256,0,stream>>>(z,zn);
  k_gemm2<<<dim3(FH/32,S/32),256,0,stream>>>(wsp,S,zn,FH,bsp,z,hh,FH,S);
  k_phi<<<2*S*H/256,256,0,stream>>>(qkv,wphi,phi);
  k_kv<<<dim3(H,3),256,0,stream>>>(qkv,phi,kv);
  k_o<<<S*D/256,256,0,stream>>>(phi,kv,o);
  k_gemm<0><<<dim3(D/32,S/32),256,0,stream>>>(o,D,wo,D,ra,D,S,D,D);
  k_gemm<0><<<dim3(D/32,S/32),256,0,stream>>>(hh,FH,wout,D,rb,D,S,D,FH);
  k_fin<<<S*D/256,256,0,stream>>>(x,ra,rb,(u16*)d_out);
}
```

```cpp
#include <hip/hip_runtime.h>
#include <hip/hip_bf16.h>
typedef unsigned short u16;
__device__ __forceinline__ float b2f(u16 v){ return __uint_as_float(((unsigned)v)<<16); }
__device__ __forceinline__ u16 f2b(float f){ unsigned u=__float_as_uint(f); u += 0x7FFF + ((u>>16)&1); return (u16)(u>>16); }
constexpr int S=2048,D=576,H=32,DH=18,F=4608,FH=2304;

__global__ void k_ln(const u16* x, float* xn){
  int r=blockIdx.x; __shared__ float red[256]; float s=0;
  for(int c=threadIdx.x;c<D;c+=256) s+=b2f(x[r*D+c]);
  red[threadIdx.x]=s; __syncthreads();
  for(int o=128;o;o>>=1){ if(threadIdx.x<o) red[threadIdx.x]+=red[threadIdx.x+o]; __syncthreads(); }
  float m=red[0]/D; __syncthreads(); s=0;
  for(int c=threadIdx.x;c<D;c+=256){ float d=b2f(x[r*D+c])-m; s+=d*d; }
  red[threadIdx.x]=s; __syncthreads();
  for(int o=128;o;o>>=1){ if(threadIdx.x<o) red[threadIdx.x]+=red[threadIdx.x+o]; __syncthreads(); }
  float rs=1.0f/sqrtf(red[0]/D+1e-5f);
  for(int c=threadIdx.x;c<D;c+=256) xn[r*D+c]=(b2f(x[r*D+c])-m)*rs;
}
template<int MODE> __global__ void k_gemm(const float* A,int lda,const u16* B,int ldb,float* C,int ldc,int M,int N,int K){
  __shared__ float As[32][33], Bs[32][33];
  int tx=threadIdx.x&31, ty=threadIdx.x>>5; int row0=blockIdx.y*32,col0=blockIdx.x*32; float acc[4]={0,0,0,0};
  for(int k0=0;k0<K;k0+=32){
    for(int i=0;i<4;i++){ int r=ty+8*i; As[r][tx]=A[(size_t)(row0+r)*lda+k0+tx]; Bs[r][tx]=b2f(B[(size_t)(k0+r)*ldb+col0+tx]); }
    __syncthreads();
    for(int k=0;k<32;k++){ float b=Bs[k][tx]; for(int i=0;i<4;i++) acc[i]+=As[ty+8*i][k]*b; }
    __syncthreads();
  }
  for(int i=0;i<4;i++){ float v=acc[i]; if(MODE==1){ float u=0.7978845608028654f*(v+0.044715f*v*v*v); v=0.5f*v*(1.f+tanhf(u)); }
    C[(size_t)(row0+ty+8*i)*ldc+col0+tx]=v; }
}
__global__ void k_gemm2(const u16* A,int lda,const float* B,int ldb,const u16* bias,const float* z,float* C,int ldc,int K){
  __shared__ float As[32][33], Bs[32][33];
  int tx=threadIdx.x&31, ty=threadIdx.x>>5; int row0=blockIdx.y*32,col0=blockIdx.x*32; float acc[4]={0,0,0,0};
  for(int k0=0;k0<K;k0+=32){
    for(int i=0;i<4;i++){ int r=ty+8*i; As[r][tx]=b2f(A[(size_t)(row0+r)*lda+k0+tx]); Bs[r][tx]=B[(size_t)(k0+r)*ldb+col0+tx]; }
    __syncthreads();
    for(int k=0;k<32;k++){ float b=Bs[k][tx]; for(int i=0;i<4;i++) acc[i]+=As[ty+8*i][k]*b; }
    __syncthreads();
  }
  for(int i=0;i<4;i++){ int r=row0+ty+8*i; C[(size_t)r*ldc+col0+tx]=(acc[i]+b2f(bias[r]))*z[(size_t)r*F+col0+tx]; }
}
__global__ void k_ln2(const float* z, float* zn){ int r=blockIdx.x; __shared__ float red[256]; float s=0; const float* p=z+(size_t)r*F+FH;
  for(int c=threadIdx.x;c<FH;c+=256) s+=p[c];
  red[threadIdx.x]=s; __syncthreads();
  for(int o=128;o;o>>=1){ if(threadIdx.x<o) red[threadIdx.x]+=red[threadIdx.x+o]; __syncthreads(); }
  float m=red[0]/FH; __syncthreads(); s=0;
  for(int c=threadIdx.x;c<FH;c+=256){ float d=p[c]-m; s+=d*d; }
  red[threadIdx.x]=s; __syncthreads();
  for(int o=128;o;o>>=1){ if(threadIdx.x<o) red[threadIdx.x]+=red[threadIdx.x+o]; __syncthreads(); }
  float rs=1.0f/sqrtf(red[0]/FH+1e-5f);
  for(int c=threadIdx.x;c<FH;c+=256) zn[(size_t)r*FH+c]=(p[c]-m)*rs;
}
__global__ void k_phi(const float* qkv,const u16* wphi,float* phi){ int idx=blockIdx.x*256+threadIdx.x; int which=idx/(S*H), s=(idx/H)%S, h=idx%H;
  const float* q=qkv+(size_t)s*3*D+which*D+h*DH; float* o=phi+(size_t)idx*36; float mx=-1e30f,mn=1e30f;
  for(int f=0;f<DH;f++){ float a=0; for(int d=0;d<DH;d++) a+=q[d]*b2f(wphi[h*DH*DH+d*DH+f]); o[f]=a; mx=fmaxf(mx,a); mn=fminf(mn,a);}
  float s1=0,s2=0; for(int f=0;f<DH;f++){ float a=o[f]; float e1=expf(a-mx), e2=expf(mn-a); o[f]=e1;o[18+f]=e2; s1+=e1;s2+=e2; }
  for(int f=0;f<DH;f++){ o[f]/=s1; o[18+f]/=s2; }
}
__global__ void k_kv(const float* qkv,const float* phi,float* kv){ int h=blockIdx.x; int i=blockIdx.y*256+threadIdx.x; if(i>=36*19) return; int f=i/19,d=i%19; float a=0;
  const float* pk=phi+(size_t)S*H*36; for(int s=0;s<S;s++){ float v= d<18? qkv[(size_t)s*3*D+2*D+h*DH+d]:1.f; a+=pk[((size_t)s*H+h)*36+f]*v; } kv[h*36*19+i]=a; }
__global__ void k_o(const float* phi,const float* kv,float* o){ int idx=blockIdx.x*256+threadIdx.x; int s=idx/D,c=idx%D,h=c/DH,d=c%DH; const float* pq=phi+((size_t)s*H+h)*36; float n=0,dd=0;
  for(int f=0;f<36;f++){ n+=pq[f]*kv[h*36*19+f*19+d]; dd+=pq[f]*kv[h*36*19+f*19+18]; } o[idx]=n/(dd+1e-6f); }
__global__ void k_fin(const u16* x,const float* a,const float* b,u16* out){ int i=blockIdx.x*256+threadIdx.x; out[i]=f2b(b2f(x[i])+a[i]+b[i]); }


typedef unsigned v4u __attribute__((ext_vector_type(4)));
template<int MODE> __global__ __launch_bounds__(256) void k_probe(const char* src,char* dst){
  int id=blockIdx.x*4+__builtin_amdgcn_readfirstlane(threadIdx.x>>6); int l=threadIdx.x&63;
  __amdgpu_buffer_rsrc_t ri=__builtin_amdgcn_make_buffer_rsrc((void*)src,0,8<<20,0x00020000);
  __amdgpu_buffer_rsrc_t rw=__builtin_amdgcn_make_buffer_rsrc((void*)dst,0,8<<20,0x00020000);
  unsigned long long t0=__builtin_amdgcn_s_memrealtime();
  v4u d[8];
  if(MODE==1){
    #pragma unroll
    for(int i=0;i<8;i++) d[i]=__builtin_amdgcn_raw_buffer_load_b128(ri,l*16,(id*8+i)*1024,0);
    asm volatile("s_waitcnt vmcnt(0)":::"memory");
    #pragma unroll
    for(int i=0;i<8;i++) asm volatile(""::"v"(d[i]));
  }
  if(MODE==2){
    v4u z={1,2,3,(unsigned)l};
    #pragma unroll
    for(int i=0;i<8;i++) __builtin_amdgcn_raw_buffer_store_b128(z,rw,l*16,(id*8+i)*1024,16);
    asm volatile("s_waitcnt vmcnt(0)":::"memory");
  }
  unsigned long long t1=__builtin_amdgcn_s_memrealtime();
  unsigned long long lim=(t1-t0)*100; if(lim>200000) lim=200000;
  while(__builtin_amdgcn_s_memrealtime()-t1<lim) __builtin_amdgcn_s_sleep(8);
}
extern "C" void kernel_launch(void* const* d_in, const int* in_sizes, int n_in, void* d_out, int out_size, void* d_ws, size_t ws_size, hipStream_t stream) {
  const u16* x=(const u16*)d_in[0]; const u16* wqkv=(const u16*)d_in[1]; const u16* wphi=(const u16*)d_in[2]; const u16* wo=(const u16*)d_in[3];
  const u16* win=(const u16*)d_in[5]; const u16* wsp=(const u16*)d_in[6]; const u16* bsp=(const u16*)d_in[7]; const u16* wout=(const u16*)d_in[8];
  float* w=(float*)d_ws; float* xn=w; w+=S*D; float* qkv=w; w+=S*3*D; float* z=w; w+=(size_t)S*F; float* zn=w; w+=(size_t)S*FH; float* hh=w; w+=(size_t)S*FH;
  float* phi=w; w+=(size_t)2*S*H*36; float* kv=w; w+=H*36*19+64; float* o=w; w+=S*D; float* ra=w; w+=S*D; float* rb=w; w+=S*D;
  k_probe<0><<<256,256,0,stream>>>((const char*)wsp,(char*)d_ws);
  k_ln<<<S,256,0,stream>>>(x,xn);
  k_gemm<0><<<dim3(3*D/32,S/32),256,0,stream>>>(xn,D,wqkv,3*D,qkv,3*D,S,3*D,D);
  k_gemm<1><<<dim3(F/32,S/32),256,0,stream>>>(xn,D,win,F,z,F,S,F,D);
  k_ln2<<<S,256,0,stream>>>(z,zn);
  k_gemm2<<<dim3(FH/32,S/32),256,0,stream>>>(wsp,S,zn,FH,bsp,z,hh,FH,S);
  k_phi<<<2*S*H/256,256,0,stream>>>(qkv,wphi,phi);
  k_kv<<<dim3(H,3),256,0,stream>>>(qkv,phi,kv);
  k_o<<<S*D/256,256,0,stream>>>(phi,kv,o);
  k_gemm<0><<<dim3(D/32,S/32),256,0,stream>>>(o,D,wo,D,ra,D,S,D,D);
  k_gemm<0><<<dim3(D/32,S/32),256,0,stream>>>(hh,FH,wout,D,rb,D,S,D,FH);
  k_fin<<<S*D/256,256,0,stream>>>(x,ra,rb,(u16*)d_out);
}
```

```cpp
#include <hip/hip_runtime.h>
#include <hip/hip_bf16.h>

struct Desc { const void* p; unsigned bytes; unsigned fmt; };
struct MegaArgs { Desc d[10]; };

__global__ __launch_bounds__(256, 1) void parallel_sum__linear_kernel_hedgehog__mixer_gmlp__prompt_kernel(MegaArgs args) {
  unsigned tid = threadIdx.x;
  asm volatile(
"s_mov_b32 s58, %[bid]\n"
"s_load_dwordx16 s[8:23], %[ka], 0\n"
"s_load_dwordx16 s[24:39], %[ka], 64\n"
"s_load_dwordx8 s[40:47], %[ka], 128\n"
"s_getpc_b64 s[68:69]\n"
".Lmk_PCHERE:\n"
"s_lshr_b32 s70, s58, 3\n"
"s_lshl_b32 s70, s70, 12\n"
"v_lshrrev_b32 v46, 3, v0\n"
"v_lshlrev_b32 v46, 7, v46\n"
"v_add_u32 v46, s70, v46\n"
"v_min_u32 v46, .Lmk_CODE_END-.Lmk_PCHERE-4, v46\n"
"global_load_dword a255, v46, s[68:69]\n"
"s_mov_b64 s[0:1], s[68:69]\n"
"s_mov_b32 s2, .Lmk_CODE_END-.Lmk_PCHERE\n"
"s_mov_b32 s3, 0x20000\n"
"v_and_b32 v1, 63, v0\n"
"v_lshrrev_b32 v2, 6, v0\n"
"s_nop 0\n"
"v_readfirstlane_b32 s54, v2\n"
"s_and_b32 s64, s58, 7\n"
"s_lshr_b32 s65, s58, 3\n"
"s_and_b32 s66, s64, 1\n"
"s_lshl_b32 s66, s66, 3\n"
"s_and_b32 s67, s65, 7\n"
"s_add_u32 s52, s66, s67\n"
"s_lshr_b32 s56, s64, 1\n"
"s_lshl_b32 s66, s56, 2\n"
"s_lshr_b32 s57, s65, 3\n"
"s_add_u32 s53, s66, s57\n"
"s_lshl_b32 s63, s52, 2\n"
"s_add_u32 s63, s63, s54\n"
"v_lshrrev_b32 v33, 3, v1\n"
"v_and_b32 v34, 7, v1\n"
"v_and_b32 v35, 4, v33\n"
"v_add_u32 v35, v35, v33\n"
"v_lshrrev_b32 v36, 1, v33\n"
"v_xor_b32 v36, v36, v34\n"
"v_lshlrev_b32 v36, 4, v36\n"
"v_mul_u32_u24 v37, 1152, v35\n"
"v_add_u32 v19, v36, v37\n"
"v_xor_b32 v20, 64, v19\n"
"v_add_u32 v20, 17408, v20\n"
"s_mul_i32 s64, s63, 36864\n"
"s_add_u32 s65, s64, 4608\n"
"s_mul_i32 s66, s54, 18432\n"
"s_lshl_b32 s67, s57, 12\n"
"s_lshr_b32 s97, s67, 4\n"
"s_add_u32 s97, s97, s64\n"
"s_add_u32 m0, s66, s67\n"
"s_add_u32 s67, s67, 2048\n"
"s_waitcnt lgkmcnt(0)\n"
"buffer_load_dwordx4 v19, s[8:11], s97 offen offset:0 lds\n"
"buffer_load_dwordx4 v20, s[8:11], s97 offen offset:1024 lds\n"
"s_cmp_eq_u32 s67, 18432\n"
"s_cselect_b32 s67, 0, s67\n"
"s_lshr_b32 s97, s67, 4\n"
"s_add_u32 s97, s97, s64\n"
"s_add_u32 m0, s66, s67\n"
"s_add_u32 s67, s67, 2048\n"
"buffer_load_dwordx4 v19, s[8:11], s97 offen offset:0 lds\n"
"buffer_load_dwordx4 v20, s[8:11], s97 offen offset:1024 lds\n"
"s_cmp_eq_u32 s67, 18432\n"
"s_cselect_b32 s67, 0, s67\n"
"s_lshr_b32 s97, s67, 4\n"
"s_add_u32 s97, s97, s64\n"
"s_add_u32 m0, s66, s67\n"
"s_add_u32 s67, s67, 2048\n"
"buffer_load_dwordx4 v19, s[8:11], s97 offen offset:0 lds\n"
"buffer_load_dwordx4 v20, s[8:11], s97 offen offset:1024 lds\n"
"s_cmp_eq_u32 s67, 18432\n"
"s_cselect_b32 s67, 0, s67\n"
"s_lshr_b32 s97, s67, 4\n"
"s_add_u32 s97, s97, s64\n"
"s_add_u32 m0, s66, s67\n"
"s_add_u32 s67, s67, 2048\n"
"buffer_load_dwordx4 v19, s[8:11], s97 offen offset:0 lds\n"
"buffer_load_dwordx4 v20, s[8:11], s97 offen offset:1024 lds\n"
"s_cmp_eq_u32 s67, 18432\n"
"s_cselect_b32 s67, 0, s67\n"
"s_lshr_b32 s97, s67, 4\n"
"s_add_u32 s97, s97, s64\n"
"s_add_u32 m0, s66, s67\n"
"s_add_u32 s67, s67, 2048\n"
"buffer_load_dwordx4 v19, s[8:11], s97 offen offset:0 lds\n"
"buffer_load_dwordx4 v20, s[8:11], s97 offen offset:1024 lds\n"
"s_cmp_eq_u32 s67, 18432\n"
"s_cselect_b32 s67, 0, s67\n"
"s_load_dword s55, s[16:17], 0\n"
"v_lshlrev_b32 v44, 4, v34\n"
"s_lshr_b32 s97, s67, 4\n"
"s_add_u32 s97, s97, s64\n"
"v_lshl_add_u32 v23, v35, 12, v44\n"
"s_add_u32 m0, s66, s67\n"
"s_add_u32 s67, s67, 2048\n"
"v_and_b32 v44, 3, v34\n"
"v_lshlrev_b32 v44, 8, v44\n"
"v_lshl_add_u32 v44, v33, 4, v44\n"
"v_lshrrev_b32 v45, 2, v34\n"
"v_lshl_add_u32 v24, v45, 11, v44\n"
"v_and_b32 v33, 15, v1\n"
"v_lshrrev_b32 v34, 4, v1\n"
"v_lshrrev_b32 v44, 1, v33\n"
"v_xor_b32 v44, v44, v34\n"
"v_lshlrev_b32 v44, 4, v44\n"
"v_lshl_add_u32 v44, v33, 7, v44\n"
"v_xor_b32 v45, 64, v44\n"
"v_add_u32 v21, s66, v44\n"
"v_add_u32 v22, s66, v45\n"
"v_lshlrev_b32 v2, 4, v1\n"
"v_add_u32 v3, 4096, v2\n"
"v_add_u32 v4, 8192, v2\n"
"v_add_u32 v5, 0x10000, v2\n"
"v_lshrrev_b32 v36, 2, v1\n"
"v_mul_u32_u24 v36, 288, v36\n"
"v_and_b32 v37, 3, v1\n"
"v_lshl_add_u32 v6, v37, 3, v36\n"
"v_add_u32 v7, 0x10000, v6\n"
"v_add_u32 v44, 0, v1\n"
"v_mul_u32_u24 v45, 911, v44\n"
"v_lshrrev_b32 v45, 14, v45\n"
"v_and_b32 v47, 12, v45\n"
"v_add_u32 v47, v47, v45\n"
"v_lshrrev_b32 v48, 4, v45\n"
"v_mul_u32_u24 v48, 12, v48\n"
"v_sub_u32 v47, v47, v48\n"
"v_mul_u32_u24 v47, 9216, v47\n"
"v_mul_u32_u24 v45, 288, v45\n"
"v_sub_u32 v47, v47, v45\n"
"v_add_u32 v8, v47, v2\n"
"v_add_u32 v44, 64, v1\n"
"v_mul_u32_u24 v45, 911, v44\n"
"v_lshrrev_b32 v45, 14, v45\n"
"v_and_b32 v47, 12, v45\n"
"v_add_u32 v47, v47, v45\n"
"v_lshrrev_b32 v48, 4, v45\n"
"v_mul_u32_u24 v48, 12, v48\n"
"v_sub_u32 v47, v47, v48\n"
"v_mul_u32_u24 v47, 9216, v47\n"
"v_mul_u32_u24 v45, 288, v45\n"
"v_sub_u32 v47, v47, v45\n"
"v_add_u32 v9, v47, v2\n"
"v_add_u32 v44, 128, v1\n"
"v_mul_u32_u24 v45, 911, v44\n"
"v_lshrrev_b32 v45, 14, v45\n"
"v_and_b32 v47, 12, v45\n"
"v_add_u32 v47, v47, v45\n"
"v_lshrrev_b32 v48, 4, v45\n"
"v_mul_u32_u24 v48, 12, v48\n"
"v_sub_u32 v47, v47, v48\n"
"v_mul_u32_u24 v47, 9216, v47\n"
"v_mul_u32_u24 v45, 288, v45\n"
"v_sub_u32 v47, v47, v45\n"
"v_add_u32 v10, v47, v2\n"
"v_add_u32 v44, 192, v1\n"
"v_mul_u32_u24 v45, 911, v44\n"
"v_lshrrev_b32 v45, 14, v45\n"
"v_and_b32 v47, 12, v45\n"
"v_add_u32 v47, v47, v45\n"
"v_lshrrev_b32 v48, 4, v45\n"
"v_mul_u32_u24 v48, 12, v48\n"
"v_sub_u32 v47, v47, v48\n"
"v_mul_u32_u24 v47, 9216, v47\n"
"v_mul_u32_u24 v45, 288, v45\n"
"v_sub_u32 v47, v47, v45\n"
"v_add_u32 v11, v47, v2\n"
"v_add_u32 v44, 256, v1\n"
"v_mul_u32_u24 v45, 911, v44\n"
"v_lshrrev_b32 v45, 14, v45\n"
"v_and_b32 v47, 12, v45\n"
"v_add_u32 v47, v47, v45\n"
"v_lshrrev_b32 v48, 4, v45\n"
"v_mul_u32_u24 v48, 12, v48\n"
"v_sub_u32 v47, v47, v48\n"
"v_mul_u32_u24 v47, 9216, v47\n"
"v_mul_u32_u24 v45, 288, v45\n"
"v_sub_u32 v47, v47, v45\n"
"v_add_u32 v12, v47, v3\n"
"v_add_u32 v44, 320, v1\n"
"v_mul_u32_u24 v45, 911, v44\n"
"v_lshrrev_b32 v45, 14, v45\n"
"v_and_b32 v47, 12, v45\n"
"v_add_u32 v47, v47, v45\n"
"v_lshrrev_b32 v48, 4, v45\n"
"v_mul_u32_u24 v48, 12, v48\n"
"v_sub_u32 v47, v47, v48\n"
"v_mul_u32_u24 v47, 9216, v47\n"
"v_mul_u32_u24 v45, 288, v45\n"
"v_sub_u32 v47, v47, v45\n"
"v_add_u32 v13, v47, v3\n"
"v_add_u32 v44, 384, v1\n"
"v_mul_u32_u24 v45, 911, v44\n"
"v_lshrrev_b32 v45, 14, v45\n"
"v_and_b32 v47, 12, v45\n"
"v_add_u32 v47, v47, v45\n"
"v_lshrrev_b32 v48, 4, v45\n"
"v_mul_u32_u24 v48, 12, v48\n"
"v_sub_u32 v47, v47, v48\n"
"v_mul_u32_u24 v47, 9216, v47\n"
"v_mul_u32_u24 v45, 288, v45\n"
"v_sub_u32 v47, v47, v45\n"
"v_add_u32 v14, v47, v3\n"
"v_add_u32 v44, 448, v1\n"
"v_mul_u32_u24 v45, 911, v44\n"
"v_lshrrev_b32 v45, 14, v45\n"
"v_and_b32 v47, 12, v45\n"
"v_add_u32 v47, v47, v45\n"
"v_lshrrev_b32 v48, 4, v45\n"
"v_mul_u32_u24 v48, 12, v48\n"
"v_sub_u32 v47, v47, v48\n"
"v_mul_u32_u24 v47, 9216, v47\n"
"v_mul_u32_u24 v45, 288, v45\n"
"v_sub_u32 v47, v47, v45\n"
"v_add_u32 v15, v47, v3\n"
"v_add_u32 v44, 512, v1\n"
"v_mul_u32_u24 v45, 911, v44\n"
"v_lshrrev_b32 v45, 14, v45\n"
"v_and_b32 v47, 12, v45\n"
"v_add_u32 v47, v47, v45\n"
"v_lshrrev_b32 v48, 4, v45\n"
"v_mul_u32_u24 v48, 12, v48\n"
"v_sub_u32 v47, v47, v48\n"
"buffer_load_dwordx4 v19, s[8:11], s97 offen offset:0 lds\n"
"v_mul_u32_u24 v47, 9216, v47\n"
"v_mul_u32_u24 v45, 288, v45\n"
"v_sub_u32 v47, v47, v45\n"
"v_add_u32 v16, v47, v4\n"
"s_lshr_b32 s70, s58, 3\n"
"s_lshl_b32 s70, s70, 2\n"
"s_add_u32 s70, s70, s54\n"
"s_lshl_b32 s70, s70, 7\n"
"s_add_u32 s70, s70, 2048\n"
"s_sub_u32 s2, s2, s70\n"
"s_add_u32 s0, s0, s70\n"
"s_addc_u32 s1, s1, 0\n"
"s_mul_i32 s60, s54, 9216\n"
"s_mul_i32 s61, s54, 294912\n"
"s_mul_i32 s70, s53, 288\n"
"s_add_u32 s61, s61, s70\n"
"s_add_u32 s61, s61, 4608\n"
"s_mov_b32 s76, 0x3f803f80\n"
"s_mov_b32 s77, 0xffff0000\n"
"s_mov_b32 s78, 0x05040100\n"
"s_mov_b32 s79, 0x07060302\n"
"s_mov_b32 s80, 0xbdd2d3e8\n"
"s_mov_b32 s81, 0xc0135761\n"
"buffer_load_dwordx4 v20, s[8:11], s97 offen offset:1024 lds\n"
"s_mov_b32 s59, 16384\n"
"s_cmp_eq_u32 s67, 18432\n"
"s_cselect_b32 s67, 0, s67\n"
"v_lshlrev_b32 v44, 4, v0\n"
"s_lshr_b32 s97, s67, 4\n"
"s_add_u32 s97, s97, s64\n"
"v_add_u32 v44, 155648, v44\n"
"s_add_u32 m0, s66, s67\n"
"s_add_u32 s67, s67, 2048\n"
"v_mov_b32 v48, 0\n"
"v_mov_b32 v49, 0\n"
"v_mov_b32 v50, 0\n"
"v_mov_b32 v51, 0\n"
"ds_write_b128 v44, v[48:51]\n"
"ds_write_b128 v44, v[48:51] offset:4096\n"
"s_mul_i32 s70, s58, 81\n"
"s_mul_i32 s71, s54, 21\n"
"v_min_u32 v44, 20, v1\n"
"v_add_u32 v44, s71, v44\n"
"v_min_u32 v44, 80, v44\n"
"v_add_u32 v44, s70, v44\n"
"v_mul_u32_u24 v45, 58255, v44\n"
"buffer_load_dwordx4 v19, s[8:11], s97 offen offset:0 lds\n"
"v_lshrrev_b32 v45, 21, v45\n"
"v_mul_u32_u24 v45, 4608, v45\n"
"v_lshl_add_u32 v38, v44, 7, v45\n"
"s_mov_b32 s98, 4608\n"
"s_mul_i32 s70, s63, 86\n"
"s_lshr_b32 s70, s70, 8\n"
"s_mul_i32 s71, s70, 3\n"
"s_sub_u32 s71, s63, s71\n"
"s_lshr_b32 s72, s71, 1\n"
"s_sub_u32 s72, 1, s72\n"
"s_xor_b32 s72, s72, s71\n"
"s_mul_i32 s72, s72, 1152\n"
"s_mul_i32 s73, s70, 110592\n"
"s_add_u32 s72, s72, s73\n"
"s_mul_i32 s73, s53, 72\n"
"s_add_u32 s72, s72, s73\n"
"v_mul_u32_u24 v44, 27648, v34\n"
"v_lshrrev_b32 v45, 3, v33\n"
"v_and_b32 v46, 7, v33\n"
"v_mul_u32_u24 v47, 36, v45\n"
"v_lshl_add_u32 v47, v46, 2, v47\n"
"v_add3_u32 v48, v44, v47, s72\n"
"buffer_load_dwordx4 v20, s[8:11], s97 offen offset:1024 lds\n"
"s_mul_i32 s74, s53, 129024\n"
"s_cmp_eq_u32 s67, 18432\n"
"s_cselect_b32 s67, 0, s67\n"
"s_add_u32 s74, s74, 3145728\n"
"s_lshr_b32 s97, s67, 4\n"
"s_add_u32 s97, s97, s64\n"
"s_mul_i32 s70, s70, 7168\n"
"s_add_u32 m0, s66, s67\n"
"s_add_u32 s67, s67, 2048\n"
"s_lshl_b32 s71, s71, 11\n"
"s_add_u32 s70, s70, s71\n"
"s_add_u32 s70, s70, s74\n"
"v_add_u32 v49, s70, v2\n"
"v_lshrrev_b32 v50, 1, v1\n"
"v_and_b32 v51, 1, v1\n"
"v_mul_u32_u24 v52, 3456, v50\n"
"v_mul_u32_u24 v53, 36, v51\n"
"v_add3_u32 v52, v52, v53, s72\n"
"v_add_u32 v52, 32, v52\n"
"v_mov_b32 v54, 1073741824\n"
"s_cmp_lt_u32 s63, 54\n"
"s_cselect_b64 s[72:73], -1, 0\n"
"buffer_load_dwordx4 v19, s[8:11], s97 offen offset:0 lds\n"
"v_cndmask_b32 v27, v54, v48, s[72:73]\n"
"v_cndmask_b32 v28, v54, v49, s[72:73]\n"
"v_cndmask_b32 v25, v54, v52, s[72:73]\n"
"s_lshr_b32 s71, s71, 11\n"
"s_mul_i32 s71, s71, 1792\n"
"s_sub_u32 s88, 6144, s71\n"
"s_lshl_b32 s70, s54, 10\n"
"s_add_u32 s70, s70, 147456\n"
"v_and_b32 v52, 7, v50\n"
"v_lshlrev_b32 v52, 1, v52\n"
"v_lshl_add_u32 v52, v51, 5, v52\n"
"v_lshl_add_u32 v52, v34, 6, v52\n"
"v_add_u32 v26, s70, v52\n"
"s_waitcnt lgkmcnt(0)\n"
"s_add_u32 s55, s55, 1\n"
"v_mov_b32 v17, s55\n"
"buffer_load_dwordx4 v20, s[8:11], s97 offen offset:1024 lds\n"
"s_cmp_eq_u32 s67, 18432\n"
"s_cselect_b32 s67, 0, s67\n"
"s_lshr_b32 s97, s67, 4\n"
"s_add_u32 s97, s97, s64\n"
"s_add_u32 m0, s66, s67\n"
"s_add_u32 s67, s67, 2048\n"
"buffer_load_dwordx4 v19, s[8:11], s97 offen offset:0 lds\n"
"buffer_load_dwordx4 v20, s[8:11], s97 offen offset:1024 lds\n"
"s_cmp_eq_u32 s67, 18432\n"
"s_cselect_b32 s67, 0, s67\n"
"v_mov_b32 v40, 0\n"
"s_add_u32 s66, s66, 73728\n"
"s_lshr_b32 s97, s67, 4\n"
"v_mov_b32 v41, 0\n"
"s_add_u32 s97, s97, s65\n"
"s_add_u32 m0, s66, s67\n"
"v_mov_b32 v42, 0\n"
"s_add_u32 s67, s67, 2048\n"
"v_mov_b32 v43, 0\n"
"s_waitcnt vmcnt(0)\n"
"ds_read_b128 v[112:115], v21 offset:0\n"
"ds_read_b128 v[116:119], v22 offset:0\n"
"ds_read_b128 v[120:123], v21 offset:2048\n"
"ds_read_b128 v[124:127], v22 offset:2048\n"
"ds_read_b128 v[128:131], v21 offset:4096\n"
"ds_read_b128 v[132:135], v22 offset:4096\n"
"ds_read_b128 v[136:139], v21 offset:6144\n"
"ds_read_b128 v[140:143], v22 offset:6144\n"
"ds_read_b128 v[144:147], v21 offset:8192\n"
"ds_read_b128 v[148:151], v22 offset:8192\n"
"ds_read_b128 v[152:155], v21 offset:10240\n"
"ds_read_b128 v[156:159], v22 offset:10240\n"
"s_waitcnt lgkmcnt(11)\n"
"v_dot2c_f32_bf16 v40, s76, v112\n"
"v_dot2c_f32_bf16 v42, v112, v112\n"
"v_dot2c_f32_bf16 v41, s76, v113\n"
"v_dot2c_f32_bf16 v43, v113, v113\n"
"v_dot2c_f32_bf16 v40, s76, v114\n"
"v_dot2c_f32_bf16 v42, v114, v114\n"
"v_dot2c_f32_bf16 v41, s76, v115\n"
"v_dot2c_f32_bf16 v43, v115, v115\n"
"s_waitcnt lgkmcnt(10)\n"
"v_dot2c_f32_bf16 v40, s76, v116\n"
"v_dot2c_f32_bf16 v42, v116, v116\n"
"v_dot2c_f32_bf16 v41, s76, v117\n"
"v_dot2c_f32_bf16 v43, v117, v117\n"
"v_dot2c_f32_bf16 v40, s76, v118\n"
"v_dot2c_f32_bf16 v42, v118, v118\n"
"v_dot2c_f32_bf16 v41, s76, v119\n"
"v_dot2c_f32_bf16 v43, v119, v119\n"
"s_waitcnt lgkmcnt(9)\n"
"v_dot2c_f32_bf16 v40, s76, v120\n"
"v_dot2c_f32_bf16 v42, v120, v120\n"
"v_dot2c_f32_bf16 v41, s76, v121\n"
"v_dot2c_f32_bf16 v43, v121, v121\n"
"v_dot2c_f32_bf16 v40, s76, v122\n"
"v_dot2c_f32_bf16 v42, v122, v122\n"
"v_dot2c_f32_bf16 v41, s76, v123\n"
"v_dot2c_f32_bf16 v43, v123, v123\n"
"s_waitcnt lgkmcnt(8)\n"
"v_dot2c_f32_bf16 v40, s76, v124\n"
"v_dot2c_f32_bf16 v42, v124, v124\n"
"v_dot2c_f32_bf16 v41, s76, v125\n"
"v_dot2c_f32_bf16 v43, v125, v125\n"
"v_dot2c_f32_bf16 v40, s76, v126\n"
"v_dot2c_f32_bf16 v42, v126, v126\n"
"v_dot2c_f32_bf16 v41, s76, v127\n"
"v_dot2c_f32_bf16 v43, v127, v127\n"
"s_waitcnt lgkmcnt(7)\n"
"v_dot2c_f32_bf16 v40, s76, v128\n"
"v_dot2c_f32_bf16 v42, v128, v128\n"
"v_dot2c_f32_bf16 v41, s76, v129\n"
"v_dot2c_f32_bf16 v43, v129, v129\n"
"v_dot2c_f32_bf16 v40, s76, v130\n"
"buffer_load_dwordx4 v19, s[8:11], s97 offen offset:0 lds\n"
"v_dot2c_f32_bf16 v42, v130, v130\n"
"v_dot2c_f32_bf16 v41, s76, v131\n"
"v_dot2c_f32_bf16 v43, v131, v131\n"
"s_waitcnt lgkmcnt(6)\n"
"v_dot2c_f32_bf16 v40, s76, v132\n"
"v_dot2c_f32_bf16 v42, v132, v132\n"
"v_dot2c_f32_bf16 v41, s76, v133\n"
"v_dot2c_f32_bf16 v43, v133, v133\n"
"v_dot2c_f32_bf16 v40, s76, v134\n"
"v_dot2c_f32_bf16 v42, v134, v134\n"
"v_dot2c_f32_bf16 v41, s76, v135\n"
"v_dot2c_f32_bf16 v43, v135, v135\n"
"ds_read_b128 v[160:163], v21 offset:12288\n"
"ds_read_b128 v[164:167], v22 offset:12288\n"
"ds_read_b128 v[168:171], v21 offset:14336\n"
"ds_read_b128 v[172:175], v22 offset:14336\n"
"ds_read_b128 v[176:179], v21 offset:16384\n"
"ds_read_b128 v[180:183], v22 offset:16384\n"
"s_waitcnt lgkmcnt(11)\n"
"v_dot2c_f32_bf16 v40, s76, v136\n"
"v_dot2c_f32_bf16 v42, v136, v136\n"
"v_dot2c_f32_bf16 v41, s76, v137\n"
"buffer_load_dwordx4 v20, s[8:11], s97 offen offset:1024 lds\n"
"v_dot2c_f32_bf16 v43, v137, v137\n"
"s_cmp_eq_u32 s67, 18432\n"
"s_cselect_b32 s67, 0, s67\n"
"v_dot2c_f32_bf16 v40, s76, v138\n"
"s_lshr_b32 s97, s67, 4\n"
"s_add_u32 s97, s97, s65\n"
"v_dot2c_f32_bf16 v42, v138, v138\n"
"s_add_u32 m0, s66, s67\n"
"s_add_u32 s67, s67, 2048\n"
"v_dot2c_f32_bf16 v41, s76, v139\n"
"v_dot2c_f32_bf16 v43, v139, v139\n"
"s_waitcnt lgkmcnt(10)\n"
"v_dot2c_f32_bf16 v40, s76, v140\n"
"v_dot2c_f32_bf16 v42, v140, v140\n"
"v_dot2c_f32_bf16 v41, s76, v141\n"
"v_dot2c_f32_bf16 v43, v141, v141\n"
"v_dot2c_f32_bf16 v40, s76, v142\n"
"v_dot2c_f32_bf16 v42, v142, v142\n"
"v_dot2c_f32_bf16 v41, s76, v143\n"
"v_dot2c_f32_bf16 v43, v143, v143\n"
"s_waitcnt lgkmcnt(9)\n"
"v_dot2c_f32_bf16 v40, s76, v144\n"
"buffer_load_dwordx4 v19, s[8:11], s97 offen offset:0 lds\n"
"v_dot2c_f32_bf16 v42, v144, v144\n"
"v_dot2c_f32_bf16 v41, s76, v145\n"
"v_dot2c_f32_bf16 v43, v145, v145\n"
"v_dot2c_f32_bf16 v40, s76, v146\n"
"v_dot2c_f32_bf16 v42, v146, v146\n"
"v_dot2c_f32_bf16 v41, s76, v147\n"
"v_dot2c_f32_bf16 v43, v147, v147\n"
"s_waitcnt lgkmcnt(8)\n"
"v_dot2c_f32_bf16 v40, s76, v148\n"
"v_dot2c_f32_bf16 v42, v148, v148\n"
"v_dot2c_f32_bf16 v41, s76, v149\n"
"v_dot2c_f32_bf16 v43, v149, v149\n"
"v_dot2c_f32_bf16 v40, s76, v150\n"
"v_dot2c_f32_bf16 v42, v150, v150\n"
"v_dot2c_f32_bf16 v41, s76, v151\n"
"v_dot2c_f32_bf16 v43, v151, v151\n"
"s_waitcnt lgkmcnt(7)\n"
"v_dot2c_f32_bf16 v40, s76, v152\n"
"v_dot2c_f32_bf16 v42, v152, v152\n"
"v_dot2c_f32_bf16 v41, s76, v153\n"
"v_dot2c_f32_bf16 v43, v153, v153\n"
"buffer_load_dwordx4 v20, s[8:11], s97 offen offset:1024 lds\n"
"v_dot2c_f32_bf16 v40, s76, v154\n"
"s_cmp_eq_u32 s67, 18432\n"
"s_cselect_b32 s67, 0, s67\n"
"v_dot2c_f32_bf16 v42, v154, v154\n"
"s_lshr_b32 s97, s67, 4\n"
"s_add_u32 s97, s97, s65\n"
"v_dot2c_f32_bf16 v41, s76, v155\n"
"s_add_u32 m0, s66, s67\n"
"s_add_u32 s67, s67, 2048\n"
"v_dot2c_f32_bf16 v43, v155, v155\n"
"s_waitcnt lgkmcnt(6)\n"
"v_dot2c_f32_bf16 v40, s76, v156\n"
"v_dot2c_f32_bf16 v42, v156, v156\n"
"v_dot2c_f32_bf16 v41, s76, v157\n"
"v_dot2c_f32_bf16 v43, v157, v157\n"
"v_dot2c_f32_bf16 v40, s76, v158\n"
"v_dot2c_f32_bf16 v42, v158, v158\n"
"v_dot2c_f32_bf16 v41, s76, v159\n"
"v_dot2c_f32_bf16 v43, v159, v159\n"
"v_add_u32 v21, 73728, v21\n"
"v_add_u32 v22, 73728, v22\n"
"s_waitcnt lgkmcnt(0)\n"
"buffer_load_dwordx4 v19, s[8:11], s97 offen offset:0 lds\n"
"s_barrier\n"
"v_dot2c_f32_bf16 v40, s76, v160\n"
"v_dot2c_f32_bf16 v42, v160, v160\n"
"v_dot2c_f32_bf16 v41, s76, v161\n"
"v_dot2c_f32_bf16 v43, v161, v161\n"
"v_dot2c_f32_bf16 v40, s76, v162\n"
"v_dot2c_f32_bf16 v42, v162, v162\n"
"v_dot2c_f32_bf16 v41, s76, v163\n"
"v_dot2c_f32_bf16 v43, v163, v163\n"
"v_dot2c_f32_bf16 v40, s76, v164\n"
"v_dot2c_f32_bf16 v42, v164, v164\n"
"v_dot2c_f32_bf16 v41, s76, v165\n"
"v_dot2c_f32_bf16 v43, v165, v165\n"
"v_dot2c_f32_bf16 v40, s76, v166\n"
"v_dot2c_f32_bf16 v42, v166, v166\n"
"v_dot2c_f32_bf16 v41, s76, v167\n"
"v_dot2c_f32_bf16 v43, v167, v167\n"
"v_dot2c_f32_bf16 v40, s76, v168\n"
"v_dot2c_f32_bf16 v42, v168, v168\n"
"buffer_load_dwordx4 v20, s[8:11], s97 offen offset:1024 lds\n"
"v_dot2c_f32_bf16 v41, s76, v169\n"
"s_cmp_eq_u32 s67, 18432\n"
"s_cselect_b32 s67, 0, s67\n"
"v_dot2c_f32_bf16 v43, v169, v169\n"
"s_lshr_b32 s97, s67, 4\n"
"s_add_u32 s97, s97, s65\n"
"v_dot2c_f32_bf16 v40, s76, v170\n"
"s_add_u32 m0, s66, s67\n"
"s_add_u32 s67, s67, 2048\n"
"v_dot2c_f32_bf16 v42, v170, v170\n"
"v_dot2c_f32_bf16 v41, s76, v171\n"
"v_dot2c_f32_bf16 v43, v171, v171\n"
"v_dot2c_f32_bf16 v40, s76, v172\n"
"v_dot2c_f32_bf16 v42, v172, v172\n"
"v_dot2c_f32_bf16 v41, s76, v173\n"
"v_dot2c_f32_bf16 v43, v173, v173\n"
"v_dot2c_f32_bf16 v40, s76, v174\n"
"v_dot2c_f32_bf16 v42, v174, v174\n"
"v_dot2c_f32_bf16 v41, s76, v175\n"
"v_dot2c_f32_bf16 v43, v175, v175\n"
"v_dot2c_f32_bf16 v40, s76, v176\n"
"buffer_load_dwordx4 v19, s[8:11], s97 offen offset:0 lds\n"
"v_dot2c_f32_bf16 v42, v176, v176\n"
"v_dot2c_f32_bf16 v41, s76, v177\n"
"v_dot2c_f32_bf16 v43, v177, v177\n"
"v_dot2c_f32_bf16 v40, s76, v178\n"
"v_dot2c_f32_bf16 v42, v178, v178\n"
"v_dot2c_f32_bf16 v41, s76, v179\n"
"v_dot2c_f32_bf16 v43, v179, v179\n"
"v_dot2c_f32_bf16 v40, s76, v180\n"
"v_dot2c_f32_bf16 v42, v180, v180\n"
"v_dot2c_f32_bf16 v41, s76, v181\n"
"v_dot2c_f32_bf16 v43, v181, v181\n"
"v_dot2c_f32_bf16 v40, s76, v182\n"
"v_dot2c_f32_bf16 v42, v182, v182\n"
"v_dot2c_f32_bf16 v41, s76, v183\n"
"v_dot2c_f32_bf16 v43, v183, v183\n"
"s_nop 1\n"
"v_add_f32 v40, v40, v41\n"
"v_add_f32 v42, v42, v43\n"
"v_mov_b32 v41, v40\n"
"v_mov_b32 v43, v42\n"
"buffer_load_dwordx4 v20, s[8:11], s97 offen offset:1024 lds\n"
"v_permlane32_swap_b32 v40, v41\n"
"s_cmp_eq_u32 s67, 18432\n"
"s_cselect_b32 s67, 0, s67\n"
"v_permlane32_swap_b32 v42, v43\n"
"s_lshr_b32 s97, s67, 4\n"
"s_add_u32 s97, s97, s65\n"
"v_add_f32 v40, v40, v41\n"
"s_add_u32 m0, s66, s67\n"
"s_add_u32 s67, s67, 2048\n"
"v_add_f32 v42, v42, v43\n"
"v_mov_b32 v41, v40\n"
"v_mov_b32 v43, v42\n"
"s_nop 0\n"
"v_permlane16_swap_b32 v40, v41\n"
"v_permlane16_swap_b32 v42, v43\n"
"v_add_f32 v40, v40, v41\n"
"v_add_f32 v42, v42, v43\n"
"v_mul_f32 v40, 0x3ae38e39, v40\n"
"buffer_load_dwordx4 v19, s[8:11], s97 offen offset:0 lds\n"
"v_mul_f32 v41, v40, v40\n"
"v_subrev_f32 v41, 0x3727c5ac, v41\n"
"v_mul_f32 v42, 0x3ae38e39, v42\n"
"v_sub_f32 v42, v42, v41\n"
"v_max_f32 v42, 0x3727c5ac, v42\n"
"v_rsq_f32 v44, v42\n"
"s_nop 0\n"
"v_mul_f32 v45, -v40, v44\n"
"v_lshlrev_b32 v48, 16, v112\n"
"v_and_b32 v49, s77, v112\n"
"v_lshlrev_b32 v50, 16, v113\n"
"v_and_b32 v51, s77, v113\n"
"v_lshlrev_b32 v52, 16, v114\n"
"v_and_b32 v53, s77, v114\n"
"v_lshlrev_b32 v54, 16, v115\n"
"v_and_b32 v55, s77, v115\n"
"v_pk_fma_f32 v[48:49], v[48:49], v[44:45], v[44:45] op_sel:[0,0,1] op_sel_hi:[1,0,1]\n"
"v_pk_fma_f32 v[50:51], v[50:51], v[44:45], v[44:45] op_sel:[0,0,1] op_sel_hi:[1,0,1]\n"
"v_pk_fma_f32 v[52:53], v[52:53], v[44:45], v[44:45] op_sel:[0,0,1] op_sel_hi:[1,0,1]\n"
"buffer_load_dwordx4 v20, s[8:11], s97 offen offset:1024 lds\n"
"v_pk_fma_f32 v[54:55], v[54:55], v[44:45], v[44:45] op_sel:[0,0,1] op_sel_hi:[1,0,1]\n"
"s_cmp_eq_u32 s67, 18432\n"
"s_cselect_b32 s67, 0, s67\n"
"v_cvt_pk_bf16_f32 v112, v48, v49\n"
"s_lshr_b32 s97, s67, 4\n"
"s_add_u32 s97, s97, s65\n"
"v_cvt_pk_bf16_f32 v113, v50, v51\n"
"s_add_u32 m0, s66, s67\n"
"s_add_u32 s67, s67, 2048\n"
"v_cvt_pk_bf16_f32 v114, v52, v53\n"
"v_cvt_pk_bf16_f32 v115, v54, v55\n"
"v_lshlrev_b32 v48, 16, v116\n"
"v_and_b32 v49, s77, v116\n"
"v_lshlrev_b32 v50, 16, v117\n"
"v_and_b32 v51, s77, v117\n"
"v_lshlrev_b32 v52, 16, v118\n"
"v_and_b32 v53, s77, v118\n"
"v_lshlrev_b32 v54, 16, v119\n"
"v_and_b32 v55, s77, v119\n"
"v_pk_fma_f32 v[48:49], v[48:49], v[44:45], v[44:45] op_sel:[0,0,1] op_sel_hi:[1,0,1]\n"
"v_pk_fma_f32 v[50:51], v[50:51], v[44:45], v[44:45] op_sel:[0,0,1] op_sel_hi:[1,0,1]\n"
"buffer_load_dwordx4 v19, s[8:11], s97 offen offset:0 lds\n"
"v_pk_fma_f32 v[52:53], v[52:53], v[44:45], v[44:45] op_sel:[0,0,1] op_sel_hi:[1,0,1]\n"
"v_pk_fma_f32 v[54:55], v[54:55], v[44:45], v[44:45] op_sel:[0,0,1] op_sel_hi:[1,0,1]\n"
"v_cvt_pk_bf16_f32 v116, v48, v49\n"
"v_cvt_pk_bf16_f32 v117, v50, v51\n"
"v_cvt_pk_bf16_f32 v118, v52, v53\n"
"v_cvt_pk_bf16_f32 v119, v54, v55\n"
"v_lshlrev_b32 v48, 16, v120\n"
"v_and_b32 v49, s77, v120\n"
"v_lshlrev_b32 v50, 16, v121\n"
"v_and_b32 v51, s77, v121\n"
"v_lshlrev_b32 v52, 16, v122\n"
"v_and_b32 v53, s77, v122\n"
"v_lshlrev_b32 v54, 16, v123\n"
"v_and_b32 v55, s77, v123\n"
"v_pk_fma_f32 v[48:49], v[48:49], v[44:45], v[44:45] op_sel:[0,0,1] op_sel_hi:[1,0,1]\n"
"v_pk_fma_f32 v[50:51], v[50:51], v[44:45], v[44:45] op_sel:[0,0,1] op_sel_hi:[1,0,1]\n"
"v_pk_fma_f32 v[52:53], v[52:53], v[44:45], v[44:45] op_sel:[0,0,1] op_sel_hi:[1,0,1]\n"
"v_pk_fma_f32 v[54:55], v[54:55], v[44:45], v[44:45] op_sel:[0,0,1] op_sel_hi:[1,0,1]\n"
"v_cvt_pk_bf16_f32 v120, v48, v49\n"
"buffer_load_dwordx4 v20, s[8:11], s97 offen offset:1024 lds\n"
"v_cvt_pk_bf16_f32 v121, v50, v51\n"
"s_cmp_eq_u32 s67, 18432\n"
"s_cselect_b32 s67, 0, s67\n"
"v_cvt_pk_bf16_f32 v122, v52, v53\n"
"s_lshr_b32 s97, s67, 4\n"
"s_add_u32 s97, s97, s65\n"
"v_cvt_pk_bf16_f32 v123, v54, v55\n"
"s_add_u32 m0, s66, s67\n"
"s_add_u32 s67, s67, 2048\n"
"v_lshlrev_b32 v48, 16, v124\n"
"v_and_b32 v49, s77, v124\n"
"v_lshlrev_b32 v50, 16, v125\n"
"v_and_b32 v51, s77, v125\n"
"v_lshlrev_b32 v52, 16, v126\n"
"v_and_b32 v53, s77, v126\n"
"v_lshlrev_b32 v54, 16, v127\n"
"v_and_b32 v55, s77, v127\n"
"v_pk_fma_f32 v[48:49], v[48:49], v[44:45], v[44:45] op_sel:[0,0,1] op_sel_hi:[1,0,1]\n"
"v_pk_fma_f32 v[50:51], v[50:51], v[44:45], v[44:45] op_sel:[0,0,1] op_sel_hi:[1,0,1]\n"
"v_pk_fma_f32 v[52:53], v[52:53], v[44:45], v[44:45] op_sel:[0,0,1] op_sel_hi:[1,0,1]\n"
"buffer_load_dwordx4 v19, s[8:11], s97 offen offset:0 lds\n"
"v_pk_fma_f32 v[54:55], v[54:55], v[44:45], v[44:45] op_sel:[0,0,1] op_sel_hi:[1,0,1]\n"
"v_cvt_pk_bf16_f32 v124, v48, v49\n"
"v_cvt_pk_bf16_f32 v125, v50, v51\n"
"v_cvt_pk_bf16_f32 v126, v52, v53\n"
"v_cvt_pk_bf16_f32 v127, v54, v55\n"
"v_lshlrev_b32 v48, 16, v128\n"
"v_and_b32 v49, s77, v128\n"
"v_lshlrev_b32 v50, 16, v129\n"
"v_and_b32 v51, s77, v129\n"
"v_lshlrev_b32 v52, 16, v130\n"
"v_and_b32 v53, s77, v130\n"
"v_lshlrev_b32 v54, 16, v131\n"
"v_and_b32 v55, s77, v131\n"
"v_pk_fma_f32 v[48:49], v[48:49], v[44:45], v[44:45] op_sel:[0,0,1] op_sel_hi:[1,0,1]\n"
"v_pk_fma_f32 v[50:51], v[50:51], v[44:45], v[44:45] op_sel:[0,0,1] op_sel_hi:[1,0,1]\n"
"v_pk_fma_f32 v[52:53], v[52:53], v[44:45], v[44:45] op_sel:[0,0,1] op_sel_hi:[1,0,1]\n"
"v_pk_fma_f32 v[54:55], v[54:55], v[44:45], v[44:45] op_sel:[0,0,1] op_sel_hi:[1,0,1]\n"
"v_cvt_pk_bf16_f32 v128, v48, v49\n"
"v_cvt_pk_bf16_f32 v129, v50, v51\n"
"v_cvt_pk_bf16_f32 v130, v52, v53\n"
"buffer_load_dwordx4 v20, s[8:11], s97 offen offset:1024 lds\n"
"v_cvt_pk_bf16_f32 v131, v54, v55\n"
"s_cmp_eq_u32 s67, 18432\n"
"s_cselect_b32 s67, 0, s67\n"
"v_lshlrev_b32 v48, 16, v132\n"
"s_lshr_b32 s97, s67, 4\n"
"s_add_u32 s97, s97, s65\n"
"v_and_b32 v49, s77, v132\n"
"s_add_u32 m0, s66, s67\n"
"s_add_u32 s67, s67, 2048\n"
"v_lshlrev_b32 v50, 16, v133\n"
"v_and_b32 v51, s77, v133\n"
"v_lshlrev_b32 v52, 16, v134\n"
"v_and_b32 v53, s77, v134\n"
"v_lshlrev_b32 v54, 16, v135\n"
"v_and_b32 v55, s77, v135\n"
"v_pk_fma_f32 v[48:49], v[48:49], v[44:45], v[44:45] op_sel:[0,0,1] op_sel_hi:[1,0,1]\n"
"v_pk_fma_f32 v[50:51], v[50:51], v[44:45], v[44:45] op_sel:[0,0,1] op_sel_hi:[1,0,1]\n"
"v_pk_fma_f32 v[52:53], v[52:53], v[44:45], v[44:45] op_sel:[0,0,1] op_sel_hi:[1,0,1]\n"
"v_pk_fma_f32 v[54:55], v[54:55], v[44:45], v[44:45] op_sel:[0,0,1] op_sel_hi:[1,0,1]\n"
"v_cvt_pk_bf16_f32 v132, v48, v49\n"
"buffer_load_dwordx4 v19, s[8:11], s97 offen offset:0 lds\n"
"v_cvt_pk_bf16_f32 v133, v50, v51\n"
"v_cvt_pk_bf16_f32 v134, v52, v53\n"
"v_cvt_pk_bf16_f32 v135, v54, v55\n"
"v_lshlrev_b32 v48, 16, v136\n"
"v_and_b32 v49, s77, v136\n"
"v_lshlrev_b32 v50, 16, v137\n"
"v_and_b32 v51, s77, v137\n"
"v_lshlrev_b32 v52, 16, v138\n"
"v_and_b32 v53, s77, v138\n"
"v_lshlrev_b32 v54, 16, v139\n"
"v_and_b32 v55, s77, v139\n"
"v_pk_fma_f32 v[48:49], v[48:49], v[44:45], v[44:45] op_sel:[0,0,1] op_sel_hi:[1,0,1]\n"
"v_pk_fma_f32 v[50:51], v[50:51], v[44:45], v[44:45] op_sel:[0,0,1] op_sel_hi:[1,0,1]\n"
"v_pk_fma_f32 v[52:53], v[52:53], v[44:45], v[44:45] op_sel:[0,0,1] op_sel_hi:[1,0,1]\n"
"v_pk_fma_f32 v[54:55], v[54:55], v[44:45], v[44:45] op_sel:[0,0,1] op_sel_hi:[1,0,1]\n"
"v_cvt_pk_bf16_f32 v136, v48, v49\n"
"v_cvt_pk_bf16_f32 v137, v50, v51\n"
"v_cvt_pk_bf16_f32 v138, v52, v53\n"
"v_cvt_pk_bf16_f32 v139, v54, v55\n"
"buffer_load_dwordx4 v20, s[8:11], s97 offen offset:1024 lds\n"
"v_lshlrev_b32 v48, 16, v140\n"
"s_cmp_eq_u32 s67, 18432\n"
"s_cselect_b32 s67, 0, s67\n"
"v_and_b32 v49, s77, v140\n"
"s_lshr_b32 s97, s67, 4\n"
"s_add_u32 s97, s97, s65\n"
"v_lshlrev_b32 v50, 16, v141\n"
"s_add_u32 m0, s66, s67\n"
"s_add_u32 s67, s67, 2048\n"
"v_and_b32 v51, s77, v141\n"
"v_lshlrev_b32 v52, 16, v142\n"
"v_and_b32 v53, s77, v142\n"
"v_lshlrev_b32 v54, 16, v143\n"
"v_and_b32 v55, s77, v143\n"
"v_pk_fma_f32 v[48:49], v[48:49], v[44:45], v[44:45] op_sel:[0,0,1] op_sel_hi:[1,0,1]\n"
"v_pk_fma_f32 v[50:51], v[50:51], v[44:45], v[44:45] op_sel:[0,0,1] op_sel_hi:[1,0,1]\n"
"v_pk_fma_f32 v[52:53], v[52:53], v[44:45], v[44:45] op_sel:[0,0,1] op_sel_hi:[1,0,1]\n"
"v_pk_fma_f32 v[54:55], v[54:55], v[44:45], v[44:45] op_sel:[0,0,1] op_sel_hi:[1,0,1]\n"
"v_cvt_pk_bf16_f32 v140, v48, v49\n"
"v_cvt_pk_bf16_f32 v141, v50, v51\n"
"v_cvt_pk_bf16_f32 v142, v52, v53\n"
"buffer_load_dwordx4 v19, s[8:11], s97 offen offset:0 lds\n"
"v_cvt_pk_bf16_f32 v143, v54, v55\n"
"v_lshlrev_b32 v48, 16, v144\n"
"v_and_b32 v49, s77, v144\n"
"v_lshlrev_b32 v50, 16, v145\n"
"v_and_b32 v51, s77, v145\n"
"v_lshlrev_b32 v52, 16, v146\n"
"v_and_b32 v53, s77, v146\n"
"v_lshlrev_b32 v54, 16, v147\n"
"v_and_b32 v55, s77, v147\n"
"v_pk_fma_f32 v[48:49], v[48:49], v[44:45], v[44:45] op_sel:[0,0,1] op_sel_hi:[1,0,1]\n"
"v_pk_fma_f32 v[50:51], v[50:51], v[44:45], v[44:45] op_sel:[0,0,1] op_sel_hi:[1,0,1]\n"
"v_pk_fma_f32 v[52:53], v[52:53], v[44:45], v[44:45] op_sel:[0,0,1] op_sel_hi:[1,0,1]\n"
"v_pk_fma_f32 v[54:55], v[54:55], v[44:45], v[44:45] op_sel:[0,0,1] op_sel_hi:[1,0,1]\n"
"v_cvt_pk_bf16_f32 v144, v48, v49\n"
"v_cvt_pk_bf16_f32 v145, v50, v51\n"
"v_cvt_pk_bf16_f32 v146, v52, v53\n"
"v_cvt_pk_bf16_f32 v147, v54, v55\n"
"v_lshlrev_b32 v48, 16, v148\n"
"v_and_b32 v49, s77, v148\n"
"buffer_load_dwordx4 v20, s[8:11], s97 offen offset:1024 lds\n"
"v_lshlrev_b32 v50, 16, v149\n"
"s_cmp_eq_u32 s67, 18432\n"
"s_cselect_b32 s67, 0, s67\n"
"v_and_b32 v51, s77, v149\n"
"s_add_u32 m0, s60, 0\n"
"v_lshlrev_b32 v52, 16, v150\n"
"v_and_b32 v53, s77, v150\n"
"v_lshlrev_b32 v54, 16, v151\n"
"v_and_b32 v55, s77, v151\n"
"v_pk_fma_f32 v[48:49], v[48:49], v[44:45], v[44:45] op_sel:[0,0,1] op_sel_hi:[1,0,1]\n"
"v_pk_fma_f32 v[50:51], v[50:51], v[44:45], v[44:45] op_sel:[0,0,1] op_sel_hi:[1,0,1]\n"
"v_pk_fma_f32 v[52:53], v[52:53], v[44:45], v[44:45] op_sel:[0,0,1] op_sel_hi:[1,0,1]\n"
"v_pk_fma_f32 v[54:55], v[54:55], v[44:45], v[44:45] op_sel:[0,0,1] op_sel_hi:[1,0,1]\n"
"v_cvt_pk_bf16_f32 v148, v48, v49\n"
"v_cvt_pk_bf16_f32 v149, v50, v51\n"
"v_cvt_pk_bf16_f32 v150, v52, v53\n"
"v_cvt_pk_bf16_f32 v151, v54, v55\n"
"v_lshlrev_b32 v48, 16, v152\n"
"v_and_b32 v49, s77, v152\n"
"v_lshlrev_b32 v50, 16, v153\n"
"buffer_load_dword a254, v38, s[12:15], s98 offen\n"
"v_and_b32 v51, s77, v153\n"
"v_lshlrev_b32 v52, 16, v154\n"
"v_and_b32 v53, s77, v154\n"
"v_lshlrev_b32 v54, 16, v155\n"
"v_and_b32 v55, s77, v155\n"
"v_pk_fma_f32 v[48:49], v[48:49], v[44:45], v[44:45] op_sel:[0,0,1] op_sel_hi:[1,0,1]\n"
"v_pk_fma_f32 v[50:51], v[50:51], v[44:45], v[44:45] op_sel:[0,0,1] op_sel_hi:[1,0,1]\n"
"v_pk_fma_f32 v[52:53], v[52:53], v[44:45], v[44:45] op_sel:[0,0,1] op_sel_hi:[1,0,1]\n"
"v_pk_fma_f32 v[54:55], v[54:55], v[44:45], v[44:45] op_sel:[0,0,1] op_sel_hi:[1,0,1]\n"
"v_cvt_pk_bf16_f32 v152, v48, v49\n"
"v_cvt_pk_bf16_f32 v153, v50, v51\n"
"v_cvt_pk_bf16_f32 v154, v52, v53\n"
"v_cvt_pk_bf16_f32 v155, v54, v55\n"
"v_lshlrev_b32 v48, 16, v156\n"
"v_and_b32 v49, s77, v156\n"
"v_lshlrev_b32 v50, 16, v157\n"
"v_and_b32 v51, s77, v157\n"
"v_lshlrev_b32 v52, 16, v158\n"
"v_and_b32 v53, s77, v158\n"
"v_lshlrev_b32 v54, 16, v159\n"
"v_and_b32 v55, s77, v159\n"
"v_pk_fma_f32 v[48:49], v[48:49], v[44:45], v[44:45] op_sel:[0,0,1] op_sel_hi:[1,0,1]\n"
"v_pk_fma_f32 v[50:51], v[50:51], v[44:45], v[44:45] op_sel:[0,0,1] op_sel_hi:[1,0,1]\n"
"v_pk_fma_f32 v[52:53], v[52:53], v[44:45], v[44:45] op_sel:[0,0,1] op_sel_hi:[1,0,1]\n"
"v_pk_fma_f32 v[54:55], v[54:55], v[44:45], v[44:45] op_sel:[0,0,1] op_sel_hi:[1,0,1]\n"
"v_cvt_pk_bf16_f32 v156, v48, v49\n"
"v_cvt_pk_bf16_f32 v157, v50, v51\n"
"v_cvt_pk_bf16_f32 v158, v52, v53\n"
"v_cvt_pk_bf16_f32 v159, v54, v55\n"
"v_lshlrev_b32 v48, 16, v160\n"
"v_and_b32 v49, s77, v160\n"
"v_lshlrev_b32 v50, 16, v161\n"
"v_and_b32 v51, s77, v161\n"
"v_lshlrev_b32 v52, 16, v162\n"
"v_and_b32 v53, s77, v162\n"
"v_lshlrev_b32 v54, 16, v163\n"
"v_and_b32 v55, s77, v163\n"
"v_pk_fma_f32 v[48:49], v[48:49], v[44:45], v[44:45] op_sel:[0,0,1] op_sel_hi:[1,0,1]\n"
"v_pk_fma_f32 v[50:51], v[50:51], v[44:45], v[44:45] op_sel:[0,0,1] op_sel_hi:[1,0,1]\n"
"v_pk_fma_f32 v[52:53], v[52:53], v[44:45], v[44:45] op_sel:[0,0,1] op_sel_hi:[1,0,1]\n"
"v_pk_fma_f32 v[54:55], v[54:55], v[44:45], v[44:45] op_sel:[0,0,1] op_sel_hi:[1,0,1]\n"
"v_cvt_pk_bf16_f32 v160, v48, v49\n"
"v_cvt_pk_bf16_f32 v161, v50, v51\n"
"v_cvt_pk_bf16_f32 v162, v52, v53\n"
"v_cvt_pk_bf16_f32 v163, v54, v55\n"
"v_lshlrev_b32 v48, 16, v164\n"
"v_and_b32 v49, s77, v164\n"
"v_lshlrev_b32 v50, 16, v165\n"
"v_and_b32 v51, s77, v165\n"
"v_lshlrev_b32 v52, 16, v166\n"
"v_and_b32 v53, s77, v166\n"
"v_lshlrev_b32 v54, 16, v167\n"
"v_and_b32 v55, s77, v167\n"
"v_pk_fma_f32 v[48:49], v[48:49], v[44:45], v[44:45] op_sel:[0,0,1] op_sel_hi:[1,0,1]\n"
"v_pk_fma_f32 v[50:51], v[50:51], v[44:45], v[44:45] op_sel:[0,0,1] op_sel_hi:[1,0,1]\n"
"v_pk_fma_f32 v[52:53], v[52:53], v[44:45], v[44:45] op_sel:[0,0,1] op_sel_hi:[1,0,1]\n"
"v_pk_fma_f32 v[54:55], v[54:55], v[44:45], v[44:45] op_sel:[0,0,1] op_sel_hi:[1,0,1]\n"
"v_cvt_pk_bf16_f32 v164, v48, v49\n"
"v_cvt_pk_bf16_f32 v165, v50, v51\n"
"v_cvt_pk_bf16_f32 v166, v52, v53\n"
"v_cvt_pk_bf16_f32 v167, v54, v55\n"
"v_lshlrev_b32 v48, 16, v168\n"
"v_and_b32 v49, s77, v168\n"
"v_lshlrev_b32 v50, 16, v169\n"
"v_and_b32 v51, s77, v169\n"
"v_lshlrev_b32 v52, 16, v170\n"
"v_and_b32 v53, s77, v170\n"
"v_lshlrev_b32 v54, 16, v171\n"
"v_and_b32 v55, s77, v171\n"
"v_pk_fma_f32 v[48:49], v[48:49], v[44:45], v[44:45] op_sel:[0,0,1] op_sel_hi:[1,0,1]\n"
"v_pk_fma_f32 v[50:51], v[50:51], v[44:45], v[44:45] op_sel:[0,0,1] op_sel_hi:[1,0,1]\n"
"v_pk_fma_f32 v[52:53], v[52:53], v[44:45], v[44:45] op_sel:[0,0,1] op_sel_hi:[1,0,1]\n"
"v_pk_fma_f32 v[54:55], v[54:55], v[44:45], v[44:45] op_sel:[0,0,1] op_sel_hi:[1,0,1]\n"
"v_cvt_pk_bf16_f32 v168, v48, v49\n"
"v_cvt_pk_bf16_f32 v169, v50, v51\n"
"v_cvt_pk_bf16_f32 v170, v52, v53\n"
"v_cvt_pk_bf16_f32 v171, v54, v55\n"
"v_lshlrev_b32 v48, 16, v172\n"
"v_and_b32 v49, s77, v172\n"
"buffer_load_dwordx4 v8, s[12:15], s61 offen offset:0 lds\n"
"v_lshlrev_b32 v50, 16, v173\n"
"v_and_b32 v51, s77, v173\n"
"v_lshlrev_b32 v52, 16, v174\n"
"v_and_b32 v53, s77, v174\n"
"v_lshlrev_b32 v54, 16, v175\n"
"v_and_b32 v55, s77, v175\n"
"v_pk_fma_f32 v[48:49], v[48:49], v[44:45], v[44:45] op_sel:[0,0,1] op_sel_hi:[1,0,1]\n"
"v_pk_fma_f32 v[50:51], v[50:51], v[44:45], v[44:45] op_sel:[0,0,1] op_sel_hi:[1,0,1]\n"
"v_pk_fma_f32 v[52:53], v[52:53], v[44:45], v[44:45] op_sel:[0,0,1] op_sel_hi:[1,0,1]\n"
"v_pk_fma_f32 v[54:55], v[54:55], v[44:45], v[44:45] op_sel:[0,0,1] op_sel_hi:[1,0,1]\n"
"v_cvt_pk_bf16_f32 v172, v48, v49\n"
"v_cvt_pk_bf16_f32 v173, v50, v51\n"
"v_cvt_pk_bf16_f32 v174, v52, v53\n"
"v_cvt_pk_bf16_f32 v175, v54, v55\n"
"v_lshlrev_b32 v48, 16, v176\n"
"v_and_b32 v49, s77, v176\n"
"v_lshlrev_b32 v50, 16, v177\n"
"v_and_b32 v51, s77, v177\n"
"v_lshlrev_b32 v52, 16, v178\n"
"v_and_b32 v53, s77, v178\n"
"buffer_load_dwordx4 v9, s[12:15], s61 offen offset:1024 lds\n"
"v_lshlrev_b32 v54, 16, v179\n"
"v_and_b32 v55, s77, v179\n"
"v_pk_fma_f32 v[48:49], v[48:49], v[44:45], v[44:45] op_sel:[0,0,1] op_sel_hi:[1,0,1]\n"
"v_pk_fma_f32 v[50:51], v[50:51], v[44:45], v[44:45] op_sel:[0,0,1] op_sel_hi:[1,0,1]\n"
"v_pk_fma_f32 v[52:53], v[52:53], v[44:45], v[44:45] op_sel:[0,0,1] op_sel_hi:[1,0,1]\n"
"v_pk_fma_f32 v[54:55], v[54:55], v[44:45], v[44:45] op_sel:[0,0,1] op_sel_hi:[1,0,1]\n"
"v_cvt_pk_bf16_f32 v176, v48, v49\n"
"v_cvt_pk_bf16_f32 v177, v50, v51\n"
"v_cvt_pk_bf16_f32 v178, v52, v53\n"
"v_cvt_pk_bf16_f32 v179, v54, v55\n"
"v_lshlrev_b32 v48, 16, v180\n"
"v_and_b32 v49, s77, v180\n"
"v_lshlrev_b32 v50, 16, v181\n"
"v_and_b32 v51, s77, v181\n"
"v_lshlrev_b32 v52, 16, v182\n"
"v_and_b32 v53, s77, v182\n"
"v_lshlrev_b32 v54, 16, v183\n"
"v_and_b32 v55, s77, v183\n"
"v_pk_fma_f32 v[48:49], v[48:49], v[44:45], v[44:45] op_sel:[0,0,1] op_sel_hi:[1,0,1]\n"
"v_pk_fma_f32 v[50:51], v[50:51], v[44:45], v[44:45] op_sel:[0,0,1] op_sel_hi:[1,0,1]\n"
"v_pk_fma_f32 v[52:53], v[52:53], v[44:45], v[44:45] op_sel:[0,0,1] op_sel_hi:[1,0,1]\n"
"buffer_load_dwordx4 v10, s[12:15], s61 offen offset:2048 lds\n"
"v_pk_fma_f32 v[54:55], v[54:55], v[44:45], v[44:45] op_sel:[0,0,1] op_sel_hi:[1,0,1]\n"
"v_cvt_pk_bf16_f32 v180, v48, v49\n"
"v_cvt_pk_bf16_f32 v181, v50, v51\n"
"v_cvt_pk_bf16_f32 v182, v52, v53\n"
"v_cvt_pk_bf16_f32 v183, v54, v55\n"
"v_mov_b32 v40, 0\n"
"v_mov_b32 v41, 0\n"
"v_mov_b32 v42, 0\n"
"v_mov_b32 v43, 0\n"
"s_waitcnt vmcnt(4)\n"
"ds_read_b128 v[184:187], v21 offset:0\n"
"ds_read_b128 v[188:191], v22 offset:0\n"
"ds_read_b128 v[192:195], v21 offset:2048\n"
"ds_read_b128 v[196:199], v22 offset:2048\n"
"ds_read_b128 v[200:203], v21 offset:4096\n"
"ds_read_b128 v[204:207], v22 offset:4096\n"
"ds_read_b128 v[208:211], v21 offset:6144\n"
"ds_read_b128 v[212:215], v22 offset:6144\n"
"ds_read_b128 v[216:219], v21 offset:8192\n"
"ds_read_b128 v[220:223], v22 offset:8192\n"
"ds_read_b128 v[224:227], v21 offset:10240\n"
"ds_read_b128 v[228:231], v22 offset:10240\n"
"buffer_load_dwordx4 v11, s[12:15], s61 offen offset:3072 lds\n"
"s_add_u32 m0, s60, 4096\n"
"s_waitcnt lgkmcnt(11)\n"
"v_dot2c_f32_bf16 v40, s76, v184\n"
"v_dot2c_f32_bf16 v42, v184, v184\n"
"v_dot2c_f32_bf16 v41, s76, v185\n"
"v_dot2c_f32_bf16 v43, v185, v185\n"
"v_dot2c_f32_bf16 v40, s76, v186\n"
"v_dot2c_f32_bf16 v42, v186, v186\n"
"v_dot2c_f32_bf16 v41, s76, v187\n"
"v_dot2c_f32_bf16 v43, v187, v187\n"
"s_waitcnt lgkmcnt(10)\n"
"v_dot2c_f32_bf16 v40, s76, v188\n"
"v_dot2c_f32_bf16 v42, v188, v188\n"
"v_dot2c_f32_bf16 v41, s76, v189\n"
"v_dot2c_f32_bf16 v43, v189, v189\n"
"v_dot2c_f32_bf16 v40, s76, v190\n"
"v_dot2c_f32_bf16 v42, v190, v190\n"
"v_dot2c_f32_bf16 v41, s76, v191\n"
"v_dot2c_f32_bf16 v43, v191, v191\n"
"s_waitcnt lgkmcnt(9)\n"
"v_dot2c_f32_bf16 v40, s76, v192\n"
"v_dot2c_f32_bf16 v42, v192, v192\n"
"v_dot2c_f32_bf16 v41, s76, v193\n"
"v_dot2c_f32_bf16 v43, v193, v193\n"
"buffer_load_dwordx4 v12, s[12:15], s61 offen offset:0 lds\n"
"v_dot2c_f32_bf16 v40, s76, v194\n"
"v_dot2c_f32_bf16 v42, v194, v194\n"
"v_dot2c_f32_bf16 v41, s76, v195\n"
"v_dot2c_f32_bf16 v43, v195, v195\n"
"s_waitcnt lgkmcnt(8)\n"
"v_dot2c_f32_bf16 v40, s76, v196\n"
"v_dot2c_f32_bf16 v42, v196, v196\n"
"v_dot2c_f32_bf16 v41, s76, v197\n"
"v_dot2c_f32_bf16 v43, v197, v197\n"
"v_dot2c_f32_bf16 v40, s76, v198\n"
"v_dot2c_f32_bf16 v42, v198, v198\n"
"v_dot2c_f32_bf16 v41, s76, v199\n"
"v_dot2c_f32_bf16 v43, v199, v199\n"
"s_waitcnt lgkmcnt(7)\n"
"v_dot2c_f32_bf16 v40, s76, v200\n"
"v_dot2c_f32_bf16 v42, v200, v200\n"
"v_dot2c_f32_bf16 v41, s76, v201\n"
"v_dot2c_f32_bf16 v43, v201, v201\n"
"v_dot2c_f32_bf16 v40, s76, v202\n"
"v_dot2c_f32_bf16 v42, v202, v202\n"
"v_dot2c_f32_bf16 v41, s76, v203\n"
"v_dot2c_f32_bf16 v43, v203, v203\n"
"s_waitcnt lgkmcnt(6)\n"
"v_dot2c_f32_bf16 v40, s76, v204\n"
"buffer_load_dwordx4 v13, s[12:15], s61 offen offset:1024 lds\n"
"v_dot2c_f32_bf16 v42, v204, v204\n"
"v_dot2c_f32_bf16 v41, s76, v205\n"
"v_dot2c_f32_bf16 v43, v205, v205\n"
"v_dot2c_f32_bf16 v40, s76, v206\n"
"v_dot2c_f32_bf16 v42, v206, v206\n"
"v_dot2c_f32_bf16 v41, s76, v207\n"
"v_dot2c_f32_bf16 v43, v207, v207\n"
"ds_read_b128 v[232:235], v21 offset:12288\n"
"ds_read_b128 v[236:239], v22 offset:12288\n"
"ds_read_b128 v[240:243], v21 offset:14336\n"
"ds_read_b128 v[244:247], v22 offset:14336\n"
"ds_read_b128 v[248:251], v21 offset:16384\n"
"ds_read_b128 v[252:255], v22 offset:16384\n"
"s_waitcnt lgkmcnt(11)\n"
"v_dot2c_f32_bf16 v40, s76, v208\n"
"v_dot2c_f32_bf16 v42, v208, v208\n"
"v_dot2c_f32_bf16 v41, s76, v209\n"
"v_dot2c_f32_bf16 v43, v209, v209\n"
"v_dot2c_f32_bf16 v40, s76, v210\n"
"v_dot2c_f32_bf16 v42, v210, v210\n"
"v_dot2c_f32_bf16 v41, s76, v211\n"
"v_dot2c_f32_bf16 v43, v211, v211\n"
"buffer_load_dwordx4 v14, s[12:15], s61 offen offset:2048 lds\n"
"s_waitcnt lgkmcnt(10)\n"
"v_dot2c_f32_bf16 v40, s76, v212\n"
"v_dot2c_f32_bf16 v42, v212, v212\n"
"v_dot2c_f32_bf16 v41, s76, v213\n"
"v_dot2c_f32_bf16 v43, v213, v213\n"
"v_dot2c_f32_bf16 v40, s76, v214\n"
"v_dot2c_f32_bf16 v42, v214, v214\n"
"v_dot2c_f32_bf16 v41, s76, v215\n"
"v_dot2c_f32_bf16 v43, v215, v215\n"
"s_waitcnt lgkmcnt(9)\n"
"v_dot2c_f32_bf16 v40, s76, v216\n"
"v_dot2c_f32_bf16 v42, v216, v216\n"
"v_dot2c_f32_bf16 v41, s76, v217\n"
"v_dot2c_f32_bf16 v43, v217, v217\n"
"v_dot2c_f32_bf16 v40, s76, v218\n"
"v_dot2c_f32_bf16 v42, v218, v218\n"
"v_dot2c_f32_bf16 v41, s76, v219\n"
"v_dot2c_f32_bf16 v43, v219, v219\n"
"s_waitcnt lgkmcnt(8)\n"
"v_dot2c_f32_bf16 v40, s76, v220\n"
"v_dot2c_f32_bf16 v42, v220, v220\n"
"v_dot2c_f32_bf16 v41, s76, v221\n"
"v_dot2c_f32_bf16 v43, v221, v221\n"
"buffer_load_dwordx4 v15, s[12:15], s61 offen offset:3072 lds\n"
"s_add_u32 m0, s60, 8192\n"
"v_dot2c_f32_bf16 v40, s76, v222\n"
"v_dot2c_f32_bf16 v42, v222, v222\n"
"v_dot2c_f32_bf16 v41, s76, v223\n"
"v_dot2c_f32_bf16 v43, v223, v223\n"
"s_waitcnt lgkmcnt(7)\n"
"v_dot2c_f32_bf16 v40, s76, v224\n"
"v_dot2c_f32_bf16 v42, v224, v224\n"
"v_dot2c_f32_bf16 v41, s76, v225\n"
"v_dot2c_f32_bf16 v43, v225, v225\n"
"v_dot2c_f32_bf16 v40, s76, v226\n"
"v_dot2c_f32_bf16 v42, v226, v226\n"
"v_dot2c_f32_bf16 v41, s76, v227\n"
"v_dot2c_f32_bf16 v43, v227, v227\n"
"s_waitcnt lgkmcnt(6)\n"
"v_dot2c_f32_bf16 v40, s76, v228\n"
"v_dot2c_f32_bf16 v42, v228, v228\n"
"v_dot2c_f32_bf16 v41, s76, v229\n"
"v_dot2c_f32_bf16 v43, v229, v229\n"
"v_dot2c_f32_bf16 v40, s76, v230\n"
"v_dot2c_f32_bf16 v42, v230, v230\n"
"v_dot2c_f32_bf16 v41, s76, v231\n"
"v_dot2c_f32_bf16 v43, v231, v231\n"
"buffer_load_dwordx4 v16, s[12:15], s61 offen offset:0 lds\n"
"s_add_u32 s61, s61, 1179648\n"
"s_waitcnt lgkmcnt(0)\n"
"s_add_u32 m0, s60, 36864\n"
"s_barrier\n"
"v_add_u32 v22, 0x20000, v6\n"
"v_dot2c_f32_bf16 v40, s76, v232\n"
"v_dot2c_f32_bf16 v42, v232, v232\n"
"v_dot2c_f32_bf16 v41, s76, v233\n"
"v_dot2c_f32_bf16 v43, v233, v233\n"
"v_dot2c_f32_bf16 v40, s76, v234\n"
"v_dot2c_f32_bf16 v42, v234, v234\n"
"v_dot2c_f32_bf16 v41, s76, v235\n"
"v_dot2c_f32_bf16 v43, v235, v235\n"
"v_dot2c_f32_bf16 v40, s76, v236\n"
"v_dot2c_f32_bf16 v42, v236, v236\n"
"v_dot2c_f32_bf16 v41, s76, v237\n"
"v_dot2c_f32_bf16 v43, v237, v237\n"
"v_dot2c_f32_bf16 v40, s76, v238\n"
"v_dot2c_f32_bf16 v42, v238, v238\n"
"v_dot2c_f32_bf16 v41, s76, v239\n"
"v_dot2c_f32_bf16 v43, v239, v239\n"
"v_dot2c_f32_bf16 v40, s76, v240\n"
"buffer_load_dwordx4 v8, s[12:15], s61 offen offset:0 lds\n"
"v_dot2c_f32_bf16 v42, v240, v240\n"
"v_dot2c_f32_bf16 v41, s76, v241\n"
"v_dot2c_f32_bf16 v43, v241, v241\n"
"v_dot2c_f32_bf16 v40, s76, v242\n"
"v_dot2c_f32_bf16 v42, v242, v242\n"
"v_dot2c_f32_bf16 v41, s76, v243\n"
"v_dot2c_f32_bf16 v43, v243, v243\n"
"v_dot2c_f32_bf16 v40, s76, v244\n"
"v_dot2c_f32_bf16 v42, v244, v244\n"
"v_dot2c_f32_bf16 v41, s76, v245\n"
"v_dot2c_f32_bf16 v43, v245, v245\n"
"v_dot2c_f32_bf16 v40, s76, v246\n"
"v_dot2c_f32_bf16 v42, v246, v246\n"
"v_dot2c_f32_bf16 v41, s76, v247\n"
"v_dot2c_f32_bf16 v43, v247, v247\n"
"v_dot2c_f32_bf16 v40, s76, v248\n"
"v_dot2c_f32_bf16 v42, v248, v248\n"
"v_dot2c_f32_bf16 v41, s76, v249\n"
"v_dot2c_f32_bf16 v43, v249, v249\n"
"v_dot2c_f32_bf16 v40, s76, v250\n"
"buffer_load_dwordx4 v9, s[12:15], s61 offen offset:1024 lds\n"
"v_dot2c_f32_bf16 v42, v250, v250\n"
"v_dot2c_f32_bf16 v41, s76, v251\n"
"v_dot2c_f32_bf16 v43, v251, v251\n"
"v_dot2c_f32_bf16 v40, s76, v252\n"
"v_dot2c_f32_bf16 v42, v252, v252\n"
"v_dot2c_f32_bf16 v41, s76, v253\n"
"v_dot2c_f32_bf16 v43, v253, v253\n"
"v_dot2c_f32_bf16 v40, s76, v254\n"
"v_dot2c_f32_bf16 v42, v254, v254\n"
"v_dot2c_f32_bf16 v41, s76, v255\n"
"v_dot2c_f32_bf16 v43, v255, v255\n"
"s_nop 1\n"
"v_add_f32 v40, v40, v41\n"
"v_add_f32 v42, v42, v43\n"
"v_mov_b32 v41, v40\n"
"v_mov_b32 v43, v42\n"
"s_nop 0\n"
"v_permlane32_swap_b32 v40, v41\n"
"v_permlane32_swap_b32 v42, v43\n"
"v_add_f32 v40, v40, v41\n"
"v_add_f32 v42, v42, v43\n"
"buffer_load_dwordx4 v10, s[12:15], s61 offen offset:2048 lds\n"
"v_mov_b32 v41, v40\n"
"v_mov_b32 v43, v42\n"
"s_nop 0\n"
"v_permlane16_swap_b32 v40, v41\n"
"v_permlane16_swap_b32 v42, v43\n"
"v_add_f32 v40, v40, v41\n"
"v_add_f32 v42, v42, v43\n"
"v_mul_f32 v40, 0x3ae38e39, v40\n"
"v_mul_f32 v41, v40, v40\n"
"v_subrev_f32 v41, 0x3727c5ac, v41\n"
"v_mul_f32 v42, 0x3ae38e39, v42\n"
"v_sub_f32 v42, v42, v41\n"
"v_max_f32 v42, 0x3727c5ac, v42\n"
"v_rsq_f32 v44, v42\n"
"s_nop 0\n"
"v_mul_f32 v45, -v40, v44\n"
"v_lshlrev_b32 v48, 16, v184\n"
"v_and_b32 v49, s77, v184\n"
"v_lshlrev_b32 v50, 16, v185\n"
"v_and_b32 v51, s77, v185\n"
"buffer_load_dwordx4 v11, s[12:15], s61 offen offset:3072 lds\n"
"s_add_u32 m0, s60, 40960\n"
"v_lshlrev_b32 v52, 16, v186\n"
"v_and_b32 v53, s77, v186\n"
"v_lshlrev_b32 v54, 16, v187\n"
"v_and_b32 v55, s77, v187\n"
"v_pk_fma_f32 v[48:49], v[48:49], v[44:45], v[44:45] op_sel:[0,0,1] op_sel_hi:[1,0,1]\n"
"v_pk_fma_f32 v[50:51], v[50:51], v[44:45], v[44:45] op_sel:[0,0,1] op_sel_hi:[1,0,1]\n"
"v_pk_fma_f32 v[52:53], v[52:53], v[44:45], v[44:45] op_sel:[0,0,1] op_sel_hi:[1,0,1]\n"
"v_pk_fma_f32 v[54:55], v[54:55], v[44:45], v[44:45] op_sel:[0,0,1] op_sel_hi:[1,0,1]\n"
"v_cvt_pk_bf16_f32 v184, v48, v49\n"
"v_cvt_pk_bf16_f32 v185, v50, v51\n"
"v_cvt_pk_bf16_f32 v186, v52, v53\n"
"v_cvt_pk_bf16_f32 v187, v54, v55\n"
"v_lshlrev_b32 v48, 16, v188\n"
"v_and_b32 v49, s77, v188\n"
"v_lshlrev_b32 v50, 16, v189\n"
"v_and_b32 v51, s77, v189\n"
"v_lshlrev_b32 v52, 16, v190\n"
"v_and_b32 v53, s77, v190\n"
"v_lshlrev_b32 v54, 16, v191\n"
"v_and_b32 v55, s77, v191\n"
"buffer_load_dwordx4 v12, s[12:15], s61 offen offset:0 lds\n"
"v_pk_fma_f32 v[48:49], v[48:49], v[44:45], v[44:45] op_sel:[0,0,1] op_sel_hi:[1,0,1]\n"
"v_pk_fma_f32 v[50:51], v[50:51], v[44:45], v[44:45] op_sel:[0,0,1] op_sel_hi:[1,0,1]\n"
"v_pk_fma_f32 v[52:53], v[52:53], v[44:45], v[44:45] op_sel:[0,0,1] op_sel_hi:[1,0,1]\n"
"v_pk_fma_f32 v[54:55], v[54:55], v[44:45], v[44:45] op_sel:[0,0,1] op_sel_hi:[1,0,1]\n"
"v_cvt_pk_bf16_f32 v188, v48, v49\n"
"v_cvt_pk_bf16_f32 v189, v50, v51\n"
"v_cvt_pk_bf16_f32 v190, v52, v53\n"
"v_cvt_pk_bf16_f32 v191, v54, v55\n"
"v_lshlrev_b32 v48, 16, v192\n"
"v_and_b32 v49, s77, v192\n"
"v_lshlrev_b32 v50, 16, v193\n"
"v_and_b32 v51, s77, v193\n"
"v_lshlrev_b32 v52, 16, v194\n"
"v_and_b32 v53, s77, v194\n"
"v_lshlrev_b32 v54, 16, v195\n"
"v_and_b32 v55, s77, v195\n"
"v_pk_fma_f32 v[48:49], v[48:49], v[44:45], v[44:45] op_sel:[0,0,1] op_sel_hi:[1,0,1]\n"
"v_pk_fma_f32 v[50:51], v[50:51], v[44:45], v[44:45] op_sel:[0,0,1] op_sel_hi:[1,0,1]\n"
"v_pk_fma_f32 v[52:53], v[52:53], v[44:45], v[44:45] op_sel:[0,0,1] op_sel_hi:[1,0,1]\n"
"v_pk_fma_f32 v[54:55], v[54:55], v[44:45], v[44:45] op_sel:[0,0,1] op_sel_hi:[1,0,1]\n"
"v_cvt_pk_bf16_f32 v192, v48, v49\n"
"buffer_load_dwordx4 v13, s[12:15], s61 offen offset:1024 lds\n"
"v_cvt_pk_bf16_f32 v193, v50, v51\n"
"v_cvt_pk_bf16_f32 v194, v52, v53\n"
"v_cvt_pk_bf16_f32 v195, v54, v55\n"
"v_lshlrev_b32 v48, 16, v196\n"
"v_and_b32 v49, s77, v196\n"
"v_lshlrev_b32 v50, 16, v197\n"
"v_and_b32 v51, s77, v197\n"
"v_lshlrev_b32 v52, 16, v198\n"
"v_and_b32 v53, s77, v198\n"
"v_lshlrev_b32 v54, 16, v199\n"
"v_and_b32 v55, s77, v199\n"
"v_pk_fma_f32 v[48:49], v[48:49], v[44:45], v[44:45] op_sel:[0,0,1] op_sel_hi:[1,0,1]\n"
"v_pk_fma_f32 v[50:51], v[50:51], v[44:45], v[44:45] op_sel:[0,0,1] op_sel_hi:[1,0,1]\n"
"v_pk_fma_f32 v[52:53], v[52:53], v[44:45], v[44:45] op_sel:[0,0,1] op_sel_hi:[1,0,1]\n"
"v_pk_fma_f32 v[54:55], v[54:55], v[44:45], v[44:45] op_sel:[0,0,1] op_sel_hi:[1,0,1]\n"
"v_cvt_pk_bf16_f32 v196, v48, v49\n"
"v_cvt_pk_bf16_f32 v197, v50, v51\n"
"v_cvt_pk_bf16_f32 v198, v52, v53\n"
"v_cvt_pk_bf16_f32 v199, v54, v55\n"
"v_lshlrev_b32 v48, 16, v200\n"
"buffer_load_dwordx4 v14, s[12:15], s61 offen offset:2048 lds\n"
"v_and_b32 v49, s77, v200\n"
"v_lshlrev_b32 v50, 16, v201\n"
"v_and_b32 v51, s77, v201\n"
"v_lshlrev_b32 v52, 16, v202\n"
"v_and_b32 v53, s77, v202\n"
"v_lshlrev_b32 v54, 16, v203\n"
"v_and_b32 v55, s77, v203\n"
"v_pk_fma_f32 v[48:49], v[48:49], v[44:45], v[44:45] op_sel:[0,0,1] op_sel_hi:[1,0,1]\n"
"v_pk_fma_f32 v[50:51], v[50:51], v[44:45], v[44:45] op_sel:[0,0,1] op_sel_hi:[1,0,1]\n"
"v_pk_fma_f32 v[52:53], v[52:53], v[44:45], v[44:45] op_sel:[0,0,1] op_sel_hi:[1,0,1]\n"
"v_pk_fma_f32 v[54:55], v[54:55], v[44:45], v[44:45] op_sel:[0,0,1] op_sel_hi:[1,0,1]\n"
"v_cvt_pk_bf16_f32 v200, v48, v49\n"
"v_cvt_pk_bf16_f32 v201, v50, v51\n"
"v_cvt_pk_bf16_f32 v202, v52, v53\n"
"v_cvt_pk_bf16_f32 v203, v54, v55\n"
"v_lshlrev_b32 v48, 16, v204\n"
"v_and_b32 v49, s77, v204\n"
"v_lshlrev_b32 v50, 16, v205\n"
"v_and_b32 v51, s77, v205\n"
"v_lshlrev_b32 v52, 16, v206\n"
"v_and_b32 v53, s77, v206\n"
"buffer_load_dwordx4 v15, s[12:15], s61 offen offset:3072 lds\n"
"s_add_u32 m0, s60, 45056\n"
"v_lshlrev_b32 v54, 16, v207\n"
"v_and_b32 v55, s77, v207\n"
"v_pk_fma_f32 v[48:49], v[48:49], v[44:45], v[44:45] op_sel:[0,0,1] op_sel_hi:[1,0,1]\n"
"v_pk_fma_f32 v[50:51], v[50:51], v[44:45], v[44:45] op_sel:[0,0,1] op_sel_hi:[1,0,1]\n"
"v_pk_fma_f32 v[52:53], v[52:53], v[44:45], v[44:45] op_sel:[0,0,1] op_sel_hi:[1,0,1]\n"
"v_pk_fma_f32 v[54:55], v[54:55], v[44:45], v[44:45] op_sel:[0,0,1] op_sel_hi:[1,0,1]\n"
"v_cvt_pk_bf16_f32 v204, v48, v49\n"
"v_cvt_pk_bf16_f32 v205, v50, v51\n"
"v_cvt_pk_bf16_f32 v206, v52, v53\n"
"v_cvt_pk_bf16_f32 v207, v54, v55\n"
"v_lshlrev_b32 v48, 16, v208\n"
"v_and_b32 v49, s77, v208\n"
"v_lshlrev_b32 v50, 16, v209\n"
"v_and_b32 v51, s77, v209\n"
"v_lshlrev_b32 v52, 16, v210\n"
"v_and_b32 v53, s77, v210\n"
"v_lshlrev_b32 v54, 16, v211\n"
"v_and_b32 v55, s77, v211\n"
"v_pk_fma_f32 v[48:49], v[48:49], v[44:45], v[44:45] op_sel:[0,0,1] op_sel_hi:[1,0,1]\n"
"v_pk_fma_f32 v[50:51], v[50:51], v[44:45], v[44:45] op_sel:[0,0,1] op_sel_hi:[1,0,1]\n"
"buffer_load_dwordx4 v16, s[12:15], s61 offen offset:0 lds\n"
"s_add_u32 s61, s61, 1179648\n"
"v_pk_fma_f32 v[52:53], v[52:53], v[44:45], v[44:45] op_sel:[0,0,1] op_sel_hi:[1,0,1]\n"
"s_add_u32 m0, s60, 73728\n"
"v_pk_fma_f32 v[54:55], v[54:55], v[44:45], v[44:45] op_sel:[0,0,1] op_sel_hi:[1,0,1]\n"
"v_cvt_pk_bf16_f32 v208, v48, v49\n"
"v_cvt_pk_bf16_f32 v209, v50, v51\n"
"v_cvt_pk_bf16_f32 v210, v52, v53\n"
"v_cvt_pk_bf16_f32 v211, v54, v55\n"
"v_lshlrev_b32 v48, 16, v212\n"
"v_and_b32 v49, s77, v212\n"
"v_lshlrev_b32 v50, 16, v213\n"
"v_and_b32 v51, s77, v213\n"
"v_lshlrev_b32 v52, 16, v214\n"
"v_and_b32 v53, s77, v214\n"
"v_lshlrev_b32 v54, 16, v215\n"
"v_and_b32 v55, s77, v215\n"
"v_pk_fma_f32 v[48:49], v[48:49], v[44:45], v[44:45] op_sel:[0,0,1] op_sel_hi:[1,0,1]\n"
"v_pk_fma_f32 v[50:51], v[50:51], v[44:45], v[44:45] op_sel:[0,0,1] op_sel_hi:[1,0,1]\n"
"v_pk_fma_f32 v[52:53], v[52:53], v[44:45], v[44:45] op_sel:[0,0,1] op_sel_hi:[1,0,1]\n"
"v_pk_fma_f32 v[54:55], v[54:55], v[44:45], v[44:45] op_sel:[0,0,1] op_sel_hi:[1,0,1]\n"
"v_cvt_pk_bf16_f32 v212, v48, v49\n"
"buffer_load_dword a253, v38, s[12:15], 0 offen\n"
"s_lshl_b32 s99, s63, 17\n"
"v_cvt_pk_bf16_f32 v213, v50, v51\n"
"s_lshl_b32 s98, s53, 8\n"
"s_add_u32 s99, s99, s98\n"
"v_cvt_pk_bf16_f32 v214, v52, v53\n"
"s_add_u32 s98, s99, 0\n"
"v_cvt_pk_bf16_f32 v215, v54, v55\n"
"v_lshlrev_b32 v48, 16, v216\n"
"v_and_b32 v49, s77, v216\n"
"v_lshlrev_b32 v50, 16, v217\n"
"v_and_b32 v51, s77, v217\n"
"v_lshlrev_b32 v52, 16, v218\n"
"v_and_b32 v53, s77, v218\n"
"v_lshlrev_b32 v54, 16, v219\n"
"v_and_b32 v55, s77, v219\n"
"v_pk_fma_f32 v[48:49], v[48:49], v[44:45], v[44:45] op_sel:[0,0,1] op_sel_hi:[1,0,1]\n"
"v_pk_fma_f32 v[50:51], v[50:51], v[44:45], v[44:45] op_sel:[0,0,1] op_sel_hi:[1,0,1]\n"
"v_pk_fma_f32 v[52:53], v[52:53], v[44:45], v[44:45] op_sel:[0,0,1] op_sel_hi:[1,0,1]\n"
"v_pk_fma_f32 v[54:55], v[54:55], v[44:45], v[44:45] op_sel:[0,0,1] op_sel_hi:[1,0,1]\n"
"v_cvt_pk_bf16_f32 v216, v48, v49\n"
"v_cvt_pk_bf16_f32 v217, v50, v51\n"
"v_cvt_pk_bf16_f32 v218, v52, v53\n"
"v_cvt_pk_bf16_f32 v219, v54, v55\n"
"v_lshlrev_b32 v48, 16, v220\n"
"v_and_b32 v49, s77, v220\n"
"v_lshlrev_b32 v50, 16, v221\n"
"v_and_b32 v51, s77, v221\n"
"v_lshlrev_b32 v52, 16, v222\n"
"v_and_b32 v53, s77, v222\n"
"v_lshlrev_b32 v54, 16, v223\n"
"v_and_b32 v55, s77, v223\n"
"v_pk_fma_f32 v[48:49], v[48:49], v[44:45], v[44:45] op_sel:[0,0,1] op_sel_hi:[1,0,1]\n"
"v_pk_fma_f32 v[50:51], v[50:51], v[44:45], v[44:45] op_sel:[0,0,1] op_sel_hi:[1,0,1]\n"
"v_pk_fma_f32 v[52:53], v[52:53], v[44:45], v[44:45] op_sel:[0,0,1] op_sel_hi:[1,0,1]\n"
"v_pk_fma_f32 v[54:55], v[54:55], v[44:45], v[44:45] op_sel:[0,0,1] op_sel_hi:[1,0,1]\n"
"v_cvt_pk_bf16_f32 v220, v48, v49\n"
"v_cvt_pk_bf16_f32 v221, v50, v51\n"
"v_cvt_pk_bf16_f32 v222, v52, v53\n"
"v_cvt_pk_bf16_f32 v223, v54, v55\n"
"v_lshlrev_b32 v48, 16, v224\n"
"v_and_b32 v49, s77, v224\n"
"v_lshlrev_b32 v50, 16, v225\n"
"v_and_b32 v51, s77, v225\n"
"v_lshlrev_b32 v52, 16, v226\n"
"v_and_b32 v53, s77, v226\n"
"v_lshlrev_b32 v54, 16, v227\n"
"v_and_b32 v55, s77, v227\n"
"v_pk_fma_f32 v[48:49], v[48:49], v[44:45], v[44:45] op_sel:[0,0,1] op_sel_hi:[1,0,1]\n"
"v_pk_fma_f32 v[50:51], v[50:51], v[44:45], v[44:45] op_sel:[0,0,1] op_sel_hi:[1,0,1]\n"
"v_pk_fma_f32 v[52:53], v[52:53], v[44:45], v[44:45] op_sel:[0,0,1] op_sel_hi:[1,0,1]\n"
"v_pk_fma_f32 v[54:55], v[54:55], v[44:45], v[44:45] op_sel:[0,0,1] op_sel_hi:[1,0,1]\n"
"v_cvt_pk_bf16_f32 v224, v48, v49\n"
"v_cvt_pk_bf16_f32 v225, v50, v51\n"
"v_cvt_pk_bf16_f32 v226, v52, v53\n"
"v_cvt_pk_bf16_f32 v227, v54, v55\n"
"v_lshlrev_b32 v48, 16, v228\n"
"v_and_b32 v49, s77, v228\n"
"v_lshlrev_b32 v50, 16, v229\n"
"v_and_b32 v51, s77, v229\n"
"v_lshlrev_b32 v52, 16, v230\n"
"v_and_b32 v53, s77, v230\n"
"v_lshlrev_b32 v54, 16, v231\n"
"v_and_b32 v55, s77, v231\n"
"v_pk_fma_f32 v[48:49], v[48:49], v[44:45], v[44:45] op_sel:[0,0,1] op_sel_hi:[1,0,1]\n"
"v_pk_fma_f32 v[50:51], v[50:51], v[44:45], v[44:45] op_sel:[0,0,1] op_sel_hi:[1,0,1]\n"
"v_pk_fma_f32 v[52:53], v[52:53], v[44:45], v[44:45] op_sel:[0,0,1] op_sel_hi:[1,0,1]\n"
"v_pk_fma_f32 v[54:55], v[54:55], v[44:45], v[44:45] op_sel:[0,0,1] op_sel_hi:[1,0,1]\n"
"v_cvt_pk_bf16_f32 v228, v48, v49\n"
"v_cvt_pk_bf16_f32 v229, v50, v51\n"
"v_cvt_pk_bf16_f32 v230, v52, v53\n"
"v_cvt_pk_bf16_f32 v231, v54, v55\n"
"v_lshlrev_b32 v48, 16, v232\n"
"v_and_b32 v49, s77, v232\n"
"v_lshlrev_b32 v50, 16, v233\n"
"v_and_b32 v51, s77, v233\n"
"v_lshlrev_b32 v52, 16, v234\n"
"v_and_b32 v53, s77, v234\n"
"v_lshlrev_b32 v54, 16, v235\n"
"v_and_b32 v55, s77, v235\n"
"v_pk_fma_f32 v[48:49], v[48:49], v[44:45], v[44:45] op_sel:[0,0,1] op_sel_hi:[1,0,1]\n"
"buffer_load_dwordx4 v8, s[12:15], s61 offen offset:0 lds\n"
"v_pk_fma_f32 v[50:51], v[50:51], v[44:45], v[44:45] op_sel:[0,0,1] op_sel_hi:[1,0,1]\n"
"v_pk_fma_f32 v[52:53], v[52:53], v[44:45], v[44:45] op_sel:[0,0,1] op_sel_hi:[1,0,1]\n"
"v_pk_fma_f32 v[54:55], v[54:55], v[44:45], v[44:45] op_sel:[0,0,1] op_sel_hi:[1,0,1]\n"
"v_cvt_pk_bf16_f32 v232, v48, v49\n"
"v_cvt_pk_bf16_f32 v233, v50, v51\n"
"v_cvt_pk_bf16_f32 v234, v52, v53\n"
"v_cvt_pk_bf16_f32 v235, v54, v55\n"
"v_lshlrev_b32 v48, 16, v236\n"
"v_and_b32 v49, s77, v236\n"
"v_lshlrev_b32 v50, 16, v237\n"
"v_and_b32 v51, s77, v237\n"
"v_lshlrev_b32 v52, 16, v238\n"
"v_and_b32 v53, s77, v238\n"
"v_lshlrev_b32 v54, 16, v239\n"
"v_and_b32 v55, s77, v239\n"
"v_pk_fma_f32 v[48:49], v[48:49], v[44:45], v[44:45] op_sel:[0,0,1] op_sel_hi:[1,0,1]\n"
"v_pk_fma_f32 v[50:51], v[50:51], v[44:45], v[44:45] op_sel:[0,0,1] op_sel_hi:[1,0,1]\n"
"v_pk_fma_f32 v[52:53], v[52:53], v[44:45], v[44:45] op_sel:[0,0,1] op_sel_hi:[1,0,1]\n"
"v_pk_fma_f32 v[54:55], v[54:55], v[44:45], v[44:45] op_sel:[0,0,1] op_sel_hi:[1,0,1]\n"
"v_cvt_pk_bf16_f32 v236, v48, v49\n"
"buffer_load_dwordx4 v9, s[12:15], s61 offen offset:1024 lds\n"
"v_cvt_pk_bf16_f32 v237, v50, v51\n"
"v_cvt_pk_bf16_f32 v238, v52, v53\n"
"v_cvt_pk_bf16_f32 v239, v54, v55\n"
"v_lshlrev_b32 v48, 16, v240\n"
"v_and_b32 v49, s77, v240\n"
"v_lshlrev_b32 v50, 16, v241\n"
"v_and_b32 v51, s77, v241\n"
"v_lshlrev_b32 v52, 16, v242\n"
"v_and_b32 v53, s77, v242\n"
"v_lshlrev_b32 v54, 16, v243\n"
"v_and_b32 v55, s77, v243\n"
"v_pk_fma_f32 v[48:49], v[48:49], v[44:45], v[44:45] op_sel:[0,0,1] op_sel_hi:[1,0,1]\n"
"v_pk_fma_f32 v[50:51], v[50:51], v[44:45], v[44:45] op_sel:[0,0,1] op_sel_hi:[1,0,1]\n"
"v_pk_fma_f32 v[52:53], v[52:53], v[44:45], v[44:45] op_sel:[0,0,1] op_sel_hi:[1,0,1]\n"
"v_pk_fma_f32 v[54:55], v[54:55], v[44:45], v[44:45] op_sel:[0,0,1] op_sel_hi:[1,0,1]\n"
"v_cvt_pk_bf16_f32 v240, v48, v49\n"
"v_cvt_pk_bf16_f32 v241, v50, v51\n"
"v_cvt_pk_bf16_f32 v242, v52, v53\n"
"v_cvt_pk_bf16_f32 v243, v54, v55\n"
"s_waitcnt vmcnt(12)\n"
"s_barrier\n"
"buffer_load_dwordx4 v10, s[12:15], s61 offen offset:2048 lds\n"
"ds_read_b64_tr_b16 a[144:145], v6 offset:0\n"
"ds_read_b64_tr_b16 a[146:147], v6 offset:4608\n"
"ds_read_b64_tr_b16 a[148:149], v6 offset:32\n"
"ds_read_b64_tr_b16 a[150:151], v6 offset:4640\n"
"ds_read_b64_tr_b16 a[152:153], v6 offset:64\n"
"ds_read_b64_tr_b16 a[154:155], v6 offset:4672\n"
"ds_read_b64_tr_b16 a[156:157], v6 offset:96\n"
"ds_read_b64_tr_b16 a[158:159], v6 offset:4704\n"
"ds_read_b64_tr_b16 a[160:161], v6 offset:128\n"
"ds_read_b64_tr_b16 a[162:163], v6 offset:4736\n"
"ds_read_b64_tr_b16 a[164:165], v6 offset:160\n"
"ds_read_b64_tr_b16 a[166:167], v6 offset:4768\n"
"ds_read_b64_tr_b16 a[168:169], v6 offset:192\n"
"ds_read_b64_tr_b16 a[170:171], v6 offset:4800\n"
"v_lshlrev_b32 v48, 16, v244\n"
"v_and_b32 v49, s77, v244\n"
"v_lshlrev_b32 v50, 16, v245\n"
"v_and_b32 v51, s77, v245\n"
"v_lshlrev_b32 v52, 16, v246\n"
"v_and_b32 v53, s77, v246\n"
"v_lshlrev_b32 v54, 16, v247\n"
"v_and_b32 v55, s77, v247\n"
"v_pk_fma_f32 v[48:49], v[48:49], v[44:45], v[44:45] op_sel:[0,0,1] op_sel_hi:[1,0,1]\n"
"v_pk_fma_f32 v[50:51], v[50:51], v[44:45], v[44:45] op_sel:[0,0,1] op_sel_hi:[1,0,1]\n"
"buffer_load_dwordx4 v11, s[12:15], s61 offen offset:3072 lds\n"
"s_add_u32 m0, s60, 77824\n"
"v_pk_fma_f32 v[52:53], v[52:53], v[44:45], v[44:45] op_sel:[0,0,1] op_sel_hi:[1,0,1]\n"
"v_pk_fma_f32 v[54:55], v[54:55], v[44:45], v[44:45] op_sel:[0,0,1] op_sel_hi:[1,0,1]\n"
"v_cvt_pk_bf16_f32 v244, v48, v49\n"
"v_cvt_pk_bf16_f32 v245, v50, v51\n"
"v_cvt_pk_bf16_f32 v246, v52, v53\n"
"v_cvt_pk_bf16_f32 v247, v54, v55\n"
"v_lshlrev_b32 v48, 16, v248\n"
"v_and_b32 v49, s77, v248\n"
"v_lshlrev_b32 v50, 16, v249\n"
"v_and_b32 v51, s77, v249\n"
"v_lshlrev_b32 v52, 16, v250\n"
"v_and_b32 v53, s77, v250\n"
"v_lshlrev_b32 v54, 16, v251\n"
"v_and_b32 v55, s77, v251\n"
"v_pk_fma_f32 v[48:49], v[48:49], v[44:45], v[44:45] op_sel:[0,0,1] op_sel_hi:[1,0,1]\n"
"v_pk_fma_f32 v[50:51], v[50:51], v[44:45], v[44:45] op_sel:[0,0,1] op_sel_hi:[1,0,1]\n"
"v_pk_fma_f32 v[52:53], v[52:53], v[44:45], v[44:45] op_sel:[0,0,1] op_sel_hi:[1,0,1]\n"
"v_pk_fma_f32 v[54:55], v[54:55], v[44:45], v[44:45] op_sel:[0,0,1] op_sel_hi:[1,0,1]\n"
"v_cvt_pk_bf16_f32 v248, v48, v49\n"
"v_cvt_pk_bf16_f32 v249, v50, v51\n"
"buffer_load_dwordx4 v12, s[12:15], s61 offen offset:0 lds\n"
"v_cvt_pk_bf16_f32 v250, v52, v53\n"
"v_cvt_pk_bf16_f32 v251, v54, v55\n"
"v_lshlrev_b32 v48, 16, v252\n"
"v_and_b32 v49, s77, v252\n"
"v_lshlrev_b32 v50, 16, v253\n"
"v_and_b32 v51, s77, v253\n"
"v_lshlrev_b32 v52, 16, v254\n"
"v_and_b32 v53, s77, v254\n"
"v_lshlrev_b32 v54, 16, v255\n"
"v_and_b32 v55, s77, v255\n"
"v_pk_fma_f32 v[48:49], v[48:49], v[44:45], v[44:45] op_sel:[0,0,1] op_sel_hi:[1,0,1]\n"
"v_pk_fma_f32 v[50:51], v[50:51], v[44:45], v[44:45] op_sel:[0,0,1] op_sel_hi:[1,0,1]\n"
"v_pk_fma_f32 v[52:53], v[52:53], v[44:45], v[44:45] op_sel:[0,0,1] op_sel_hi:[1,0,1]\n"
"v_pk_fma_f32 v[54:55], v[54:55], v[44:45], v[44:45] op_sel:[0,0,1] op_sel_hi:[1,0,1]\n"
"v_cvt_pk_bf16_f32 v252, v48, v49\n"
"v_cvt_pk_bf16_f32 v253, v50, v51\n"
"v_cvt_pk_bf16_f32 v254, v52, v53\n"
"v_cvt_pk_bf16_f32 v255, v54, v55\n"
"s_waitcnt lgkmcnt(12)\n"
"v_mfma_f32_16x16x32_bf16 v[40:43], v[112:115], a[144:147], 0\n"
".Lmk_PCHERE_1:\n"
"s_add_u32 s0, s0, .Lmk_PCHERE_1-.Lmk_PCHERE\n"
"s_addc_u32 s1, s1, 0\n"
"s_sub_u32 s2, s2, .Lmk_PCHERE_1-.Lmk_PCHERE\n"
"s_max_i32 s2, s2, 0\n"
"buffer_load_dword a255, off, s[0:3], 0\n"
"ds_read_b64_tr_b16 a[172:173], v6 offset:224\n"
"ds_read_b64_tr_b16 a[174:175], v6 offset:4832\n"
"v_mfma_f32_16x16x32_bf16 v[44:47], v[184:187], a[144:147], 0\n"
"buffer_load_dwordx4 v13, s[12:15], s61 offen offset:1024 lds\n"
"s_lshl_b32 s62, s63, 17\n"
"s_waitcnt lgkmcnt(12)\n"
"v_mfma_f32_16x16x32_bf16 v[48:51], v[112:115], a[148:151], 0\n"
"ds_read_b64_tr_b16 a[144:145], v6 offset:256\n"
"ds_read_b64_tr_b16 a[146:147], v6 offset:4864\n"
"v_mfma_f32_16x16x32_bf16 v[52:55], v[184:187], a[148:151], 0\n"
"s_add_u32 s62, s62, 9437184\n"
"s_lshl_b32 s74, s53, 13\n"
"s_waitcnt lgkmcnt(12)\n"
"v_mfma_f32_16x16x32_bf16 v[56:59], v[112:115], a[152:155], 0\n"
"ds_read_b64_tr_b16 a[148:149], v6 offset:9216\n"
"ds_read_b64_tr_b16 a[150:151], v6 offset:13824\n"
"v_mfma_f32_16x16x32_bf16 v[60:63], v[184:187], a[152:155], 0\n"
"s_add_u32 s74, s74, s62\n"
"s_waitcnt lgkmcnt(8)\n"
"v_mfma_f32_16x16x32_bf16 v[64:67], v[112:115], a[156:159], 0\n"
"ds_read_b64_tr_b16 a[152:153], v6 offset:9248\n"
"ds_read_b64_tr_b16 a[154:155], v6 offset:13856\n"
"v_mfma_f32_16x16x32_bf16 v[68:71], v[184:187], a[156:159], 0\n"
"buffer_load_dwordx4 v14, s[12:15], s61 offen offset:2048 lds\n"
"s_add_u32 s75, s74, 4096\n"
"v_mfma_f32_16x16x32_bf16 v[72:75], v[112:115], a[160:163], 0\n"
"ds_read_b64_tr_b16 a[156:157], v6 offset:9280\n"
"ds_read_b64_tr_b16 a[158:159], v6 offset:13888\n"
"v_mfma_f32_16x16x32_bf16 v[76:79], v[184:187], a[160:163], 0\n"
"s_mov_b32 s65, 6912\n"
"s_mov_b32 s66, 13824\n"
"v_mfma_f32_16x16x32_bf16 v[80:83], v[112:115], a[164:167], 0\n"
"ds_read_b64_tr_b16 a[160:161], v6 offset:9312\n"
"ds_read_b64_tr_b16 a[162:163], v6 offset:13920\n"
"v_mfma_f32_16x16x32_bf16 v[84:87], v[184:187], a[164:167], 0\n"
"s_mov_b32 s67, 20736\n"
"s_waitcnt lgkmcnt(8)\n"
"v_mfma_f32_16x16x32_bf16 v[88:91], v[112:115], a[168:171], 0\n"
"ds_read_b64_tr_b16 a[164:165], v6 offset:9344\n"
"ds_read_b64_tr_b16 a[166:167], v6 offset:13952\n"
"v_mfma_f32_16x16x32_bf16 v[92:95], v[184:187], a[168:171], 0\n"
"buffer_load_dwordx4 v15, s[12:15], s61 offen offset:3072 lds\n"
"s_add_u32 m0, s60, 81920\n"
"v_mfma_f32_16x16x32_bf16 v[96:99], v[112:115], a[172:175], 0\n"
"ds_read_b64_tr_b16 a[168:169], v6 offset:9376\n"
"ds_read_b64_tr_b16 a[170:171], v6 offset:13984\n"
"v_mfma_f32_16x16x32_bf16 v[100:103], v[184:187], a[172:175], 0\n"
"v_accvgpr_write_b32 a0, v112\n"
"v_accvgpr_write_b32 a1, v113\n"
"v_mfma_f32_16x16x32_bf16 v[104:107], v[112:115], a[144:147], 0\n"
"ds_read_b64_tr_b16 a[172:173], v6 offset:9408\n"
"ds_read_b64_tr_b16 a[174:175], v6 offset:14016\n"
"v_mfma_f32_16x16x32_bf16 v[108:111], v[184:187], a[144:147], 0\n"
"v_accvgpr_write_b32 a2, v114\n"
"s_waitcnt lgkmcnt(8)\n"
"v_mfma_f32_16x16x32_bf16 v[40:43], v[116:119], a[148:151], v[40:43]\n"
"ds_read_b64_tr_b16 a[144:145], v6 offset:9440\n"
"ds_read_b64_tr_b16 a[146:147], v6 offset:14048\n"
"v_mfma_f32_16x16x32_bf16 v[44:47], v[188:191], a[148:151], v[44:47]\n"
"buffer_load_dwordx4 v16, s[12:15], s61 offen offset:0 lds\n"
"s_add_u32 s61, s61, 1179648\n"
"v_mfma_f32_16x16x32_bf16 v[48:51], v[116:119], a[152:155], v[48:51]\n"
"ds_read_b64_tr_b16 a[148:149], v6 offset:9472\n"
"ds_read_b64_tr_b16 a[150:151], v6 offset:14080\n"
"v_mfma_f32_16x16x32_bf16 v[52:55], v[188:191], a[152:155], v[52:55]\n"
"s_add_u32 m0, s60, 110592\n"
"v_accvgpr_write_b32 a3, v115\n"
"v_mfma_f32_16x16x32_bf16 v[56:59], v[116:119], a[156:159], v[56:59]\n"
"ds_read_b64_tr_b16 a[152:153], v6 offset:18432\n"
"ds_read_b64_tr_b16 a[154:155], v6 offset:23040\n"
"v_mfma_f32_16x16x32_bf16 v[60:63], v[188:191], a[156:159], v[60:63]\n"
"buffer_load_dwordx4 v8, s[12:15], s61 offen offset:0 lds\n"
"s_waitcnt lgkmcnt(8)\n"
"v_mfma_f32_16x16x32_bf16 v[64:67], v[116:119], a[160:163], v[64:67]\n"
"ds_read_b64_tr_b16 a[156:157], v6 offset:18464\n"
"ds_read_b64_tr_b16 a[158:159], v6 offset:23072\n"
"v_mfma_f32_16x16x32_bf16 v[68:71], v[188:191], a[160:163], v[68:71]\n"
"v_accvgpr_write_b32 a4, v184\n"
"v_accvgpr_write_b32 a5, v185\n"
"v_mfma_f32_16x16x32_bf16 v[72:75], v[116:119], a[164:167], v[72:75]\n"
"ds_read_b64_tr_b16 a[160:161], v6 offset:18496\n"
"ds_read_b64_tr_b16 a[162:163], v6 offset:23104\n"
"v_mfma_f32_16x16x32_bf16 v[76:79], v[188:191], a[164:167], v[76:79]\n"
"v_accvgpr_write_b32 a6, v186\n"
"v_accvgpr_write_b32 a7, v187\n"
"v_mfma_f32_16x16x32_bf16 v[80:83], v[116:119], a[168:171], v[80:83]\n"
"ds_read_b64_tr_b16 a[164:165], v6 offset:18528\n"
"ds_read_b64_tr_b16 a[166:167], v6 offset:23136\n"
"v_mfma_f32_16x16x32_bf16 v[84:87], v[188:191], a[168:171], v[84:87]\n"
"buffer_load_dwordx4 v9, s[12:15], s61 offen offset:1024 lds\n"
"s_waitcnt lgkmcnt(8)\n"
"v_mfma_f32_16x16x32_bf16 v[88:91], v[116:119], a[172:175], v[88:91]\n"
"ds_read_b64_tr_b16 a[168:169], v6 offset:18560\n"
"ds_read_b64_tr_b16 a[170:171], v6 offset:23168\n"
"v_mfma_f32_16x16x32_bf16 v[92:95], v[188:191], a[172:175], v[92:95]\n"
"v_accvgpr_write_b32 a8, v116\n"
"v_accvgpr_write_b32 a9, v117\n"
"v_mfma_f32_16x16x32_bf16 v[96:99], v[116:119], a[144:147], v[96:99]\n"
"ds_read_b64_tr_b16 a[172:173], v6 offset:18592\n"
"ds_read_b64_tr_b16 a[174:175], v6 offset:23200\n"
"v_mfma_f32_16x16x32_bf16 v[100:103], v[188:191], a[144:147], v[100:103]\n"
"v_accvgpr_write_b32 a10, v118\n"
"v_accvgpr_write_b32 a11, v119\n"
"v_mfma_f32_16x16x32_bf16 v[104:107], v[116:119], a[148:151], v[104:107]\n"
"ds_read_b64_tr_b16 a[144:145], v6 offset:18624\n"
"ds_read_b64_tr_b16 a[146:147], v6 offset:23232\n"
"v_mfma_f32_16x16x32_bf16 v[108:111], v[188:191], a[148:151], v[108:111]\n"
"buffer_load_dwordx4 v10, s[12:15], s61 offen offset:2048 lds\n"
"s_waitcnt lgkmcnt(8)\n"
"v_mfma_f32_16x16x32_bf16 v[40:43], v[120:123], a[152:155], v[40:43]\n"
"ds_read_b64_tr_b16 a[148:149], v6 offset:18656\n"
"ds_read_b64_tr_b16 a[150:151], v6 offset:23264\n"
"v_mfma_f32_16x16x32_bf16 v[44:47], v[192:195], a[152:155], v[44:47]\n"
"v_accvgpr_write_b32 a12, v188\n"
"v_accvgpr_write_b32 a13, v189\n"
"v_mfma_f32_16x16x32_bf16 v[48:51], v[120:123], a[156:159], v[48:51]\n"
"ds_read_b64_tr_b16 a[152:153], v6 offset:18688\n"
"ds_read_b64_tr_b16 a[154:155], v6 offset:23296\n"
"v_mfma_f32_16x16x32_bf16 v[52:55], v[192:195], a[156:159], v[52:55]\n"
"v_accvgpr_write_b32 a14, v190\n"
"v_accvgpr_write_b32 a15, v191\n"
"v_mfma_f32_16x16x32_bf16 v[56:59], v[120:123], a[160:163], v[56:59]\n"
"ds_read_b64_tr_b16 a[156:157], v6 offset:27648\n"
"ds_read_b64_tr_b16 a[158:159], v6 offset:32256\n"
"v_mfma_f32_16x16x32_bf16 v[60:63], v[192:195], a[160:163], v[60:63]\n"
"buffer_load_dwordx4 v11, s[12:15], s61 offen offset:3072 lds\n"
"s_waitcnt lgkmcnt(8)\n"
"v_mfma_f32_16x16x32_bf16 v[64:67], v[120:123], a[164:167], v[64:67]\n"
"ds_read_b64_tr_b16 a[160:161], v6 offset:27680\n"
"ds_read_b64_tr_b16 a[162:163], v6 offset:32288\n"
"v_mfma_f32_16x16x32_bf16 v[68:71], v[192:195], a[164:167], v[68:71]\n"
"s_add_u32 m0, s60, 114688\n"
"v_accvgpr_write_b32 a16, v120\n"
"v_mfma_f32_16x16x32_bf16 v[72:75], v[120:123], a[168:171], v[72:75]\n"
"ds_read_b64_tr_b16 a[164:165], v6 offset:27712\n"
"ds_read_b64_tr_b16 a[166:167], v6 offset:32320\n"
"v_mfma_f32_16x16x32_bf16 v[76:79], v[192:195], a[168:171], v[76:79]\n"
"v_accvgpr_write_b32 a17, v121\n"
"v_accvgpr_write_b32 a18, v122\n"
"v_mfma_f32_16x16x32_bf16 v[80:83], v[120:123], a[172:175], v[80:83]\n"
"ds_read_b64_tr_b16 a[168:169], v6 offset:27744\n"
"ds_read_b64_tr_b16 a[170:171], v6 offset:32352\n"
"v_mfma_f32_16x16x32_bf16 v[84:87], v[192:195], a[172:175], v[84:87]\n"
"buffer_load_dwordx4 v12, s[12:15], s61 offen offset:0 lds\n"
"s_waitcnt lgkmcnt(8)\n"
"v_mfma_f32_16x16x32_bf16 v[88:91], v[120:123], a[144:147], v[88:91]\n"
"ds_read_b64_tr_b16 a[172:173], v6 offset:27776\n"
"ds_read_b64_tr_b16 a[174:175], v6 offset:32384\n"
"v_mfma_f32_16x16x32_bf16 v[92:95], v[192:195], a[144:147], v[92:95]\n"
"v_accvgpr_write_b32 a19, v123\n"
"v_accvgpr_write_b32 a20, v192\n"
"v_mfma_f32_16x16x32_bf16 v[96:99], v[120:123], a[148:151], v[96:99]\n"
"ds_read_b64_tr_b16 a[144:145], v6 offset:27808\n"
"ds_read_b64_tr_b16 a[146:147], v6 offset:32416\n"
"v_mfma_f32_16x16x32_bf16 v[100:103], v[192:195], a[148:151], v[100:103]\n"
"v_accvgpr_write_b32 a21, v193\n"
"v_accvgpr_write_b32 a22, v194\n"
"v_mfma_f32_16x16x32_bf16 v[104:107], v[120:123], a[152:155], v[104:107]\n"
"ds_read_b64_tr_b16 a[148:149], v6 offset:27840\n"
"ds_read_b64_tr_b16 a[150:151], v6 offset:32448\n"
"v_mfma_f32_16x16x32_bf16 v[108:111], v[192:195], a[152:155], v[108:111]\n"
"buffer_load_dwordx4 v13, s[12:15], s61 offen offset:1024 lds\n"
"s_waitcnt lgkmcnt(8)\n"
"v_mfma_f32_16x16x32_bf16 v[40:43], v[124:127], a[156:159], v[40:43]\n"
"ds_read_b64_tr_b16 a[152:153], v6 offset:27872\n"
"ds_read_b64_tr_b16 a[154:155], v6 offset:32480\n"
"v_mfma_f32_16x16x32_bf16 v[44:47], v[196:199], a[156:159], v[44:47]\n"
"v_accvgpr_write_b32 a23, v195\n"
"v_accvgpr_write_b32 a24, v124\n"
"v_mfma_f32_16x16x32_bf16 v[48:51], v[124:127], a[160:163], v[48:51]\n"
"ds_read_b64_tr_b16 a[156:157], v6 offset:27904\n"
"ds_read_b64_tr_b16 a[158:159], v6 offset:32512\n"
"v_mfma_f32_16x16x32_bf16 v[52:55], v[196:199], a[160:163], v[52:55]\n"
"buffer_load_dwordx4 v14, s[12:15], s61 offen offset:2048 lds\n"
"v_accvgpr_write_b32 a25, v125\n"
"v_mfma_f32_16x16x32_bf16 v[56:59], v[124:127], a[164:167], v[56:59]\n"
"v_accvgpr_write_b32 a26, v126\n"
"v_accvgpr_write_b32 a27, v127\n"
"v_mfma_f32_16x16x32_bf16 v[60:63], v[196:199], a[164:167], v[60:63]\n"
"v_accvgpr_write_b32 a28, v196\n"
"s_waitcnt lgkmcnt(6)\n"
"v_mfma_f32_16x16x32_bf16 v[64:67], v[124:127], a[168:171], v[64:67]\n"
"v_accvgpr_write_b32 a29, v197\n"
"v_accvgpr_write_b32 a30, v198\n"
"v_mfma_f32_16x16x32_bf16 v[68:71], v[196:199], a[168:171], v[68:71]\n"
"s_waitcnt vmcnt(18)\n"
"s_waitcnt lgkmcnt(0)\n"
"s_barrier\n"
"v_mfma_f32_16x16x32_bf16 v[72:75], v[124:127], a[172:175], v[72:75]\n"
"ds_read_b64_tr_b16 a[160:161], v6 offset:36864\n"
"ds_read_b64_tr_b16 a[162:163], v6 offset:41472\n"
"v_mfma_f32_16x16x32_bf16 v[76:79], v[196:199], a[172:175], v[76:79]\n"
"ds_read_b64_tr_b16 a[164:165], v6 offset:36896\n"
"ds_read_b64_tr_b16 a[166:167], v6 offset:41504\n"
"v_mfma_f32_16x16x32_bf16 v[80:83], v[124:127], a[144:147], v[80:83]\n"
"ds_read_b64_tr_b16 a[168:169], v6 offset:36928\n"
"ds_read_b64_tr_b16 a[170:171], v6 offset:41536\n"
"v_mfma_f32_16x16x32_bf16 v[84:87], v[196:199], a[144:147], v[84:87]\n"
"ds_read_b64_tr_b16 a[172:173], v6 offset:36960\n"
"ds_read_b64_tr_b16 a[174:175], v6 offset:41568\n"
"v_mfma_f32_16x16x32_bf16 v[88:91], v[124:127], a[148:151], v[88:91]\n"
"ds_read_b64_tr_b16 a[144:145], v6 offset:36992\n"
"ds_read_b64_tr_b16 a[146:147], v6 offset:41600\n"
"v_mfma_f32_16x16x32_bf16 v[92:95], v[196:199], a[148:151], v[92:95]\n"
"buffer_load_dwordx4 v15, s[12:15], s61 offen offset:3072 lds\n"
"s_add_u32 m0, s60, 118784\n"
"v_mfma_f32_16x16x32_bf16 v[96:99], v[124:127], a[152:155], v[96:99]\n"
"ds_read_b64_tr_b16 a[148:149], v6 offset:37024\n"
"ds_read_b64_tr_b16 a[150:151], v6 offset:41632\n"
"v_mfma_f32_16x16x32_bf16 v[100:103], v[196:199], a[152:155], v[100:103]\n"
"buffer_load_dwordx4 v16, s[12:15], s61 offen offset:0 lds\n"
"s_add_u32 s61, s61, 1179648\n"
"v_mfma_f32_16x16x32_bf16 v[104:107], v[124:127], a[156:159], v[104:107]\n"
"ds_read_b64_tr_b16 a[152:153], v6 offset:37056\n"
"ds_read_b64_tr_b16 a[154:155], v6 offset:41664\n"
"v_mfma_f32_16x16x32_bf16 v[108:111], v[196:199], a[156:159], v[108:111]\n"
"s_sub_u32 s93, s61, 5313024\n"
"s_waitcnt lgkmcnt(8)\n"
"v_mfma_f32_16x16x32_bf16 v[40:43], v[128:131], a[160:163], v[40:43]\n"
"ds_read_b64_tr_b16 a[156:157], v6 offset:37088\n"
"ds_read_b64_tr_b16 a[158:159], v6 offset:41696\n"
"v_mfma_f32_16x16x32_bf16 v[44:47], v[200:203], a[160:163], v[44:47]\n"
"s_cmp_ge_u32 s54, 2\n"
"s_cselect_b32 s61, s93, s61\n"
"v_mfma_f32_16x16x32_bf16 v[48:51], v[128:131], a[164:167], v[48:51]\n"
"ds_read_b64_tr_b16 a[160:161], v6 offset:37120\n"
"ds_read_b64_tr_b16 a[162:163], v6 offset:41728\n"
"v_mfma_f32_16x16x32_bf16 v[52:55], v[200:203], a[164:167], v[52:55]\n"
"s_add_u32 m0, s60, 0\n"
"buffer_load_dword v112, v27, s[20:23], 0 offen offset:0\n"
"v_mfma_f32_16x16x32_bf16 v[56:59], v[128:131], a[168:171], v[56:59]\n"
"ds_read_b64_tr_b16 a[164:165], v6 offset:46080\n"
"ds_read_b64_tr_b16 a[166:167], v6 offset:50688\n"
"v_mfma_f32_16x16x32_bf16 v[60:63], v[200:203], a[168:171], v[60:63]\n"
"v_accvgpr_write_b32 a31, v199\n"
"s_waitcnt lgkmcnt(8)\n"
"v_mfma_f32_16x16x32_bf16 v[64:67], v[128:131], a[172:175], v[64:67]\n"
"ds_read_b64_tr_b16 a[168:169], v6 offset:46112\n"
"ds_read_b64_tr_b16 a[170:171], v6 offset:50720\n"
"v_mfma_f32_16x16x32_bf16 v[68:71], v[200:203], a[172:175], v[68:71]\n"
"buffer_load_dword v113, v27, s[20:23], 0 offen offset:3456\n"
"v_accvgpr_write_b32 a32, v128\n"
"v_mfma_f32_16x16x32_bf16 v[72:75], v[128:131], a[144:147], v[72:75]\n"
"ds_read_b64_tr_b16 a[172:173], v6 offset:46144\n"
"ds_read_b64_tr_b16 a[174:175], v6 offset:50752\n"
"v_mfma_f32_16x16x32_bf16 v[76:79], v[200:203], a[144:147], v[76:79]\n"
"v_accvgpr_write_b32 a33, v129\n"
"v_accvgpr_write_b32 a34, v130\n"
"v_mfma_f32_16x16x32_bf16 v[80:83], v[128:131], a[148:151], v[80:83]\n"
"ds_read_b64_tr_b16 a[144:145], v6 offset:46176\n"
"ds_read_b64_tr_b16 a[146:147], v6 offset:50784\n"
"v_mfma_f32_16x16x32_bf16 v[84:87], v[200:203], a[148:151], v[84:87]\n"
"buffer_load_dword v114, v27, s[20:23], s65 offen offset:0\n"
"s_waitcnt lgkmcnt(8)\n"
"v_mfma_f32_16x16x32_bf16 v[88:91], v[128:131], a[152:155], v[88:91]\n"
"ds_read_b64_tr_b16 a[148:149], v6 offset:46208\n"
"ds_read_b64_tr_b16 a[150:151], v6 offset:50816\n"
"v_mfma_f32_16x16x32_bf16 v[92:95], v[200:203], a[152:155], v[92:95]\n"
"v_accvgpr_write_b32 a35, v131\n"
"v_accvgpr_write_b32 a36, v200\n"
"v_mfma_f32_16x16x32_bf16 v[96:99], v[128:131], a[156:159], v[96:99]\n"
"ds_read_b64_tr_b16 a[152:153], v6 offset:46240\n"
"ds_read_b64_tr_b16 a[154:155], v6 offset:50848\n"
"v_mfma_f32_16x16x32_bf16 v[100:103], v[200:203], a[156:159], v[100:103]\n"
"v_accvgpr_write_b32 a37, v201\n"
"v_accvgpr_write_b32 a38, v202\n"
"v_mfma_f32_16x16x32_bf16 v[104:107], v[128:131], a[160:163], v[104:107]\n"
"ds_read_b64_tr_b16 a[156:157], v6 offset:46272\n"
"ds_read_b64_tr_b16 a[158:159], v6 offset:50880\n"
"v_mfma_f32_16x16x32_bf16 v[108:111], v[200:203], a[160:163], v[108:111]\n"
"buffer_load_dword v115, v27, s[20:23], s65 offen offset:3456\n"
"s_waitcnt lgkmcnt(8)\n"
"v_mfma_f32_16x16x32_bf16 v[40:43], v[132:135], a[164:167], v[40:43]\n"
"ds_read_b64_tr_b16 a[160:161], v6 offset:46304\n"
"ds_read_b64_tr_b16 a[162:163], v6 offset:50912\n"
"v_mfma_f32_16x16x32_bf16 v[44:47], v[204:207], a[164:167], v[44:47]\n"
"v_accvgpr_write_b32 a39, v203\n"
"v_accvgpr_write_b32 a40, v132\n"
"v_mfma_f32_16x16x32_bf16 v[48:51], v[132:135], a[168:171], v[48:51]\n"
"ds_read_b64_tr_b16 a[164:165], v6 offset:46336\n"
"ds_read_b64_tr_b16 a[166:167], v6 offset:50944\n"
"v_mfma_f32_16x16x32_bf16 v[52:55], v[204:207], a[168:171], v[52:55]\n"
"buffer_load_dword v116, v27, s[20:23], s66 offen offset:0\n"
"v_accvgpr_write_b32 a41, v133\n"
"v_mfma_f32_16x16x32_bf16 v[56:59], v[132:135], a[172:175], v[56:59]\n"
"ds_read_b64_tr_b16 a[168:169], v6 offset:55296\n"
"ds_read_b64_tr_b16 a[170:171], v6 offset:59904\n"
"v_mfma_f32_16x16x32_bf16 v[60:63], v[204:207], a[172:175], v[60:63]\n"
"v_accvgpr_write_b32 a42, v134\n"
"s_waitcnt lgkmcnt(8)\n"
"v_mfma_f32_16x16x32_bf16 v[64:67], v[132:135], a[144:147], v[64:67]\n"
"ds_read_b64_tr_b16 a[172:173], v6 offset:55328\n"
"ds_read_b64_tr_b16 a[174:175], v6 offset:59936\n"
"v_mfma_f32_16x16x32_bf16 v[68:71], v[204:207], a[144:147], v[68:71]\n"
"v_accvgpr_write_b32 a43, v135\n"
"v_accvgpr_write_b32 a44, v204\n"
"v_mfma_f32_16x16x32_bf16 v[72:75], v[132:135], a[148:151], v[72:75]\n"
"ds_read_b64_tr_b16 a[144:145], v6 offset:55360\n"
"ds_read_b64_tr_b16 a[146:147], v6 offset:59968\n"
"v_mfma_f32_16x16x32_bf16 v[76:79], v[204:207], a[148:151], v[76:79]\n"
"buffer_load_dword v117, v27, s[20:23], s66 offen offset:3456\n"
"v_accvgpr_write_b32 a45, v205\n"
"v_mfma_f32_16x16x32_bf16 v[80:83], v[132:135], a[152:155], v[80:83]\n"
"ds_read_b64_tr_b16 a[148:149], v6 offset:55392\n"
"ds_read_b64_tr_b16 a[150:151], v6 offset:60000\n"
"v_mfma_f32_16x16x32_bf16 v[84:87], v[204:207], a[152:155], v[84:87]\n"
"v_accvgpr_write_b32 a46, v206\n"
"s_waitcnt lgkmcnt(8)\n"
"v_mfma_f32_16x16x32_bf16 v[88:91], v[132:135], a[156:159], v[88:91]\n"
"ds_read_b64_tr_b16 a[152:153], v6 offset:55424\n"
"ds_read_b64_tr_b16 a[154:155], v6 offset:60032\n"
"v_mfma_f32_16x16x32_bf16 v[92:95], v[204:207], a[156:159], v[92:95]\n"
"buffer_load_dword v118, v27, s[20:23], s67 offen offset:0\n"
"v_accvgpr_write_b32 a47, v207\n"
"v_mfma_f32_16x16x32_bf16 v[96:99], v[132:135], a[160:163], v[96:99]\n"
"ds_read_b64_tr_b16 a[156:157], v6 offset:55456\n"
"ds_read_b64_tr_b16 a[158:159], v6 offset:60064\n"
"v_mfma_f32_16x16x32_bf16 v[100:103], v[204:207], a[160:163], v[100:103]\n"
"v_accvgpr_write_b32 a48, v136\n"
"v_accvgpr_write_b32 a49, v137\n"
"v_mfma_f32_16x16x32_bf16 v[104:107], v[132:135], a[164:167], v[104:107]\n"
"ds_read_b64_tr_b16 a[160:161], v6 offset:55488\n"
"ds_read_b64_tr_b16 a[162:163], v6 offset:60096\n"
"v_mfma_f32_16x16x32_bf16 v[108:111], v[204:207], a[164:167], v[108:111]\n"
"buffer_load_dword v119, v27, s[20:23], s67 offen offset:3456\n"
"s_waitcnt lgkmcnt(8)\n"
"v_mfma_f32_16x16x32_bf16 v[40:43], v[136:139], a[168:171], v[40:43]\n"
"ds_read_b64_tr_b16 a[164:165], v6 offset:55520\n"
"ds_read_b64_tr_b16 a[166:167], v6 offset:60128\n"
"v_mfma_f32_16x16x32_bf16 v[44:47], v[208:211], a[168:171], v[44:47]\n"
"buffer_load_dword v33, v25, s[20:23], 0 offen\n"
"s_lshl_b32 s64, s54, 10\n"
"v_mfma_f32_16x16x32_bf16 v[48:51], v[136:139], a[172:175], v[48:51]\n"
"ds_read_b64_tr_b16 a[168:169], v6 offset:55552\n"
"ds_read_b64_tr_b16 a[170:171], v6 offset:60160\n"
"v_mfma_f32_16x16x32_bf16 v[52:55], v[208:211], a[172:175], v[52:55]\n"
"s_add_u32 s64, s64, 147456\n"
"v_add_u32 v34, s64, v2\n"
"v_mfma_f32_16x16x32_bf16 v[56:59], v[136:139], a[144:147], v[56:59]\n"
"ds_read_b64_tr_b16 a[172:173], v6 offset:64512\n"
"ds_read_b64_tr_b16 a[174:175], v7 offset:3584\n"
"v_mfma_f32_16x16x32_bf16 v[60:63], v[208:211], a[144:147], v[60:63]\n"
"v_lshrrev_b32 v21, 4, v1\n"
"s_waitcnt lgkmcnt(8)\n"
"v_mfma_f32_16x16x32_bf16 v[64:67], v[136:139], a[148:151], v[64:67]\n"
"ds_read_b64_tr_b16 a[144:145], v6 offset:64544\n"
"ds_read_b64_tr_b16 a[146:147], v7 offset:3616\n"
"v_mfma_f32_16x16x32_bf16 v[68:71], v[208:211], a[148:151], v[68:71]\n"
"v_lshl_add_u32 v21, v21, 28, v28\n"
"v_accvgpr_write_b32 a50, v138\n"
"v_mfma_f32_16x16x32_bf16 v[72:75], v[136:139], a[152:155], v[72:75]\n"
"ds_read_b64_tr_b16 a[148:149], v6 offset:64576\n"
"ds_read_b64_tr_b16 a[150:151], v7 offset:3648\n"
"v_mfma_f32_16x16x32_bf16 v[76:79], v[208:211], a[152:155], v[76:79]\n"
"v_accvgpr_write_b32 a51, v139\n"
"v_accvgpr_write_b32 a52, v208\n"
"v_mfma_f32_16x16x32_bf16 v[80:83], v[136:139], a[156:159], v[80:83]\n"
"ds_read_b64_tr_b16 a[152:153], v6 offset:64608\n"
"ds_read_b64_tr_b16 a[154:155], v7 offset:3680\n"
"v_mfma_f32_16x16x32_bf16 v[84:87], v[208:211], a[156:159], v[84:87]\n"
"buffer_load_dwordx4 v8, s[12:15], s61 offen offset:0 lds\n"
"s_waitcnt lgkmcnt(8)\n"
"v_mfma_f32_16x16x32_bf16 v[88:91], v[136:139], a[160:163], v[88:91]\n"
"ds_read_b64_tr_b16 a[156:157], v6 offset:64640\n"
"ds_read_b64_tr_b16 a[158:159], v7 offset:3712\n"
"v_mfma_f32_16x16x32_bf16 v[92:95], v[208:211], a[160:163], v[92:95]\n"
"v_accvgpr_write_b32 a53, v209\n"
"v_accvgpr_write_b32 a54, v210\n"
"v_mfma_f32_16x16x32_bf16 v[96:99], v[136:139], a[164:167], v[96:99]\n"
"ds_read_b64_tr_b16 a[160:161], v6 offset:64672\n"
"ds_read_b64_tr_b16 a[162:163], v7 offset:3744\n"
"v_mfma_f32_16x16x32_bf16 v[100:103], v[208:211], a[164:167], v[100:103]\n"
"v_accvgpr_write_b32 a55, v211\n"
"v_accvgpr_write_b32 a56, v140\n"
"v_mfma_f32_16x16x32_bf16 v[104:107], v[136:139], a[168:171], v[104:107]\n"
"ds_read_b64_tr_b16 a[164:165], v6 offset:64704\n"
"ds_read_b64_tr_b16 a[166:167], v7 offset:3776\n"
"v_mfma_f32_16x16x32_bf16 v[108:111], v[208:211], a[168:171], v[108:111]\n"
"buffer_load_dwordx4 v9, s[12:15], s61 offen offset:1024 lds\n"
"s_waitcnt lgkmcnt(8)\n"
"v_mfma_f32_16x16x32_bf16 v[40:43], v[140:143], a[172:175], v[40:43]\n"
"ds_read_b64_tr_b16 a[168:169], v6 offset:64736\n"
"ds_read_b64_tr_b16 a[170:171], v7 offset:3808\n"
"v_mfma_f32_16x16x32_bf16 v[44:47], v[212:215], a[172:175], v[44:47]\n"
"v_accvgpr_write_b32 a57, v141\n"
"v_accvgpr_write_b32 a58, v142\n"
"v_mfma_f32_16x16x32_bf16 v[48:51], v[140:143], a[144:147], v[48:51]\n"
"ds_read_b64_tr_b16 a[172:173], v6 offset:64768\n"
"ds_read_b64_tr_b16 a[174:175], v7 offset:3840\n"
"v_mfma_f32_16x16x32_bf16 v[52:55], v[212:215], a[144:147], v[52:55]\n"
"v_accvgpr_write_b32 a59, v143\n"
"v_accvgpr_write_b32 a60, v212\n"
"v_mfma_f32_16x16x32_bf16 v[56:59], v[140:143], a[148:151], v[56:59]\n"
"buffer_load_dwordx4 v10, s[12:15], s61 offen offset:2048 lds\n"
"v_accvgpr_write_b32 a61, v213\n"
"v_mfma_f32_16x16x32_bf16 v[60:63], v[212:215], a[148:151], v[60:63]\n"
"v_accvgpr_write_b32 a62, v214\n"
"s_waitcnt lgkmcnt(6)\n"
"v_mfma_f32_16x16x32_bf16 v[64:67], v[140:143], a[152:155], v[64:67]\n"
"v_accvgpr_write_b32 a63, v215\n"
"v_accvgpr_write_b32 a64, v144\n"
"v_mfma_f32_16x16x32_bf16 v[68:71], v[212:215], a[152:155], v[68:71]\n"
"s_waitcnt vmcnt(21)\n"
"s_waitcnt lgkmcnt(0)\n"
"s_barrier\n"
"v_mfma_f32_16x16x32_bf16 v[72:75], v[140:143], a[156:159], v[72:75]\n"
"ds_read_b64_tr_b16 a[144:145], v7 offset:8192\n"
"ds_read_b64_tr_b16 a[146:147], v7 offset:12800\n"
"v_mfma_f32_16x16x32_bf16 v[76:79], v[212:215], a[156:159], v[76:79]\n"
"ds_read_b64_tr_b16 a[148:149], v7 offset:8224\n"
"ds_read_b64_tr_b16 a[150:151], v7 offset:12832\n"
"v_mfma_f32_16x16x32_bf16 v[80:83], v[140:143], a[160:163], v[80:83]\n"
"ds_read_b64_tr_b16 a[152:153], v7 offset:8256\n"
"ds_read_b64_tr_b16 a[154:155], v7 offset:12864\n"
"v_mfma_f32_16x16x32_bf16 v[84:87], v[212:215], a[160:163], v[84:87]\n"
"ds_read_b64_tr_b16 a[156:157], v7 offset:8288\n"
"ds_read_b64_tr_b16 a[158:159], v7 offset:12896\n"
"v_mfma_f32_16x16x32_bf16 v[88:91], v[140:143], a[164:167], v[88:91]\n"
"ds_read_b64_tr_b16 a[160:161], v7 offset:8320\n"
"ds_read_b64_tr_b16 a[162:163], v7 offset:12928\n"
"v_mfma_f32_16x16x32_bf16 v[92:95], v[212:215], a[164:167], v[92:95]\n"
"buffer_load_dwordx4 v11, s[12:15], s61 offen offset:3072 lds\n"
"s_add_u32 m0, s60, 4096\n"
"v_mfma_f32_16x16x32_bf16 v[96:99], v[140:143], a[168:171], v[96:99]\n"
"ds_read_b64_tr_b16 a[164:165], v7 offset:8352\n"
"ds_read_b64_tr_b16 a[166:167], v7 offset:12960\n"
"v_mfma_f32_16x16x32_bf16 v[100:103], v[212:215], a[168:171], v[100:103]\n"
"buffer_load_dwordx4 v12, s[12:15], s61 offen offset:0 lds\n"
"v_accvgpr_write_b32 a65, v145\n"
"v_mfma_f32_16x16x32_bf16 v[104:107], v[140:143], a[172:175], v[104:107]\n"
"ds_read_b64_tr_b16 a[168:169], v7 offset:8384\n"
"ds_read_b64_tr_b16 a[170:171], v7 offset:12992\n"
"v_mfma_f32_16x16x32_bf16 v[108:111], v[212:215], a[172:175], v[108:111]\n"
"v_accvgpr_write_b32 a66, v146\n"
"s_waitcnt lgkmcnt(8)\n"
"v_mfma_f32_16x16x32_bf16 v[40:43], v[144:147], a[144:147], v[40:43]\n"
"ds_read_b64_tr_b16 a[172:173], v7 offset:8416\n"
"ds_read_b64_tr_b16 a[174:175], v7 offset:13024\n"
"v_mfma_f32_16x16x32_bf16 v[44:47], v[216:219], a[144:147], v[44:47]\n"
"v_accvgpr_write_b32 a67, v147\n"
"v_accvgpr_write_b32 a68, v216\n"
"v_mfma_f32_16x16x32_bf16 v[48:51], v[144:147], a[148:151], v[48:51]\n"
"ds_read_b64_tr_b16 a[144:145], v7 offset:8448\n"
"ds_read_b64_tr_b16 a[146:147], v7 offset:13056\n"
"v_mfma_f32_16x16x32_bf16 v[52:55], v[216:219], a[148:151], v[52:55]\n"
"buffer_load_dwordx4 v13, s[12:15], s61 offen offset:1024 lds\n"
"v_accvgpr_write_b32 a69, v217\n"
"v_mfma_f32_16x16x32_bf16 v[56:59], v[144:147], a[152:155], v[56:59]\n"
"ds_read_b64_tr_b16 a[148:149], v7 offset:17408\n"
"ds_read_b64_tr_b16 a[150:151], v7 offset:22016\n"
"v_mfma_f32_16x16x32_bf16 v[60:63], v[216:219], a[152:155], v[60:63]\n"
"v_accvgpr_write_b32 a70, v218\n"
"s_waitcnt lgkmcnt(8)\n"
"v_mfma_f32_16x16x32_bf16 v[64:67], v[144:147], a[156:159], v[64:67]\n"
"ds_read_b64_tr_b16 a[152:153], v7 offset:17440\n"
"ds_read_b64_tr_b16 a[154:155], v7 offset:22048\n"
"v_mfma_f32_16x16x32_bf16 v[68:71], v[216:219], a[156:159], v[68:71]\n"
"v_accvgpr_write_b32 a71, v219\n"
"v_accvgpr_write_b32 a72, v148\n"
"v_mfma_f32_16x16x32_bf16 v[72:75], v[144:147], a[160:163], v[72:75]\n"
"ds_read_b64_tr_b16 a[156:157], v7 offset:17472\n"
"ds_read_b64_tr_b16 a[158:159], v7 offset:22080\n"
"v_mfma_f32_16x16x32_bf16 v[76:79], v[216:219], a[160:163], v[76:79]\n"
"buffer_load_dwordx4 v14, s[12:15], s61 offen offset:2048 lds\n"
"v_accvgpr_write_b32 a73, v149\n"
"v_mfma_f32_16x16x32_bf16 v[80:83], v[144:147], a[164:167], v[80:83]\n"
"ds_read_b64_tr_b16 a[160:161], v7 offset:17504\n"
"ds_read_b64_tr_b16 a[162:163], v7 offset:22112\n"
"v_mfma_f32_16x16x32_bf16 v[84:87], v[216:219], a[164:167], v[84:87]\n"
"v_accvgpr_write_b32 a74, v150\n"
"s_waitcnt lgkmcnt(8)\n"
"v_mfma_f32_16x16x32_bf16 v[88:91], v[144:147], a[168:171], v[88:91]\n"
"ds_read_b64_tr_b16 a[164:165], v7 offset:17536\n"
"ds_read_b64_tr_b16 a[166:167], v7 offset:22144\n"
"v_mfma_f32_16x16x32_bf16 v[92:95], v[216:219], a[168:171], v[92:95]\n"
"v_accvgpr_write_b32 a75, v151\n"
"v_accvgpr_write_b32 a76, v220\n"
"v_mfma_f32_16x16x32_bf16 v[96:99], v[144:147], a[172:175], v[96:99]\n"
"ds_read_b64_tr_b16 a[168:169], v7 offset:17568\n"
"ds_read_b64_tr_b16 a[170:171], v7 offset:22176\n"
"v_mfma_f32_16x16x32_bf16 v[100:103], v[216:219], a[172:175], v[100:103]\n"
"buffer_load_dwordx4 v15, s[12:15], s61 offen offset:3072 lds\n"
"s_add_u32 m0, s60, 8192\n"
"v_mfma_f32_16x16x32_bf16 v[104:107], v[144:147], a[144:147], v[104:107]\n"
"ds_read_b64_tr_b16 a[172:173], v7 offset:17600\n"
"ds_read_b64_tr_b16 a[174:175], v7 offset:22208\n"
"v_mfma_f32_16x16x32_bf16 v[108:111], v[216:219], a[144:147], v[108:111]\n"
"v_accvgpr_write_b32 a77, v221\n"
"s_waitcnt lgkmcnt(8)\n"
"v_mfma_f32_16x16x32_bf16 v[40:43], v[148:151], a[148:151], v[40:43]\n"
"ds_read_b64_tr_b16 a[144:145], v7 offset:17632\n"
"ds_read_b64_tr_b16 a[146:147], v7 offset:22240\n"
"v_mfma_f32_16x16x32_bf16 v[44:47], v[220:223], a[148:151], v[44:47]\n"
"v_accvgpr_write_b32 a78, v222\n"
"v_accvgpr_write_b32 a79, v223\n"
"v_mfma_f32_16x16x32_bf16 v[48:51], v[148:151], a[152:155], v[48:51]\n"
"ds_read_b64_tr_b16 a[148:149], v7 offset:17664\n"
"ds_read_b64_tr_b16 a[150:151], v7 offset:22272\n"
"v_mfma_f32_16x16x32_bf16 v[52:55], v[220:223], a[152:155], v[52:55]\n"
"buffer_load_dwordx4 v16, s[12:15], s61 offen offset:0 lds\n"
"s_add_u32 s61, s61, 1179648\n"
"v_mfma_f32_16x16x32_bf16 v[56:59], v[148:151], a[156:159], v[56:59]\n"
"ds_read_b64_tr_b16 a[152:153], v7 offset:26624\n"
"ds_read_b64_tr_b16 a[154:155], v7 offset:31232\n"
"v_mfma_f32_16x16x32_bf16 v[60:63], v[220:223], a[156:159], v[60:63]\n"
"s_sub_u32 s93, s61, 5313024\n"
"s_waitcnt lgkmcnt(8)\n"
"v_mfma_f32_16x16x32_bf16 v[64:67], v[148:151], a[160:163], v[64:67]\n"
"ds_read_b64_tr_b16 a[156:157], v7 offset:26656\n"
"ds_read_b64_tr_b16 a[158:159], v7 offset:31264\n"
"v_mfma_f32_16x16x32_bf16 v[68:71], v[220:223], a[160:163], v[68:71]\n"
"s_cmp_lt_u32 s54, 2\n"
"s_cselect_b32 s61, s93, s61\n"
"v_mfma_f32_16x16x32_bf16 v[72:75], v[148:151], a[164:167], v[72:75]\n"
"ds_read_b64_tr_b16 a[160:161], v7 offset:26688\n"
"ds_read_b64_tr_b16 a[162:163], v7 offset:31296\n"
"v_mfma_f32_16x16x32_bf16 v[76:79], v[220:223], a[164:167], v[76:79]\n"
"s_add_u32 m0, s60, 36864\n"
"buffer_load_dwordx4 a[176:179], v23, s[24:27], s98 offen offset:0\n"
"v_mfma_f32_16x16x32_bf16 v[80:83], v[148:151], a[168:171], v[80:83]\n"
"ds_read_b64_tr_b16 a[164:165], v7 offset:26720\n"
"ds_read_b64_tr_b16 a[166:167], v7 offset:31328\n"
"v_mfma_f32_16x16x32_bf16 v[84:87], v[220:223], a[168:171], v[84:87]\n"
"v_accvgpr_write_b32 a80, v152\n"
"s_waitcnt lgkmcnt(8)\n"
"v_mfma_f32_16x16x32_bf16 v[88:91], v[148:151], a[172:175], v[88:91]\n"
"ds_read_b64_tr_b16 a[168:169], v7 offset:26752\n"
"ds_read_b64_tr_b16 a[170:171], v7 offset:31360\n"
"v_mfma_f32_16x16x32_bf16 v[92:95], v[220:223], a[172:175], v[92:95]\n"
"v_accvgpr_write_b32 a81, v153\n"
"v_accvgpr_write_b32 a82, v154\n"
"v_mfma_f32_16x16x32_bf16 v[96:99], v[148:151], a[144:147], v[96:99]\n"
"ds_read_b64_tr_b16 a[172:173], v7 offset:26784\n"
"ds_read_b64_tr_b16 a[174:175], v7 offset:31392\n"
"v_mfma_f32_16x16x32_bf16 v[100:103], v[220:223], a[144:147], v[100:103]\n"
"v_accvgpr_write_b32 a83, v155\n"
"v_accvgpr_write_b32 a84, v224\n"
"v_mfma_f32_16x16x32_bf16 v[104:107], v[148:151], a[148:151], v[104:107]\n"
"ds_read_b64_tr_b16 a[144:145], v7 offset:26816\n"
"ds_read_b64_tr_b16 a[146:147], v7 offset:31424\n"
"v_mfma_f32_16x16x32_bf16 v[108:111], v[220:223], a[148:151], v[108:111]\n"
"v_accvgpr_write_b32 a85, v225\n"
"s_waitcnt lgkmcnt(8)\n"
"v_mfma_f32_16x16x32_bf16 v[40:43], v[152:155], a[152:155], v[40:43]\n"
"ds_read_b64_tr_b16 a[148:149], v7 offset:26848\n"
"ds_read_b64_tr_b16 a[150:151], v7 offset:31456\n"
"v_mfma_f32_16x16x32_bf16 v[44:47], v[224:227], a[152:155], v[44:47]\n"
"buffer_load_dwordx4 a[180:183], v23, s[24:27], s98 offen offset:128\n"
"s_add_u32 s98, s99, 65536\n"
"v_mfma_f32_16x16x32_bf16 v[48:51], v[152:155], a[156:159], v[48:51]\n"
"ds_read_b64_tr_b16 a[152:153], v7 offset:26880\n"
"ds_read_b64_tr_b16 a[154:155], v7 offset:31488\n"
"v_mfma_f32_16x16x32_bf16 v[52:55], v[224:227], a[156:159], v[52:55]\n"
"v_accvgpr_write_b32 a86, v226\n"
"v_accvgpr_write_b32 a87, v227\n"
"v_mfma_f32_16x16x32_bf16 v[56:59], v[152:155], a[160:163], v[56:59]\n"
"ds_read_b64_tr_b16 a[156:157], v7 offset:35840\n"
"ds_read_b64_tr_b16 a[158:159], v7 offset:40448\n"
"v_mfma_f32_16x16x32_bf16 v[60:63], v[224:227], a[160:163], v[60:63]\n"
"v_accvgpr_write_b32 a88, v156\n"
"s_waitcnt lgkmcnt(8)\n"
"v_mfma_f32_16x16x32_bf16 v[64:67], v[152:155], a[164:167], v[64:67]\n"
"ds_read_b64_tr_b16 a[160:161], v7 offset:35872\n"
"ds_read_b64_tr_b16 a[162:163], v7 offset:40480\n"
"v_mfma_f32_16x16x32_bf16 v[68:71], v[224:227], a[164:167], v[68:71]\n"
"v_accvgpr_write_b32 a89, v157\n"
"v_accvgpr_write_b32 a90, v158\n"
"v_mfma_f32_16x16x32_bf16 v[72:75], v[152:155], a[168:171], v[72:75]\n"
"ds_read_b64_tr_b16 a[164:165], v7 offset:35904\n"
"ds_read_b64_tr_b16 a[166:167], v7 offset:40512\n"
"v_mfma_f32_16x16x32_bf16 v[76:79], v[224:227], a[168:171], v[76:79]\n"
"v_accvgpr_write_b32 a91, v159\n"
"v_accvgpr_write_b32 a92, v228\n"
"v_mfma_f32_16x16x32_bf16 v[80:83], v[152:155], a[172:175], v[80:83]\n"
"ds_read_b64_tr_b16 a[168:169], v7 offset:35936\n"
"ds_read_b64_tr_b16 a[170:171], v7 offset:40544\n"
"v_mfma_f32_16x16x32_bf16 v[84:87], v[224:227], a[172:175], v[84:87]\n"
"buffer_load_dwordx4 a[184:187], v23, s[24:27], s98 offen offset:0\n"
"s_waitcnt lgkmcnt(8)\n"
"v_mfma_f32_16x16x32_bf16 v[88:91], v[152:155], a[144:147], v[88:91]\n"
"ds_read_b64_tr_b16 a[172:173], v7 offset:35968\n"
"ds_read_b64_tr_b16 a[174:175], v7 offset:40576\n"
"v_mfma_f32_16x16x32_bf16 v[92:95], v[224:227], a[144:147], v[92:95]\n"
"v_accvgpr_write_b32 a93, v229\n"
"v_accvgpr_write_b32 a94, v230\n"
"v_mfma_f32_16x16x32_bf16 v[96:99], v[152:155], a[148:151], v[96:99]\n"
"ds_read_b64_tr_b16 a[144:145], v7 offset:36000\n"
"ds_read_b64_tr_b16 a[146:147], v7 offset:40608\n"
"v_mfma_f32_16x16x32_bf16 v[100:103], v[224:227], a[148:151], v[100:103]\n"
"v_accvgpr_write_b32 a95, v231\n"
"v_accvgpr_write_b32 a96, v160\n"
"v_mfma_f32_16x16x32_bf16 v[104:107], v[152:155], a[152:155], v[104:107]\n"
"ds_read_b64_tr_b16 a[148:149], v7 offset:36032\n"
"ds_read_b64_tr_b16 a[150:151], v7 offset:40640\n"
"v_mfma_f32_16x16x32_bf16 v[108:111], v[224:227], a[152:155], v[108:111]\n"
"v_accvgpr_write_b32 a97, v161\n"
"s_waitcnt lgkmcnt(8)\n"
"v_mfma_f32_16x16x32_bf16 v[40:43], v[156:159], a[156:159], v[40:43]\n"
"ds_read_b64_tr_b16 a[152:153], v7 offset:36064\n"
"ds_read_b64_tr_b16 a[154:155], v7 offset:40672\n"
"v_mfma_f32_16x16x32_bf16 v[44:47], v[228:231], a[156:159], v[44:47]\n"
"v_accvgpr_write_b32 a98, v162\n"
"v_accvgpr_write_b32 a99, v163\n"
"v_mfma_f32_16x16x32_bf16 v[48:51], v[156:159], a[160:163], v[48:51]\n"
"ds_read_b64_tr_b16 a[156:157], v7 offset:36096\n"
"ds_read_b64_tr_b16 a[158:159], v7 offset:40704\n"
"v_mfma_f32_16x16x32_bf16 v[52:55], v[228:231], a[160:163], v[52:55]\n"
"v_accvgpr_write_b32 a100, v232\n"
"v_accvgpr_write_b32 a101, v233\n"
"v_mfma_f32_16x16x32_bf16 v[56:59], v[156:159], a[164:167], v[56:59]\n"
"buffer_load_dwordx4 a[188:191], v23, s[24:27], s98 offen offset:128\n"
"s_add_u32 s98, s99, 16384\n"
"v_mfma_f32_16x16x32_bf16 v[60:63], v[228:231], a[164:167], v[60:63]\n"
"v_accvgpr_write_b32 a102, v234\n"
"s_waitcnt lgkmcnt(6)\n"
"v_mfma_f32_16x16x32_bf16 v[64:67], v[156:159], a[168:171], v[64:67]\n"
"v_accvgpr_write_b32 a103, v235\n"
"v_accvgpr_write_b32 a104, v164\n"
"v_mfma_f32_16x16x32_bf16 v[68:71], v[228:231], a[168:171], v[68:71]\n"
"s_waitcnt vmcnt(22)\n"
"s_waitcnt lgkmcnt(0)\n"
"s_barrier\n"
"v_mfma_f32_16x16x32_bf16 v[72:75], v[156:159], a[172:175], v[72:75]\n"
"ds_read_b64_tr_b16 a[160:161], v7 offset:45056\n"
"ds_read_b64_tr_b16 a[162:163], v7 offset:49664\n"
"v_mfma_f32_16x16x32_bf16 v[76:79], v[228:231], a[172:175], v[76:79]\n"
"ds_read_b64_tr_b16 a[164:165], v7 offset:45088\n"
"ds_read_b64_tr_b16 a[166:167], v7 offset:49696\n"
"v_mfma_f32_16x16x32_bf16 v[80:83], v[156:159], a[144:147], v[80:83]\n"
"ds_read_b64_tr_b16 a[168:169], v7 offset:45120\n"
"ds_read_b64_tr_b16 a[170:171], v7 offset:49728\n"
"v_mfma_f32_16x16x32_bf16 v[84:87], v[228:231], a[144:147], v[84:87]\n"
"ds_read_b64_tr_b16 a[172:173], v7 offset:45152\n"
"ds_read_b64_tr_b16 a[174:175], v7 offset:49760\n"
"v_mfma_f32_16x16x32_bf16 v[88:91], v[156:159], a[148:151], v[88:91]\n"
"ds_read_b64_tr_b16 a[144:145], v7 offset:45184\n"
"ds_read_b64_tr_b16 a[146:147], v7 offset:49792\n"
"v_mfma_f32_16x16x32_bf16 v[92:95], v[228:231], a[148:151], v[92:95]\n"
"buffer_load_dwordx4 a[192:195], v23, s[24:27], s98 offen offset:0\n"
"s_waitcnt vmcnt(14)\n"
"ds_write_b16 v26, v33\n"
"v_mfma_f32_16x16x32_bf16 v[96:99], v[156:159], a[152:155], v[96:99]\n"
"ds_read_b64_tr_b16 a[148:149], v7 offset:45216\n"
"ds_read_b64_tr_b16 a[150:151], v7 offset:49824\n"
"v_mfma_f32_16x16x32_bf16 v[100:103], v[228:231], a[152:155], v[100:103]\n"
"ds_write_b16_d16_hi v26, v33 offset:16\n"
"ds_read_b128 a[208:211], v34\n"
"v_mfma_f32_16x16x32_bf16 v[104:107], v[156:159], a[156:159], v[104:107]\n"
"ds_read_b64_tr_b16 a[152:153], v7 offset:45248\n"
"ds_read_b64_tr_b16 a[154:155], v7 offset:49856\n"
"v_mfma_f32_16x16x32_bf16 v[108:111], v[228:231], a[156:159], v[108:111]\n"
"buffer_load_dwordx4 a[196:199], v23, s[24:27], s98 offen offset:128\n"
"s_waitcnt lgkmcnt(11)\n"
"v_mfma_f32_16x16x32_bf16 v[40:43], v[160:163], a[160:163], v[40:43]\n"
"ds_read_b64_tr_b16 a[156:157], v7 offset:45280\n"
"ds_read_b64_tr_b16 a[158:159], v7 offset:49888\n"
"v_mfma_f32_16x16x32_bf16 v[44:47], v[232:235], a[160:163], v[44:47]\n"
"s_add_u32 s98, s99, 81920\n"
"v_perm_b32 v184, v113, v112, s78\n"
"v_mfma_f32_16x16x32_bf16 v[48:51], v[160:163], a[164:167], v[48:51]\n"
"ds_read_b64_tr_b16 a[160:161], v7 offset:45312\n"
"ds_read_b64_tr_b16 a[162:163], v7 offset:49920\n"
"v_mfma_f32_16x16x32_bf16 v[52:55], v[232:235], a[164:167], v[52:55]\n"
"buffer_load_dwordx4 a[200:203], v23, s[24:27], s98 offen offset:0\n"
"v_perm_b32 v188, v113, v112, s79\n"
"v_mfma_f32_16x16x32_bf16 v[56:59], v[160:163], a[168:171], v[56:59]\n"
"ds_read_b64_tr_b16 a[164:165], v7 offset:54272\n"
"ds_read_b64_tr_b16 a[166:167], v7 offset:58880\n"
"v_mfma_f32_16x16x32_bf16 v[60:63], v[232:235], a[168:171], v[60:63]\n"
"v_perm_b32 v185, v115, v114, s78\n"
"s_waitcnt lgkmcnt(10)\n"
"v_mfma_f32_16x16x32_bf16 v[64:67], v[160:163], a[172:175], v[64:67]\n"
"ds_read_b64_tr_b16 a[168:169], v7 offset:54304\n"
"ds_read_b64_tr_b16 a[170:171], v7 offset:58912\n"
"v_mfma_f32_16x16x32_bf16 v[68:71], v[232:235], a[172:175], v[68:71]\n"
"buffer_load_dwordx4 a[204:207], v23, s[24:27], s98 offen offset:128\n"
"v_perm_b32 v189, v115, v114, s79\n"
"v_mfma_f32_16x16x32_bf16 v[72:75], v[160:163], a[144:147], v[72:75]\n"
"ds_read_b64_tr_b16 a[172:173], v7 offset:54336\n"
"ds_read_b64_tr_b16 a[174:175], v7 offset:58944\n"
"v_mfma_f32_16x16x32_bf16 v[76:79], v[232:235], a[144:147], v[76:79]\n"
"v_perm_b32 v186, v117, v116, s78\n"
"v_perm_b32 v190, v117, v116, s79\n"
"v_mfma_f32_16x16x32_bf16 v[80:83], v[160:163], a[148:151], v[80:83]\n"
"ds_read_b64_tr_b16 a[144:145], v7 offset:54368\n"
"ds_read_b64_tr_b16 a[146:147], v7 offset:58976\n"
"v_mfma_f32_16x16x32_bf16 v[84:87], v[232:235], a[148:151], v[84:87]\n"
"v_perm_b32 v187, v119, v118, s78\n"
"s_waitcnt lgkmcnt(8)\n"
"v_mfma_f32_16x16x32_bf16 v[88:91], v[160:163], a[152:155], v[88:91]\n"
"ds_read_b64_tr_b16 a[148:149], v7 offset:54400\n"
"ds_read_b64_tr_b16 a[150:151], v7 offset:59008\n"
"v_mfma_f32_16x16x32_bf16 v[92:95], v[232:235], a[152:155], v[92:95]\n"
"v_perm_b32 v191, v119, v118, s79\n"
"v_accvgpr_write_b32 a105, v165\n"
"v_mfma_f32_16x16x32_bf16 v[96:99], v[160:163], a[156:159], v[96:99]\n"
"ds_read_b64_tr_b16 a[152:153], v7 offset:54432\n"
"ds_read_b64_tr_b16 a[154:155], v7 offset:59040\n"
"v_mfma_f32_16x16x32_bf16 v[100:103], v[232:235], a[156:159], v[100:103]\n"
"buffer_store_dwordx4 v[184:187], v28, s[16:19], 0 offen sc1\n"
"v_accvgpr_write_b32 a106, v166\n"
"v_mfma_f32_16x16x32_bf16 v[104:107], v[160:163], a[160:163], v[104:107]\n"
"ds_read_b64_tr_b16 a[156:157], v7 offset:54464\n"
"ds_read_b64_tr_b16 a[158:159], v7 offset:59072\n"
"v_mfma_f32_16x16x32_bf16 v[108:111], v[232:235], a[160:163], v[108:111]\n"
"v_accvgpr_write_b32 a107, v167\n"
"s_waitcnt lgkmcnt(8)\n"
"v_mfma_f32_16x16x32_bf16 v[40:43], v[164:167], a[164:167], v[40:43]\n"
"ds_read_b64_tr_b16 a[160:161], v7 offset:54496\n"
"ds_read_b64_tr_b16 a[162:163], v7 offset:59104\n"
"v_mfma_f32_16x16x32_bf16 v[44:47], v[236:239], a[164:167], v[44:47]\n"
"v_accvgpr_write_b32 a108, v236\n"
"v_accvgpr_write_b32 a109, v237\n"
"v_mfma_f32_16x16x32_bf16 v[48:51], v[164:167], a[168:171], v[48:51]\n"
"ds_read_b64_tr_b16 a[164:165], v7 offset:54528\n"
"ds_read_b64_tr_b16 a[166:167], v7 offset:59136\n"
"v_mfma_f32_16x16x32_bf16 v[52:55], v[236:239], a[168:171], v[52:55]\n"
"v_accvgpr_write_b32 a110, v238\n"
"v_accvgpr_write_b32 a111, v239\n"
"v_mfma_f32_16x16x32_bf16 v[56:59], v[164:167], a[172:175], v[56:59]\n"
"ds_read_b64_tr_b16 a[168:169], v7 offset:63488\n"
"ds_read_b64_tr_b16 a[170:171], v22 offset:2560\n"
"v_mfma_f32_16x16x32_bf16 v[60:63], v[236:239], a[172:175], v[60:63]\n"
"v_accvgpr_write_b32 a112, v168\n"
"s_waitcnt lgkmcnt(8)\n"
"v_mfma_f32_16x16x32_bf16 v[64:67], v[164:167], a[144:147], v[64:67]\n"
"ds_read_b64_tr_b16 a[172:173], v7 offset:63520\n"
"ds_read_b64_tr_b16 a[174:175], v22 offset:2592\n"
"v_mfma_f32_16x16x32_bf16 v[68:71], v[236:239], a[144:147], v[68:71]\n"
"buffer_store_dwordx4 v[188:191], v28, s[16:19], 0 offen offset:1024 sc1\n"
"v_accvgpr_write_b32 a113, v169\n"
"v_mfma_f32_16x16x32_bf16 v[72:75], v[164:167], a[148:151], v[72:75]\n"
"ds_read_b64_tr_b16 a[144:145], v7 offset:63552\n"
"ds_read_b64_tr_b16 a[146:147], v22 offset:2624\n"
"v_mfma_f32_16x16x32_bf16 v[76:79], v[236:239], a[148:151], v[76:79]\n"
"v_accvgpr_write_b32 a114, v170\n"
"v_accvgpr_write_b32 a115, v171\n"
"v_mfma_f32_16x16x32_bf16 v[80:83], v[164:167], a[152:155], v[80:83]\n"
"ds_read_b64_tr_b16 a[148:149], v7 offset:63584\n"
"ds_read_b64_tr_b16 a[150:151], v22 offset:2656\n"
"v_mfma_f32_16x16x32_bf16 v[84:87], v[236:239], a[152:155], v[84:87]\n"
"v_accvgpr_write_b32 a116, v240\n"
"s_waitcnt lgkmcnt(8)\n"
"v_mfma_f32_16x16x32_bf16 v[88:91], v[164:167], a[156:159], v[88:91]\n"
"ds_read_b64_tr_b16 a[152:153], v7 offset:63616\n"
"ds_read_b64_tr_b16 a[154:155], v22 offset:2688\n"
"v_mfma_f32_16x16x32_bf16 v[92:95], v[236:239], a[156:159], v[92:95]\n"
"v_accvgpr_write_b32 a117, v241\n"
"v_accvgpr_write_b32 a118, v242\n"
"v_mfma_f32_16x16x32_bf16 v[96:99], v[164:167], a[160:163], v[96:99]\n"
"ds_read_b64_tr_b16 a[156:157], v7 offset:63648\n"
"ds_read_b64_tr_b16 a[158:159], v22 offset:2720\n"
"v_mfma_f32_16x16x32_bf16 v[100:103], v[236:239], a[160:163], v[100:103]\n"
"v_accvgpr_write_b32 a119, v243\n"
"v_accvgpr_write_b32 a120, v172\n"
"v_mfma_f32_16x16x32_bf16 v[104:107], v[164:167], a[164:167], v[104:107]\n"
"ds_read_b64_tr_b16 a[160:161], v7 offset:63680\n"
"ds_read_b64_tr_b16 a[162:163], v22 offset:2752\n"
"v_mfma_f32_16x16x32_bf16 v[108:111], v[236:239], a[164:167], v[108:111]\n"
"buffer_store_dwordx4 a[208:211], v21, s[16:19], s88 offen sc1\n"
"s_waitcnt lgkmcnt(8)\n"
"v_mfma_f32_16x16x32_bf16 v[40:43], v[168:171], a[168:171], v[40:43]\n"
"ds_read_b64_tr_b16 a[164:165], v7 offset:63712\n"
"ds_read_b64_tr_b16 a[166:167], v22 offset:2784\n"
"v_mfma_f32_16x16x32_bf16 v[44:47], v[240:243], a[168:171], v[44:47]\n"
"v_accvgpr_write_b32 a121, v173\n"
"v_accvgpr_write_b32 a122, v174\n"
"v_mfma_f32_16x16x32_bf16 v[48:51], v[168:171], a[172:175], v[48:51]\n"
"ds_read_b64_tr_b16 a[168:169], v7 offset:63744\n"
"ds_read_b64_tr_b16 a[170:171], v22 offset:2816\n"
"v_mfma_f32_16x16x32_bf16 v[52:55], v[240:243], a[172:175], v[52:55]\n"
"buffer_load_dwordx4 v8, s[12:15], s61 offen offset:0 lds\n"
"v_accvgpr_write_b32 a123, v175\n"
"v_mfma_f32_16x16x32_bf16 v[56:59], v[168:171], a[144:147], v[56:59]\n"
"ds_read_b64_tr_b16 a[172:173], v22 offset:7168\n"
"ds_read_b64_tr_b16 a[174:175], v22 offset:11776\n"
"v_mfma_f32_16x16x32_bf16 v[60:63], v[240:243], a[144:147], v[60:63]\n"
"v_accvgpr_write_b32 a124, v244\n"
"s_waitcnt lgkmcnt(8)\n"
"v_mfma_f32_16x16x32_bf16 v[64:67], v[168:171], a[148:151], v[64:67]\n"
"ds_read_b64_tr_b16 a[144:145], v22 offset:7200\n"
"ds_read_b64_tr_b16 a[146:147], v22 offset:11808\n"
"v_mfma_f32_16x16x32_bf16 v[68:71], v[240:243], a[148:151], v[68:71]\n"
"buffer_load_dwordx4 v9, s[12:15], s61 offen offset:1024 lds\n"
"v_accvgpr_write_b32 a125, v245\n"
"v_mfma_f32_16x16x32_bf16 v[72:75], v[168:171], a[152:155], v[72:75]\n"
"ds_read_b64_tr_b16 a[148:149], v22 offset:7232\n"
"ds_read_b64_tr_b16 a[150:151], v22 offset:11840\n"
"v_mfma_f32_16x16x32_bf16 v[76:79], v[240:243], a[152:155], v[76:79]\n"
"v_accvgpr_write_b32 a126, v246\n"
"v_accvgpr_write_b32 a127, v247\n"
"v_mfma_f32_16x16x32_bf16 v[80:83], v[168:171], a[156:159], v[80:83]\n"
"ds_read_b64_tr_b16 a[152:153], v22 offset:7264\n"
"ds_read_b64_tr_b16 a[154:155], v22 offset:11872\n"
"v_mfma_f32_16x16x32_bf16 v[84:87], v[240:243], a[156:159], v[84:87]\n"
"buffer_load_dwordx4 v10, s[12:15], s61 offen offset:2048 lds\n"
"s_waitcnt lgkmcnt(8)\n"
"v_mfma_f32_16x16x32_bf16 v[88:91], v[168:171], a[160:163], v[88:91]\n"
"ds_read_b64_tr_b16 a[156:157], v22 offset:7296\n"
"ds_read_b64_tr_b16 a[158:159], v22 offset:11904\n"
"v_mfma_f32_16x16x32_bf16 v[92:95], v[240:243], a[160:163], v[92:95]\n"
"v_accvgpr_write_b32 a128, v176\n"
"v_accvgpr_write_b32 a129, v177\n"
"v_mfma_f32_16x16x32_bf16 v[96:99], v[168:171], a[164:167], v[96:99]\n"
"ds_read_b64_tr_b16 a[160:161], v22 offset:7328\n"
"ds_read_b64_tr_b16 a[162:163], v22 offset:11936\n"
"v_mfma_f32_16x16x32_bf16 v[100:103], v[240:243], a[164:167], v[100:103]\n"
"buffer_load_dwordx4 v11, s[12:15], s61 offen offset:3072 lds\n"
"s_add_u32 m0, s60, 40960\n"
"v_mfma_f32_16x16x32_bf16 v[104:107], v[168:171], a[168:171], v[104:107]\n"
"ds_read_b64_tr_b16 a[164:165], v22 offset:7360\n"
"ds_read_b64_tr_b16 a[166:167], v22 offset:11968\n"
"v_mfma_f32_16x16x32_bf16 v[108:111], v[240:243], a[168:171], v[108:111]\n"
"v_accvgpr_write_b32 a130, v178\n"
"s_waitcnt lgkmcnt(8)\n"
"v_mfma_f32_16x16x32_bf16 v[40:43], v[172:175], a[172:175], v[40:43]\n"
"ds_read_b64_tr_b16 a[168:169], v22 offset:7392\n"
"ds_read_b64_tr_b16 a[170:171], v22 offset:12000\n"
"v_mfma_f32_16x16x32_bf16 v[44:47], v[244:247], a[172:175], v[44:47]\n"
"buffer_load_dwordx4 v12, s[12:15], s61 offen offset:0 lds\n"
"v_accvgpr_write_b32 a131, v179\n"
"v_mfma_f32_16x16x32_bf16 v[48:51], v[172:175], a[144:147], v[48:51]\n"
"ds_read_b64_tr_b16 a[172:173], v22 offset:7424\n"
"ds_read_b64_tr_b16 a[174:175], v22 offset:12032\n"
"v_mfma_f32_16x16x32_bf16 v[52:55], v[244:247], a[144:147], v[52:55]\n"
"v_accvgpr_write_b32 a132, v248\n"
"v_accvgpr_write_b32 a133, v249\n"
"v_mfma_f32_16x16x32_bf16 v[56:59], v[172:175], a[148:151], v[56:59]\n"
"buffer_load_dwordx4 v13, s[12:15], s61 offen offset:1024 lds\n"
"v_accvgpr_write_b32 a134, v250\n"
"v_mfma_f32_16x16x32_bf16 v[60:63], v[244:247], a[148:151], v[60:63]\n"
"v_accvgpr_write_b32 a135, v251\n"
"s_waitcnt lgkmcnt(6)\n"
"v_mfma_f32_16x16x32_bf16 v[64:67], v[172:175], a[152:155], v[64:67]\n"
"v_accvgpr_write_b32 a136, v180\n"
"v_accvgpr_write_b32 a137, v181\n"
"v_mfma_f32_16x16x32_bf16 v[68:71], v[244:247], a[152:155], v[68:71]\n"
"s_waitcnt vmcnt(17)\n"
"s_waitcnt lgkmcnt(0)\n"
"s_barrier\n"
"v_mfma_f32_16x16x32_bf16 v[72:75], v[172:175], a[156:159], v[72:75]\n"
"ds_read_b64_tr_b16 a[144:145], v6 offset:0\n"
"ds_read_b64_tr_b16 a[146:147], v6 offset:4608\n"
"v_mfma_f32_16x16x32_bf16 v[76:79], v[244:247], a[156:159], v[76:79]\n"
"ds_read_b64_tr_b16 a[148:149], v6 offset:32\n"
"ds_read_b64_tr_b16 a[150:151], v6 offset:4640\n"
"v_mfma_f32_16x16x32_bf16 v[80:83], v[172:175], a[160:163], v[80:83]\n"
"ds_read_b64_tr_b16 a[152:153], v6 offset:64\n"
"ds_read_b64_tr_b16 a[154:155], v6 offset:4672\n"
"v_mfma_f32_16x16x32_bf16 v[84:87], v[244:247], a[160:163], v[84:87]\n"
"ds_read_b64_tr_b16 a[156:157], v6 offset:96\n"
"ds_read_b64_tr_b16 a[158:159], v6 offset:4704\n"
"v_mfma_f32_16x16x32_bf16 v[88:91], v[172:175], a[164:167], v[88:91]\n"
"ds_read_b64_tr_b16 a[160:161], v6 offset:128\n"
"ds_read_b64_tr_b16 a[162:163], v6 offset:4736\n"
"v_mfma_f32_16x16x32_bf16 v[92:95], v[244:247], a[164:167], v[92:95]\n"
"buffer_load_dwordx4 v14, s[12:15], s61 offen offset:2048 lds\n"
"v_accvgpr_write_b32 a138, v182\n"
"v_mfma_f32_16x16x32_bf16 v[96:99], v[172:175], a[168:171], v[96:99]\n"
"ds_read_b64_tr_b16 a[164:165], v6 offset:160\n"
"ds_read_b64_tr_b16 a[166:167], v6 offset:4768\n"
"v_mfma_f32_16x16x32_bf16 v[100:103], v[244:247], a[168:171], v[100:103]\n"
"v_accvgpr_write_b32 a139, v183\n"
"v_accvgpr_write_b32 a140, v252\n"
"v_mfma_f32_16x16x32_bf16 v[104:107], v[172:175], a[172:175], v[104:107]\n"
"ds_read_b64_tr_b16 a[168:169], v6 offset:192\n"
"ds_read_b64_tr_b16 a[170:171], v6 offset:4800\n"
"v_mfma_f32_16x16x32_bf16 v[108:111], v[244:247], a[172:175], v[108:111]\n"
"buffer_load_dwordx4 v15, s[12:15], s61 offen offset:3072 lds\n"
"s_waitcnt lgkmcnt(8)\n"
"v_mfma_f32_16x16x32_bf16 v[40:43], v[176:179], a[144:147], v[40:43]\n"
"ds_read_b64_tr_b16 a[172:173], v6 offset:224\n"
"ds_read_b64_tr_b16 a[174:175], v6 offset:4832\n"
"v_mfma_f32_16x16x32_bf16 v[44:47], v[248:251], a[144:147], v[44:47]\n"
"s_add_u32 m0, s60, 45056\n"
"v_accvgpr_write_b32 a141, v253\n"
"v_mfma_f32_16x16x32_bf16 v[48:51], v[176:179], a[148:151], v[48:51]\n"
"ds_read_b64_tr_b16 a[144:145], v6 offset:256\n"
"ds_read_b64_tr_b16 a[146:147], v6 offset:4864\n"
"v_mfma_f32_16x16x32_bf16 v[52:55], v[248:251], a[148:151], v[52:55]\n"
"buffer_load_dwordx4 v16, s[12:15], s61 offen offset:0 lds\n"
"s_add_u32 s61, s61, 1179648\n"
"v_mfma_f32_16x16x32_bf16 v[56:59], v[176:179], a[152:155], v[56:59]\n"
"ds_read_b64_tr_b16 a[148:149], v6 offset:9216\n"
"ds_read_b64_tr_b16 a[150:151], v6 offset:13824\n"
"v_mfma_f32_16x16x32_bf16 v[60:63], v[248:251], a[152:155], v[60:63]\n"
"s_add_u32 m0, s60, 73728\n"
"s_waitcnt lgkmcnt(8)\n"
"v_mfma_f32_16x16x32_bf16 v[64:67], v[176:179], a[156:159], v[64:67]\n"
"ds_read_b64_tr_b16 a[152:153], v6 offset:9248\n"
"ds_read_b64_tr_b16 a[154:155], v6 offset:13856\n"
"v_mfma_f32_16x16x32_bf16 v[68:71], v[248:251], a[156:159], v[68:71]\n"
"v_accvgpr_write_b32 a142, v254\n"
"v_accvgpr_write_b32 a143, v255\n"
"v_mfma_f32_16x16x32_bf16 v[72:75], v[176:179], a[160:163], v[72:75]\n"
"ds_read_b64_tr_b16 a[156:157], v6 offset:9280\n"
"ds_read_b64_tr_b16 a[158:159], v6 offset:13888\n"
"v_mfma_f32_16x16x32_bf16 v[76:79], v[248:251], a[160:163], v[76:79]\n"
"v_mfma_f32_16x16x32_bf16 v[80:83], v[176:179], a[164:167], v[80:83]\n"
"ds_read_b64_tr_b16 a[160:161], v6 offset:9312\n"
"ds_read_b64_tr_b16 a[162:163], v6 offset:13920\n"
"v_mfma_f32_16x16x32_bf16 v[84:87], v[248:251], a[164:167], v[84:87]\n"
"s_waitcnt lgkmcnt(8)\n"
"v_mfma_f32_16x16x32_bf16 v[88:91], v[176:179], a[168:171], v[88:91]\n"
"ds_read_b64_tr_b16 a[164:165], v6 offset:9344\n"
"ds_read_b64_tr_b16 a[166:167], v6 offset:13952\n"
"v_mfma_f32_16x16x32_bf16 v[92:95], v[248:251], a[168:171], v[92:95]\n"
"v_mfma_f32_16x16x32_bf16 v[96:99], v[176:179], a[172:175], v[96:99]\n"
"ds_read_b64_tr_b16 a[168:169], v6 offset:9376\n"
"ds_read_b64_tr_b16 a[170:171], v6 offset:13984\n"
"v_mfma_f32_16x16x32_bf16 v[100:103], v[248:251], a[172:175], v[100:103]\n"
"v_mfma_f32_16x16x32_bf16 v[104:107], v[176:179], a[144:147], v[104:107]\n"
"ds_read_b64_tr_b16 a[172:173], v6 offset:9408\n"
"ds_read_b64_tr_b16 a[174:175], v6 offset:14016\n"
"v_mfma_f32_16x16x32_bf16 v[108:111], v[248:251], a[144:147], v[108:111]\n"
"s_waitcnt lgkmcnt(8)\n"
"v_mfma_f32_16x16x32_bf16 v[40:43], v[180:183], a[148:151], v[40:43]\n"
"ds_read_b64_tr_b16 a[144:145], v6 offset:9440\n"
"ds_read_b64_tr_b16 a[146:147], v6 offset:14048\n"
"v_mfma_f32_16x16x32_bf16 v[44:47], v[252:255], a[148:151], v[44:47]\n"
"v_mfma_f32_16x16x32_bf16 v[48:51], v[180:183], a[152:155], v[48:51]\n"
"ds_read_b64_tr_b16 a[148:149], v6 offset:9472\n"
"ds_read_b64_tr_b16 a[150:151], v6 offset:14080\n"
"v_mfma_f32_16x16x32_bf16 v[52:55], v[252:255], a[152:155], v[52:55]\n"
"v_mfma_f32_16x16x32_bf16 v[56:59], v[180:183], a[156:159], v[56:59]\n"
"ds_read_b64_tr_b16 a[152:153], v6 offset:18432\n"
"ds_read_b64_tr_b16 a[154:155], v6 offset:23040\n"
"v_mfma_f32_16x16x32_bf16 v[60:63], v[252:255], a[156:159], v[60:63]\n"
"s_waitcnt lgkmcnt(8)\n"
"v_mfma_f32_16x16x32_bf16 v[64:67], v[180:183], a[160:163], v[64:67]\n"
"ds_read_b64_tr_b16 a[156:157], v6 offset:18464\n"
"ds_read_b64_tr_b16 a[158:159], v6 offset:23072\n"
"v_mfma_f32_16x16x32_bf16 v[68:71], v[252:255], a[160:163], v[68:71]\n"
"v_mfma_f32_16x16x32_bf16 v[72:75], v[180:183], a[164:167], v[72:75]\n"
"ds_read_b64_tr_b16 a[160:161], v6 offset:18496\n"
"ds_read_b64_tr_b16 a[162:163], v6 offset:23104\n"
"v_mfma_f32_16x16x32_bf16 v[76:79], v[252:255], a[164:167], v[76:79]\n"
"v_mfma_f32_16x16x32_bf16 v[80:83], v[180:183], a[168:171], v[80:83]\n"
"ds_read_b64_tr_b16 a[164:165], v6 offset:18528\n"
"ds_read_b64_tr_b16 a[166:167], v6 offset:23136\n"
"v_mfma_f32_16x16x32_bf16 v[84:87], v[252:255], a[168:171], v[84:87]\n"
"s_waitcnt lgkmcnt(8)\n"
"v_mfma_f32_16x16x32_bf16 v[88:91], v[180:183], a[172:175], v[88:91]\n"
"ds_read_b64_tr_b16 a[168:169], v6 offset:18560\n"
"ds_read_b64_tr_b16 a[170:171], v6 offset:23168\n"
"v_mfma_f32_16x16x32_bf16 v[92:95], v[252:255], a[172:175], v[92:95]\n"
"buffer_load_dwordx4 v8, s[12:15], s61 offen offset:0 lds\n"
"v_mfma_f32_16x16x32_bf16 v[96:99], v[180:183], a[144:147], v[96:99]\n"
"ds_read_b64_tr_b16 a[172:173], v6 offset:18592\n"
"ds_read_b64_tr_b16 a[174:175], v6 offset:23200\n"
"v_mfma_f32_16x16x32_bf16 v[100:103], v[252:255], a[144:147], v[100:103]\n"
"v_mfma_f32_16x16x32_bf16 v[104:107], v[180:183], a[148:151], v[104:107]\n"
"ds_read_b64_tr_b16 a[144:145], v6 offset:18624\n"
"ds_read_b64_tr_b16 a[146:147], v6 offset:23232\n"
"v_mfma_f32_16x16x32_bf16 v[108:111], v[252:255], a[148:151], v[108:111]\n"
"buffer_load_dwordx4 v9, s[12:15], s61 offen offset:1024 lds\n"
"s_waitcnt lgkmcnt(8)\n"
"v_pk_mul_f32 v[200:201], v[40:41], v[40:41]\n"
"s_lshl_b32 s67, s53, 8\n"
"s_lshl_b32 s68, s63, 2\n"
"v_pk_mul_f32 v[202:203], v[42:43], v[42:43]\n"
"s_add_u32 s67, s67, s68\n"
"s_add_u32 s68, s67, 4096\n"
"v_pk_mul_f32 v[204:205], v[44:45], v[44:45]\n"
"s_lshl_b32 s65, s63, 6\n"
"s_add_u32 s65, s65, 8192\n"
"v_pk_mul_f32 v[206:207], v[46:47], v[46:47]\n"
"s_lshl_b32 s66, s53, 2\n"
"s_add_u32 s66, s66, s65\n"
"v_pk_fma_f32 v[200:201], v[200:201], s[80:81], s[80:81] op_sel:[0,0,1] op_sel_hi:[1,0,1]\n"
"v_pk_fma_f32 v[202:203], v[202:203], s[80:81], s[80:81] op_sel:[0,0,1] op_sel_hi:[1,0,1]\n"
"v_pk_fma_f32 v[204:205], v[204:205], s[80:81], s[80:81] op_sel:[0,0,1] op_sel_hi:[1,0,1]\n"
"v_pk_fma_f32 v[206:207], v[206:207], s[80:81], s[80:81] op_sel:[0,0,1] op_sel_hi:[1,0,1]\n"
"v_pk_mul_f32 v[200:201], v[200:201], v[40:41]\n"
"v_pk_mul_f32 v[202:203], v[202:203], v[42:43]\n"
"v_pk_mul_f32 v[204:205], v[204:205], v[44:45]\n"
"buffer_load_dwordx4 v10, s[12:15], s61 offen offset:2048 lds\n"
"v_pk_mul_f32 v[206:207], v[206:207], v[46:47]\n"
"v_exp_f32 v200, v200\n"
"v_exp_f32 v201, v201\n"
"v_exp_f32 v202, v202\n"
"v_exp_f32 v203, v203\n"
"v_exp_f32 v204, v204\n"
"v_exp_f32 v205, v205\n"
"v_exp_f32 v206, v206\n"
"v_exp_f32 v207, v207\n"
"s_waitcnt vmcnt(22)\n"
"buffer_store_dwordx4 a[176:179], v24, s[16:19], s74 offen offset:0 sc1\n"
"v_pk_add_f32 v[200:201], v[200:201], 1.0 op_sel_hi:[1,0]\n"
"v_pk_add_f32 v[202:203], v[202:203], 1.0 op_sel_hi:[1,0]\n"
"v_pk_add_f32 v[204:205], v[204:205], 1.0 op_sel_hi:[1,0]\n"
"v_pk_add_f32 v[206:207], v[206:207], 1.0 op_sel_hi:[1,0]\n"
"v_rcp_f32 v200, v200\n"
"v_rcp_f32 v201, v201\n"
"v_rcp_f32 v202, v202\n"
"v_rcp_f32 v203, v203\n"
"v_rcp_f32 v204, v204\n"
"v_rcp_f32 v205, v205\n"
"v_rcp_f32 v206, v206\n"
"v_rcp_f32 v207, v207\n"
"v_pk_mul_f32 v[40:41], v[40:41], v[200:201]\n"
"v_pk_mul_f32 v[42:43], v[42:43], v[202:203]\n"
"v_pk_mul_f32 v[44:45], v[44:45], v[204:205]\n"
"v_pk_mul_f32 v[46:47], v[46:47], v[206:207]\n"
"v_pk_mul_f32 v[184:185], v[40:41], 1.0 op_sel_hi:[1,0]\n"
"v_pk_mul_f32 v[186:187], v[42:43], 1.0 op_sel_hi:[1,0]\n"
"v_pk_mul_f32 v[188:189], v[44:45], 1.0 op_sel_hi:[1,0]\n"
"v_pk_mul_f32 v[190:191], v[46:47], 1.0 op_sel_hi:[1,0]\n"
"v_pk_mul_f32 v[192:193], v[40:41], v[40:41]\n"
"v_pk_mul_f32 v[194:195], v[42:43], v[42:43]\n"
"v_pk_mul_f32 v[196:197], v[44:45], v[44:45]\n"
"v_pk_mul_f32 v[198:199], v[46:47], v[46:47]\n"
"v_pk_mul_f32 v[200:201], v[48:49], v[48:49]\n"
"v_pk_mul_f32 v[202:203], v[50:51], v[50:51]\n"
"v_pk_mul_f32 v[204:205], v[52:53], v[52:53]\n"
"v_pk_mul_f32 v[206:207], v[54:55], v[54:55]\n"
"v_pk_fma_f32 v[200:201], v[200:201], s[80:81], s[80:81] op_sel:[0,0,1] op_sel_hi:[1,0,1]\n"
"v_pk_fma_f32 v[202:203], v[202:203], s[80:81], s[80:81] op_sel:[0,0,1] op_sel_hi:[1,0,1]\n"
"v_pk_fma_f32 v[204:205], v[204:205], s[80:81], s[80:81] op_sel:[0,0,1] op_sel_hi:[1,0,1]\n"
"buffer_load_dwordx4 v11, s[12:15], s61 offen offset:3072 lds\n"
"s_add_u32 m0, s60, 77824\n"
"v_pk_fma_f32 v[206:207], v[206:207], s[80:81], s[80:81] op_sel:[0,0,1] op_sel_hi:[1,0,1]\n"
"v_pk_mul_f32 v[200:201], v[200:201], v[48:49]\n"
"v_pk_mul_f32 v[202:203], v[202:203], v[50:51]\n"
"v_pk_mul_f32 v[204:205], v[204:205], v[52:53]\n"
"v_pk_mul_f32 v[206:207], v[206:207], v[54:55]\n"
"s_waitcnt vmcnt(23)\n"
"buffer_store_dwordx4 a[180:183], v24, s[16:19], s75 offen offset:0 sc1\n"
"v_exp_f32 v200, v200\n"
"v_exp_f32 v201, v201\n"
"v_exp_f32 v202, v202\n"
"v_exp_f32 v203, v203\n"
"v_exp_f32 v204, v204\n"
"v_exp_f32 v205, v205\n"
"v_exp_f32 v206, v206\n"
"v_exp_f32 v207, v207\n"
"v_pk_add_f32 v[200:201], v[200:201], 1.0 op_sel_hi:[1,0]\n"
"v_pk_add_f32 v[202:203], v[202:203], 1.0 op_sel_hi:[1,0]\n"
"v_pk_add_f32 v[204:205], v[204:205], 1.0 op_sel_hi:[1,0]\n"
"v_pk_add_f32 v[206:207], v[206:207], 1.0 op_sel_hi:[1,0]\n"
"v_rcp_f32 v200, v200\n"
"v_rcp_f32 v201, v201\n"
"v_rcp_f32 v202, v202\n"
"v_rcp_f32 v203, v203\n"
"v_rcp_f32 v204, v204\n"
"v_rcp_f32 v205, v205\n"
"v_rcp_f32 v206, v206\n"
"v_rcp_f32 v207, v207\n"
"v_pk_mul_f32 v[48:49], v[48:49], v[200:201]\n"
"v_pk_mul_f32 v[50:51], v[50:51], v[202:203]\n"
"v_pk_mul_f32 v[52:53], v[52:53], v[204:205]\n"
"v_pk_mul_f32 v[54:55], v[54:55], v[206:207]\n"
"v_pk_add_f32 v[184:185], v[184:185], v[48:49]\n"
"v_pk_add_f32 v[186:187], v[186:187], v[50:51]\n"
"v_pk_add_f32 v[188:189], v[188:189], v[52:53]\n"
"v_pk_add_f32 v[190:191], v[190:191], v[54:55]\n"
"v_pk_fma_f32 v[192:193], v[48:49], v[48:49], v[192:193]\n"
"v_pk_fma_f32 v[194:195], v[50:51], v[50:51], v[194:195]\n"
"v_pk_fma_f32 v[196:197], v[52:53], v[52:53], v[196:197]\n"
"v_pk_fma_f32 v[198:199], v[54:55], v[54:55], v[198:199]\n"
"v_pk_mul_f32 v[200:201], v[56:57], v[56:57]\n"
"v_pk_mul_f32 v[202:203], v[58:59], v[58:59]\n"
"buffer_load_dwordx4 v12, s[12:15], s61 offen offset:0 lds\n"
"v_pk_mul_f32 v[204:205], v[60:61], v[60:61]\n"
"v_pk_mul_f32 v[206:207], v[62:63], v[62:63]\n"
"s_waitcnt vmcnt(24)\n"
"buffer_store_dwordx4 a[184:187], v24, s[16:19], s74 offen offset:128 sc1\n"
"v_pk_fma_f32 v[200:201], v[200:201], s[80:81], s[80:81] op_sel:[0,0,1] op_sel_hi:[1,0,1]\n"
"v_pk_fma_f32 v[202:203], v[202:203], s[80:81], s[80:81] op_sel:[0,0,1] op_sel_hi:[1,0,1]\n"
"v_pk_fma_f32 v[204:205], v[204:205], s[80:81], s[80:81] op_sel:[0,0,1] op_sel_hi:[1,0,1]\n"
"v_pk_fma_f32 v[206:207], v[206:207], s[80:81], s[80:81] op_sel:[0,0,1] op_sel_hi:[1,0,1]\n"
"v_pk_mul_f32 v[200:201], v[200:201], v[56:57]\n"
"v_pk_mul_f32 v[202:203], v[202:203], v[58:59]\n"
"v_pk_mul_f32 v[204:205], v[204:205], v[60:61]\n"
"v_pk_mul_f32 v[206:207], v[206:207], v[62:63]\n"
"v_exp_f32 v200, v200\n"
"v_exp_f32 v201, v201\n"
"v_exp_f32 v202, v202\n"
"v_exp_f32 v203, v203\n"
"v_exp_f32 v204, v204\n"
"v_exp_f32 v205, v205\n"
"v_exp_f32 v206, v206\n"
"v_exp_f32 v207, v207\n"
"v_pk_add_f32 v[200:201], v[200:201], 1.0 op_sel_hi:[1,0]\n"
"v_pk_add_f32 v[202:203], v[202:203], 1.0 op_sel_hi:[1,0]\n"
"v_pk_add_f32 v[204:205], v[204:205], 1.0 op_sel_hi:[1,0]\n"
"v_pk_add_f32 v[206:207], v[206:207], 1.0 op_sel_hi:[1,0]\n"
"v_rcp_f32 v200, v200\n"
"v_rcp_f32 v201, v201\n"
"v_rcp_f32 v202, v202\n"
"v_rcp_f32 v203, v203\n"
"v_rcp_f32 v204, v204\n"
"v_rcp_f32 v205, v205\n"
"v_rcp_f32 v206, v206\n"
"v_rcp_f32 v207, v207\n"
"v_pk_mul_f32 v[56:57], v[56:57], v[200:201]\n"
"v_pk_mul_f32 v[58:59], v[58:59], v[202:203]\n"
"v_pk_mul_f32 v[60:61], v[60:61], v[204:205]\n"
"v_pk_mul_f32 v[62:63], v[62:63], v[206:207]\n"
"v_pk_add_f32 v[184:185], v[184:185], v[56:57]\n"
"v_pk_add_f32 v[186:187], v[186:187], v[58:59]\n"
"v_pk_add_f32 v[188:189], v[188:189], v[60:61]\n"
"v_pk_add_f32 v[190:191], v[190:191], v[62:63]\n"
"s_waitcnt vmcnt(24)\n"
"buffer_store_dwordx4 a[188:191], v24, s[16:19], s75 offen offset:128 sc1\n"
"v_pk_fma_f32 v[192:193], v[56:57], v[56:57], v[192:193]\n"
"v_pk_fma_f32 v[194:195], v[58:59], v[58:59], v[194:195]\n"
"v_pk_fma_f32 v[196:197], v[60:61], v[60:61], v[196:197]\n"
"v_pk_fma_f32 v[198:199], v[62:63], v[62:63], v[198:199]\n"
"v_pk_mul_f32 v[200:201], v[64:65], v[64:65]\n"
"v_pk_mul_f32 v[202:203], v[66:67], v[66:67]\n"
"v_pk_mul_f32 v[204:205], v[68:69], v[68:69]\n"
"v_pk_mul_f32 v[206:207], v[70:71], v[70:71]\n"
"v_pk_fma_f32 v[200:201], v[200:201], s[80:81], s[80:81] op_sel:[0,0,1] op_sel_hi:[1,0,1]\n"
"v_pk_fma_f32 v[202:203], v[202:203], s[80:81], s[80:81] op_sel:[0,0,1] op_sel_hi:[1,0,1]\n"
"v_pk_fma_f32 v[204:205], v[204:205], s[80:81], s[80:81] op_sel:[0,0,1] op_sel_hi:[1,0,1]\n"
"v_pk_fma_f32 v[206:207], v[206:207], s[80:81], s[80:81] op_sel:[0,0,1] op_sel_hi:[1,0,1]\n"
"v_pk_mul_f32 v[200:201], v[200:201], v[64:65]\n"
"v_pk_mul_f32 v[202:203], v[202:203], v[66:67]\n"
"v_pk_mul_f32 v[204:205], v[204:205], v[68:69]\n"
"v_pk_mul_f32 v[206:207], v[206:207], v[70:71]\n"
"v_exp_f32 v200, v200\n"
"v_exp_f32 v201, v201\n"
"v_exp_f32 v202, v202\n"
"v_exp_f32 v203, v203\n"
"v_exp_f32 v204, v204\n"
"v_exp_f32 v205, v205\n"
"v_exp_f32 v206, v206\n"
"v_exp_f32 v207, v207\n"
"v_pk_add_f32 v[200:201], v[200:201], 1.0 op_sel_hi:[1,0]\n"
"buffer_load_dwordx4 v13, s[12:15], s61 offen offset:1024 lds\n"
"v_pk_add_f32 v[202:203], v[202:203], 1.0 op_sel_hi:[1,0]\n"
"v_pk_add_f32 v[204:205], v[204:205], 1.0 op_sel_hi:[1,0]\n"
"v_pk_add_f32 v[206:207], v[206:207], 1.0 op_sel_hi:[1,0]\n"
"v_rcp_f32 v200, v200\n"
"v_rcp_f32 v201, v201\n"
"v_rcp_f32 v202, v202\n"
"v_rcp_f32 v203, v203\n"
"v_rcp_f32 v204, v204\n"
"v_rcp_f32 v205, v205\n"
"v_rcp_f32 v206, v206\n"
"v_rcp_f32 v207, v207\n"
"s_waitcnt vmcnt(25)\n"
"buffer_store_dwordx4 a[192:195], v24, s[16:19], s74 offen offset:1024 sc1\n"
"v_pk_mul_f32 v[64:65], v[64:65], v[200:201]\n"
"v_pk_mul_f32 v[66:67], v[66:67], v[202:203]\n"
"v_pk_mul_f32 v[68:69], v[68:69], v[204:205]\n"
"v_pk_mul_f32 v[70:71], v[70:71], v[206:207]\n"
"v_pk_add_f32 v[184:185], v[184:185], v[64:65]\n"
"v_pk_add_f32 v[186:187], v[186:187], v[66:67]\n"
"v_pk_add_f32 v[188:189], v[188:189], v[68:69]\n"
"v_pk_add_f32 v[190:191], v[190:191], v[70:71]\n"
"v_pk_fma_f32 v[192:193], v[64:65], v[64:65], v[192:193]\n"
"v_pk_fma_f32 v[194:195], v[66:67], v[66:67], v[194:195]\n"
"v_pk_fma_f32 v[196:197], v[68:69], v[68:69], v[196:197]\n"
"v_pk_fma_f32 v[198:199], v[70:71], v[70:71], v[198:199]\n"
"v_pk_mul_f32 v[200:201], v[72:73], v[72:73]\n"
"v_pk_mul_f32 v[202:203], v[74:75], v[74:75]\n"
"v_pk_mul_f32 v[204:205], v[76:77], v[76:77]\n"
"v_pk_mul_f32 v[206:207], v[78:79], v[78:79]\n"
"v_pk_fma_f32 v[200:201], v[200:201], s[80:81], s[80:81] op_sel:[0,0,1] op_sel_hi:[1,0,1]\n"
"v_pk_fma_f32 v[202:203], v[202:203], s[80:81], s[80:81] op_sel:[0,0,1] op_sel_hi:[1,0,1]\n"
"v_pk_fma_f32 v[204:205], v[204:205], s[80:81], s[80:81] op_sel:[0,0,1] op_sel_hi:[1,0,1]\n"
"v_pk_fma_f32 v[206:207], v[206:207], s[80:81], s[80:81] op_sel:[0,0,1] op_sel_hi:[1,0,1]\n"
"v_pk_mul_f32 v[200:201], v[200:201], v[72:73]\n"
"v_pk_mul_f32 v[202:203], v[202:203], v[74:75]\n"
"v_pk_mul_f32 v[204:205], v[204:205], v[76:77]\n"
"v_pk_mul_f32 v[206:207], v[206:207], v[78:79]\n"
"v_exp_f32 v200, v200\n"
"v_exp_f32 v201, v201\n"
"v_exp_f32 v202, v202\n"
"v_exp_f32 v203, v203\n"
"v_exp_f32 v204, v204\n"
"v_exp_f32 v205, v205\n"
"v_exp_f32 v206, v206\n"
"buffer_load_dwordx4 v14, s[12:15], s61 offen offset:2048 lds\n"
"v_exp_f32 v207, v207\n"
"v_pk_add_f32 v[200:201], v[200:201], 1.0 op_sel_hi:[1,0]\n"
"v_pk_add_f32 v[202:203], v[202:203], 1.0 op_sel_hi:[1,0]\n"
"v_pk_add_f32 v[204:205], v[204:205], 1.0 op_sel_hi:[1,0]\n"
"v_pk_add_f32 v[206:207], v[206:207], 1.0 op_sel_hi:[1,0]\n"
"s_waitcnt vmcnt(26)\n"
"buffer_store_dwordx4 a[196:199], v24, s[16:19], s75 offen offset:1024 sc1\n"
"v_rcp_f32 v200, v200\n"
"v_rcp_f32 v201, v201\n"
"v_rcp_f32 v202, v202\n"
"v_rcp_f32 v203, v203\n"
"v_rcp_f32 v204, v204\n"
"v_rcp_f32 v205, v205\n"
"v_rcp_f32 v206, v206\n"
"v_rcp_f32 v207, v207\n"
"v_pk_mul_f32 v[72:73], v[72:73], v[200:201]\n"
"v_pk_mul_f32 v[74:75], v[74:75], v[202:203]\n"
"v_pk_mul_f32 v[76:77], v[76:77], v[204:205]\n"
"v_pk_mul_f32 v[78:79], v[78:79], v[206:207]\n"
"v_pk_add_f32 v[184:185], v[184:185], v[72:73]\n"
"v_pk_add_f32 v[186:187], v[186:187], v[74:75]\n"
"v_pk_add_f32 v[188:189], v[188:189], v[76:77]\n"
"v_pk_add_f32 v[190:191], v[190:191], v[78:79]\n"
"v_pk_fma_f32 v[192:193], v[72:73], v[72:73], v[192:193]\n"
"v_pk_fma_f32 v[194:195], v[74:75], v[74:75], v[194:195]\n"
"v_pk_fma_f32 v[196:197], v[76:77], v[76:77], v[196:197]\n"
"v_pk_fma_f32 v[198:199], v[78:79], v[78:79], v[198:199]\n"
"v_pk_mul_f32 v[200:201], v[80:81], v[80:81]\n"
"v_pk_mul_f32 v[202:203], v[82:83], v[82:83]\n"
"v_pk_mul_f32 v[204:205], v[84:85], v[84:85]\n"
"v_pk_mul_f32 v[206:207], v[86:87], v[86:87]\n"
"v_pk_fma_f32 v[200:201], v[200:201], s[80:81], s[80:81] op_sel:[0,0,1] op_sel_hi:[1,0,1]\n"
"v_pk_fma_f32 v[202:203], v[202:203], s[80:81], s[80:81] op_sel:[0,0,1] op_sel_hi:[1,0,1]\n"
"v_pk_fma_f32 v[204:205], v[204:205], s[80:81], s[80:81] op_sel:[0,0,1] op_sel_hi:[1,0,1]\n"
"v_pk_fma_f32 v[206:207], v[206:207], s[80:81], s[80:81] op_sel:[0,0,1] op_sel_hi:[1,0,1]\n"
"v_pk_mul_f32 v[200:201], v[200:201], v[80:81]\n"
"v_pk_mul_f32 v[202:203], v[202:203], v[82:83]\n"
"v_pk_mul_f32 v[204:205], v[204:205], v[84:85]\n"
"v_pk_mul_f32 v[206:207], v[206:207], v[86:87]\n"
"v_exp_f32 v200, v200\n"
"v_exp_f32 v201, v201\n"
"v_exp_f32 v202, v202\n"
"v_exp_f32 v203, v203\n"
"s_waitcnt vmcnt(26)\n"
"buffer_store_dwordx4 a[200:203], v24, s[16:19], s74 offen offset:1152 sc1\n"
"v_exp_f32 v204, v204\n"
"v_exp_f32 v205, v205\n"
"v_exp_f32 v206, v206\n"
"v_exp_f32 v207, v207\n"
"v_pk_add_f32 v[200:201], v[200:201], 1.0 op_sel_hi:[1,0]\n"
"v_pk_add_f32 v[202:203], v[202:203], 1.0 op_sel_hi:[1,0]\n"
"v_pk_add_f32 v[204:205], v[204:205], 1.0 op_sel_hi:[1,0]\n"
"v_pk_add_f32 v[206:207], v[206:207], 1.0 op_sel_hi:[1,0]\n"
"v_rcp_f32 v200, v200\n"
"v_rcp_f32 v201, v201\n"
"v_rcp_f32 v202, v202\n"
"v_rcp_f32 v203, v203\n"
"v_rcp_f32 v204, v204\n"
"v_rcp_f32 v205, v205\n"
"v_rcp_f32 v206, v206\n"
"v_rcp_f32 v207, v207\n"
"v_pk_mul_f32 v[80:81], v[80:81], v[200:201]\n"
"v_pk_mul_f32 v[82:83], v[82:83], v[202:203]\n"
"v_pk_mul_f32 v[84:85], v[84:85], v[204:205]\n"
"v_pk_mul_f32 v[86:87], v[86:87], v[206:207]\n"
"buffer_load_dwordx4 v15, s[12:15], s61 offen offset:3072 lds\n"
"s_add_u32 m0, s60, 81920\n"
"v_pk_add_f32 v[184:185], v[184:185], v[80:81]\n"
"v_pk_add_f32 v[186:187], v[186:187], v[82:83]\n"
"v_pk_add_f32 v[188:189], v[188:189], v[84:85]\n"
"v_pk_add_f32 v[190:191], v[190:191], v[86:87]\n"
"v_pk_fma_f32 v[192:193], v[80:81], v[80:81], v[192:193]\n"
"v_pk_fma_f32 v[194:195], v[82:83], v[82:83], v[194:195]\n"
"v_pk_fma_f32 v[196:197], v[84:85], v[84:85], v[196:197]\n"
"v_pk_fma_f32 v[198:199], v[86:87], v[86:87], v[198:199]\n"
"v_pk_mul_f32 v[200:201], v[88:89], v[88:89]\n"
"v_pk_mul_f32 v[202:203], v[90:91], v[90:91]\n"
"v_pk_mul_f32 v[204:205], v[92:93], v[92:93]\n"
"v_pk_mul_f32 v[206:207], v[94:95], v[94:95]\n"
"v_pk_fma_f32 v[200:201], v[200:201], s[80:81], s[80:81] op_sel:[0,0,1] op_sel_hi:[1,0,1]\n"
"v_pk_fma_f32 v[202:203], v[202:203], s[80:81], s[80:81] op_sel:[0,0,1] op_sel_hi:[1,0,1]\n"
"v_pk_fma_f32 v[204:205], v[204:205], s[80:81], s[80:81] op_sel:[0,0,1] op_sel_hi:[1,0,1]\n"
"v_pk_fma_f32 v[206:207], v[206:207], s[80:81], s[80:81] op_sel:[0,0,1] op_sel_hi:[1,0,1]\n"
"s_waitcnt vmcnt(27)\n"
"buffer_store_dwordx4 a[204:207], v24, s[16:19], s75 offen offset:1152 sc1\n"
"v_pk_mul_f32 v[200:201], v[200:201], v[88:89]\n"
"v_pk_mul_f32 v[202:203], v[202:203], v[90:91]\n"
"v_pk_mul_f32 v[204:205], v[204:205], v[92:93]\n"
"v_pk_mul_f32 v[206:207], v[206:207], v[94:95]\n"
"v_exp_f32 v200, v200\n"
"v_exp_f32 v201, v201\n"
"v_exp_f32 v202, v202\n"
"v_exp_f32 v203, v203\n"
"v_exp_f32 v204, v204\n"
"v_exp_f32 v205, v205\n"
"v_exp_f32 v206, v206\n"
"v_exp_f32 v207, v207\n"
"v_pk_add_f32 v[200:201], v[200:201], 1.0 op_sel_hi:[1,0]\n"
"v_pk_add_f32 v[202:203], v[202:203], 1.0 op_sel_hi:[1,0]\n"
"v_pk_add_f32 v[204:205], v[204:205], 1.0 op_sel_hi:[1,0]\n"
"v_pk_add_f32 v[206:207], v[206:207], 1.0 op_sel_hi:[1,0]\n"
"v_rcp_f32 v200, v200\n"
"v_rcp_f32 v201, v201\n"
"v_rcp_f32 v202, v202\n"
"v_rcp_f32 v203, v203\n"
"v_rcp_f32 v204, v204\n"
"v_rcp_f32 v205, v205\n"
"v_rcp_f32 v206, v206\n"
"v_rcp_f32 v207, v207\n"
"buffer_load_dwordx4 v16, s[12:15], s61 offen offset:0 lds\n"
"s_add_u32 s61, s61, 1179648\n"
"v_pk_mul_f32 v[88:89], v[88:89], v[200:201]\n"
"s_add_u32 m0, s60, 110592\n"
"v_pk_mul_f32 v[90:91], v[90:91], v[202:203]\n"
"v_pk_mul_f32 v[92:93], v[92:93], v[204:205]\n"
"v_pk_mul_f32 v[94:95], v[94:95], v[206:207]\n"
"v_pk_add_f32 v[184:185], v[184:185], v[88:89]\n"
"v_pk_add_f32 v[186:187], v[186:187], v[90:91]\n"
"v_pk_add_f32 v[188:189], v[188:189], v[92:93]\n"
"v_pk_add_f32 v[190:191], v[190:191], v[94:95]\n"
"v_pk_fma_f32 v[192:193], v[88:89], v[88:89], v[192:193]\n"
"v_pk_fma_f32 v[194:195], v[90:91], v[90:91], v[194:195]\n"
"v_pk_fma_f32 v[196:197], v[92:93], v[92:93], v[196:197]\n"
"v_pk_fma_f32 v[198:199], v[94:95], v[94:95], v[198:199]\n"
"v_pk_mul_f32 v[200:201], v[96:97], v[96:97]\n"
"v_pk_mul_f32 v[202:203], v[98:99], v[98:99]\n"
"v_pk_mul_f32 v[204:205], v[100:101], v[100:101]\n"
"v_pk_mul_f32 v[206:207], v[102:103], v[102:103]\n"
"v_pk_fma_f32 v[200:201], v[200:201], s[80:81], s[80:81] op_sel:[0,0,1] op_sel_hi:[1,0,1]\n"
"v_pk_fma_f32 v[202:203], v[202:203], s[80:81], s[80:81] op_sel:[0,0,1] op_sel_hi:[1,0,1]\n"
"v_pk_fma_f32 v[204:205], v[204:205], s[80:81], s[80:81] op_sel:[0,0,1] op_sel_hi:[1,0,1]\n"
"v_pk_fma_f32 v[206:207], v[206:207], s[80:81], s[80:81] op_sel:[0,0,1] op_sel_hi:[1,0,1]\n"
"v_pk_mul_f32 v[200:201], v[200:201], v[96:97]\n"
"buffer_load_dwordx4 v8, s[12:15], s61 offen offset:0 lds\n"
"v_pk_mul_f32 v[202:203], v[202:203], v[98:99]\n"
"v_pk_mul_f32 v[204:205], v[204:205], v[100:101]\n"
"v_pk_mul_f32 v[206:207], v[206:207], v[102:103]\n"
"v_exp_f32 v200, v200\n"
"v_exp_f32 v201, v201\n"
"v_exp_f32 v202, v202\n"
"v_exp_f32 v203, v203\n"
"v_exp_f32 v204, v204\n"
"v_exp_f32 v205, v205\n"
"v_exp_f32 v206, v206\n"
"v_exp_f32 v207, v207\n"
"v_pk_add_f32 v[200:201], v[200:201], 1.0 op_sel_hi:[1,0]\n"
"v_pk_add_f32 v[202:203], v[202:203], 1.0 op_sel_hi:[1,0]\n"
"v_pk_add_f32 v[204:205], v[204:205], 1.0 op_sel_hi:[1,0]\n"
"v_pk_add_f32 v[206:207], v[206:207], 1.0 op_sel_hi:[1,0]\n"
"v_rcp_f32 v200, v200\n"
"v_rcp_f32 v201, v201\n"
"buffer_load_dwordx4 v9, s[12:15], s61 offen offset:1024 lds\n"
"v_rcp_f32 v202, v202\n"
"v_rcp_f32 v203, v203\n"
"v_rcp_f32 v204, v204\n"
"v_rcp_f32 v205, v205\n"
"v_rcp_f32 v206, v206\n"
"v_rcp_f32 v207, v207\n"
"v_pk_mul_f32 v[96:97], v[96:97], v[200:201]\n"
"v_pk_mul_f32 v[98:99], v[98:99], v[202:203]\n"
"v_pk_mul_f32 v[100:101], v[100:101], v[204:205]\n"
"v_pk_mul_f32 v[102:103], v[102:103], v[206:207]\n"
"v_pk_add_f32 v[184:185], v[184:185], v[96:97]\n"
"v_pk_add_f32 v[186:187], v[186:187], v[98:99]\n"
"v_pk_add_f32 v[188:189], v[188:189], v[100:101]\n"
"v_pk_add_f32 v[190:191], v[190:191], v[102:103]\n"
"v_pk_fma_f32 v[192:193], v[96:97], v[96:97], v[192:193]\n"
"v_pk_fma_f32 v[194:195], v[98:99], v[98:99], v[194:195]\n"
"v_pk_fma_f32 v[196:197], v[100:101], v[100:101], v[196:197]\n"
"v_pk_fma_f32 v[198:199], v[102:103], v[102:103], v[198:199]\n"
"buffer_load_dwordx4 v10, s[12:15], s61 offen offset:2048 lds\n"
"v_pk_mul_f32 v[200:201], v[104:105], v[104:105]\n"
"v_pk_mul_f32 v[202:203], v[106:107], v[106:107]\n"
"v_pk_mul_f32 v[204:205], v[108:109], v[108:109]\n"
"v_pk_mul_f32 v[206:207], v[110:111], v[110:111]\n"
"v_pk_fma_f32 v[200:201], v[200:201], s[80:81], s[80:81] op_sel:[0,0,1] op_sel_hi:[1,0,1]\n"
"v_pk_fma_f32 v[202:203], v[202:203], s[80:81], s[80:81] op_sel:[0,0,1] op_sel_hi:[1,0,1]\n"
"v_pk_fma_f32 v[204:205], v[204:205], s[80:81], s[80:81] op_sel:[0,0,1] op_sel_hi:[1,0,1]\n"
"v_pk_fma_f32 v[206:207], v[206:207], s[80:81], s[80:81] op_sel:[0,0,1] op_sel_hi:[1,0,1]\n"
"v_pk_mul_f32 v[200:201], v[200:201], v[104:105]\n"
"v_pk_mul_f32 v[202:203], v[202:203], v[106:107]\n"
"v_pk_mul_f32 v[204:205], v[204:205], v[108:109]\n"
"v_pk_mul_f32 v[206:207], v[206:207], v[110:111]\n"
"v_exp_f32 v200, v200\n"
"v_exp_f32 v201, v201\n"
"v_exp_f32 v202, v202\n"
"v_exp_f32 v203, v203\n"
"v_exp_f32 v204, v204\n"
"v_exp_f32 v205, v205\n"
"v_exp_f32 v206, v206\n"
"buffer_load_dwordx4 v11, s[12:15], s61 offen offset:3072 lds\n"
"s_add_u32 m0, s60, 114688\n"
"v_exp_f32 v207, v207\n"
"v_pk_add_f32 v[200:201], v[200:201], 1.0 op_sel_hi:[1,0]\n"
"v_pk_add_f32 v[202:203], v[202:203], 1.0 op_sel_hi:[1,0]\n"
"v_pk_add_f32 v[204:205], v[204:205], 1.0 op_sel_hi:[1,0]\n"
"v_pk_add_f32 v[206:207], v[206:207], 1.0 op_sel_hi:[1,0]\n"
"v_rcp_f32 v200, v200\n"
"v_rcp_f32 v201, v201\n"
"v_rcp_f32 v202, v202\n"
"v_rcp_f32 v203, v203\n"
"v_rcp_f32 v204, v204\n"
"v_rcp_f32 v205, v205\n"
"v_rcp_f32 v206, v206\n"
"v_rcp_f32 v207, v207\n"
"v_pk_mul_f32 v[104:105], v[104:105], v[200:201]\n"
"v_pk_mul_f32 v[106:107], v[106:107], v[202:203]\n"
"buffer_load_dwordx4 v12, s[12:15], s61 offen offset:0 lds\n"
"v_pk_mul_f32 v[108:109], v[108:109], v[204:205]\n"
"v_pk_mul_f32 v[110:111], v[110:111], v[206:207]\n"
"v_pk_add_f32 v[184:185], v[184:185], v[104:105]\n"
"v_pk_add_f32 v[186:187], v[186:187], v[106:107]\n"
"v_pk_add_f32 v[188:189], v[188:189], v[108:109]\n"
"v_pk_add_f32 v[190:191], v[190:191], v[110:111]\n"
"v_pk_fma_f32 v[192:193], v[104:105], v[104:105], v[192:193]\n"
"v_pk_fma_f32 v[194:195], v[106:107], v[106:107], v[194:195]\n"
"v_pk_fma_f32 v[196:197], v[108:109], v[108:109], v[196:197]\n"
"v_pk_fma_f32 v[198:199], v[110:111], v[110:111], v[198:199]\n"
"v_add_f32_dpp v184, v184, v184 row_ror:8 row_mask:0xf bank_mask:0x3\n"
"v_add_f32_dpp v185, v185, v185 row_ror:8 row_mask:0xf bank_mask:0x3\n"
"v_add_f32_dpp v186, v186, v186 row_ror:8 row_mask:0xf bank_mask:0x3\n"
"v_add_f32_dpp v187, v187, v187 row_ror:8 row_mask:0xf bank_mask:0x3\n"
"v_add_f32_dpp v192, v192, v192 row_ror:8 row_mask:0xf bank_mask:0x3\n"
"v_add_f32_dpp v193, v193, v193 row_ror:8 row_mask:0xf bank_mask:0x3\n"
"v_add_f32_dpp v194, v194, v194 row_ror:8 row_mask:0xf bank_mask:0x3\n"
"v_add_f32_dpp v195, v195, v195 row_ror:8 row_mask:0xf bank_mask:0x3\n"
"v_add_f32_dpp v184, v188, v188 row_ror:8 row_mask:0xf bank_mask:0xc\n"
"v_add_f32_dpp v185, v189, v189 row_ror:8 row_mask:0xf bank_mask:0xc\n"
"v_add_f32_dpp v186, v190, v190 row_ror:8 row_mask:0xf bank_mask:0xc\n"
"v_add_f32_dpp v187, v191, v191 row_ror:8 row_mask:0xf bank_mask:0xc\n"
"v_add_f32_dpp v192, v196, v196 row_ror:8 row_mask:0xf bank_mask:0xc\n"
"v_add_f32_dpp v193, v197, v197 row_ror:8 row_mask:0xf bank_mask:0xc\n"
"v_add_f32_dpp v194, v198, v198 row_ror:8 row_mask:0xf bank_mask:0xc\n"
"v_add_f32_dpp v195, v199, v199 row_ror:8 row_mask:0xf bank_mask:0xc\n"
"v_add_f32_dpp v184, v184, v184 row_half_mirror row_mask:0xf bank_mask:0x5\n"
"v_add_f32_dpp v185, v185, v185 row_half_mirror row_mask:0xf bank_mask:0x5\n"
"v_add_f32_dpp v192, v192, v192 row_half_mirror row_mask:0xf bank_mask:0x5\n"
"v_add_f32_dpp v193, v193, v193 row_half_mirror row_mask:0xf bank_mask:0x5\n"
"v_add_f32_dpp v184, v186, v186 row_half_mirror row_mask:0xf bank_mask:0xa\n"
"v_add_f32_dpp v185, v187, v187 row_half_mirror row_mask:0xf bank_mask:0xa\n"
"v_add_f32_dpp v192, v194, v194 row_half_mirror row_mask:0xf bank_mask:0xa\n"
"v_add_f32_dpp v193, v195, v195 row_half_mirror row_mask:0xf bank_mask:0xa\n"
"v_add_f32_dpp v184, v184, v184 quad_perm:[1,0,3,2] row_mask:0xf bank_mask:0xf\n"
"v_add_f32_dpp v185, v185, v185 quad_perm:[1,0,3,2] row_mask:0xf bank_mask:0xf\n"
"v_add_f32_dpp v192, v192, v192 quad_perm:[1,0,3,2] row_mask:0xf bank_mask:0xf\n"
"v_add_f32_dpp v193, v193, v193 quad_perm:[1,0,3,2] row_mask:0xf bank_mask:0xf\n"
"v_add_f32_dpp v184, v184, v184 quad_perm:[2,3,0,1] row_mask:0xf bank_mask:0xf\n"
"v_add_f32_dpp v185, v185, v185 quad_perm:[2,3,0,1] row_mask:0xf bank_mask:0xf\n"
"v_add_f32_dpp v192, v192, v192 quad_perm:[2,3,0,1] row_mask:0xf bank_mask:0xf\n"
"v_add_f32_dpp v193, v193, v193 quad_perm:[2,3,0,1] row_mask:0xf bank_mask:0xf\n"
"s_mov_b32 s82, 0xaaaaaaaa\n"
"s_mov_b32 s83, 0xaaaaaaaa\n"
"s_mov_b32 s84, 0xcccccccc\n"
"s_mov_b32 s85, 0xcccccccc\n"
"v_cndmask_b32 v184, v184, v192, s[82:83]\n"
"v_cndmask_b32 v185, v185, v193, s[82:83]\n"
"s_lshl_b32 s69, s63, 13\n"
"s_add_u32 s69, s69, 65536\n"
"s_lshl_b32 s70, s53, 9\n"
"s_add_u32 s70, s70, s69\n"
"v_cndmask_b32 v184, v184, v185, s[84:85]\n"
"v_mov_b32 v185, s55\n"
"v_lshlrev_b32 v186, 3, v1\n"
"buffer_store_dwordx2 v[184:185], v186, s[16:19], s70 offen sc1\n"
"s_waitcnt vmcnt(7)\n"
"s_mov_b64 exec, 1\n"
"buffer_store_dword v17, off, s[16:19], s68 sc1\n"
"buffer_store_dword v17, off, s[16:19], s66 sc1\n"
"s_mov_b64 exec, -1\n"
"v_mfma_f32_16x16x32_bf16 v[112:115], a[152:155], a[0:3], 0\n"
".Lmk_PCHERE_2:\n"
"s_add_u32 s0, s0, .Lmk_PCHERE_2-.Lmk_PCHERE_1\n"
"s_addc_u32 s1, s1, 0\n"
"s_sub_u32 s2, s2, .Lmk_PCHERE_2-.Lmk_PCHERE_1\n"
"s_max_i32 s2, s2, 0\n"
"buffer_load_dword a255, off, s[0:3], 0\n"
"ds_read_b64_tr_b16 a[148:149], v6 offset:18656\n"
"ds_read_b64_tr_b16 a[150:151], v6 offset:23264\n"
"v_mfma_f32_16x16x32_bf16 v[116:119], a[152:155], a[4:7], 0\n"
"v_lshlrev_b32 v33, 2, v1\n"
"s_lshl_b32 s71, s53, 8\n"
"v_mfma_f32_16x16x32_bf16 v[120:123], a[156:159], a[0:3], 0\n"
"ds_read_b64_tr_b16 a[152:153], v6 offset:18688\n"
"ds_read_b64_tr_b16 a[154:155], v6 offset:23296\n"
"v_mfma_f32_16x16x32_bf16 v[124:127], a[156:159], a[4:7], 0\n"
"s_add_u32 s71, s71, 4096\n"
"v_and_b32 v35, 15, v1\n"
"v_mfma_f32_16x16x32_bf16 v[128:131], a[160:163], a[0:3], 0\n"
"ds_read_b64_tr_b16 a[156:157], v6 offset:27648\n"
"ds_read_b64_tr_b16 a[158:159], v6 offset:32256\n"
"v_mfma_f32_16x16x32_bf16 v[132:135], a[160:163], a[4:7], 0\n"
"v_lshlrev_b32 v35, 2, v35\n"
"s_waitcnt lgkmcnt(8)\n"
"v_mfma_f32_16x16x32_bf16 v[136:139], a[164:167], a[0:3], 0\n"
"ds_read_b64_tr_b16 a[160:161], v6 offset:27680\n"
"ds_read_b64_tr_b16 a[162:163], v6 offset:32288\n"
"v_mfma_f32_16x16x32_bf16 v[140:143], a[164:167], a[4:7], 0\n"
"v_and_b32 v26, 12, v1\n"
"v_lshlrev_b32 v26, 6, v26\n"
"v_mfma_f32_16x16x32_bf16 v[144:147], a[168:171], a[0:3], 0\n"
"ds_read_b64_tr_b16 a[164:165], v6 offset:27712\n"
"ds_read_b64_tr_b16 a[166:167], v6 offset:32320\n"
"v_mfma_f32_16x16x32_bf16 v[148:151], a[168:171], a[4:7], 0\n"
"v_lshrrev_b32 v27, 4, v1\n"
"v_lshl_add_u32 v26, v27, 6, v26\n"
"v_mfma_f32_16x16x32_bf16 v[152:155], a[172:175], a[0:3], 0\n"
"ds_read_b64_tr_b16 a[168:169], v6 offset:27744\n"
"ds_read_b64_tr_b16 a[170:171], v6 offset:32352\n"
"v_mfma_f32_16x16x32_bf16 v[156:159], a[172:175], a[4:7], 0\n"
"v_and_b32 v27, 3, v1\n"
"s_waitcnt lgkmcnt(8)\n"
"v_mfma_f32_16x16x32_bf16 v[160:163], a[144:147], a[0:3], 0\n"
"ds_read_b64_tr_b16 a[172:173], v6 offset:27776\n"
"ds_read_b64_tr_b16 a[174:175], v6 offset:32384\n"
"v_mfma_f32_16x16x32_bf16 v[164:167], a[144:147], a[4:7], 0\n"
"v_lshl_add_u32 v26, v27, 4, v26\n"
"v_add_u32 v27, 0x10000, v26\n"
"v_mfma_f32_16x16x32_bf16 v[168:171], a[148:151], a[0:3], 0\n"
"ds_read_b64_tr_b16 a[144:145], v6 offset:27808\n"
"ds_read_b64_tr_b16 a[146:147], v6 offset:32416\n"
"v_mfma_f32_16x16x32_bf16 v[172:175], a[148:151], a[4:7], 0\n"
"v_add_u32 v28, 0x20000, v26\n"
"s_mul_i32 s20, s53, 1296\n"
"v_mfma_f32_16x16x32_bf16 v[176:179], a[152:155], a[0:3], 0\n"
"ds_read_b64_tr_b16 a[148:149], v6 offset:27840\n"
"ds_read_b64_tr_b16 a[150:151], v6 offset:32448\n"
"v_mfma_f32_16x16x32_bf16 v[180:183], a[152:155], a[4:7], 0\n"
"buffer_load_dwordx4 v13, s[12:15], s61 offen offset:1024 lds\n"
"s_waitcnt lgkmcnt(8)\n"
"v_mfma_f32_16x16x32_bf16 v[112:115], a[156:159], a[8:11], v[112:115]\n"
"ds_read_b64_tr_b16 a[152:153], v6 offset:27872\n"
"ds_read_b64_tr_b16 a[154:155], v6 offset:32480\n"
"v_mfma_f32_16x16x32_bf16 v[116:119], a[156:159], a[12:15], v[116:119]\n"
"buffer_load_dwordx4 v14, s[12:15], s61 offen offset:2048 lds\n"
"buffer_load_dwordx4 v15, s[12:15], s61 offen offset:3072 lds\n"
"v_mfma_f32_16x16x32_bf16 v[120:123], a[160:163], a[8:11], v[120:123]\n"
"ds_read_b64_tr_b16 a[156:157], v6 offset:27904\n"
"ds_read_b64_tr_b16 a[158:159], v6 offset:32512\n"
"v_mfma_f32_16x16x32_bf16 v[124:127], a[160:163], a[12:15], v[124:127]\n"
"s_add_u32 m0, s60, 118784\n"
"s_nop 0\n"
"buffer_load_dwordx4 v16, s[12:15], s61 offen offset:0 lds\n"
"v_mfma_f32_16x16x32_bf16 v[128:131], a[164:167], a[8:11], v[128:131]\n"
"v_lshl_add_u32 v240, v0, 1, s20\n"
"v_mfma_f32_16x16x32_bf16 v[132:135], a[164:167], a[12:15], v[132:135]\n"
"buffer_load_ushort v249, v240, s[36:39], 0 offen offset:0\n"
"s_waitcnt lgkmcnt(6)\n"
"v_mfma_f32_16x16x32_bf16 v[136:139], a[168:171], a[8:11], v[136:139]\n"
"buffer_load_ushort v250, v240, s[36:39], 0 offen offset:512\n"
"buffer_load_ushort v251, v240, s[36:39], 0 offen offset:1024\n"
"v_mfma_f32_16x16x32_bf16 v[140:143], a[168:171], a[12:15], v[140:143]\n"
"s_waitcnt lgkmcnt(0)\n"
"s_barrier\n"
"v_mfma_f32_16x16x32_bf16 v[144:147], a[172:175], a[8:11], v[144:147]\n"
"ds_read_b64_tr_b16 a[160:161], v6 offset:36864\n"
"ds_read_b64_tr_b16 a[162:163], v6 offset:41472\n"
"v_mfma_f32_16x16x32_bf16 v[148:151], a[172:175], a[12:15], v[148:151]\n"
"ds_read_b64_tr_b16 a[164:165], v6 offset:36896\n"
"ds_read_b64_tr_b16 a[166:167], v6 offset:41504\n"
"v_mfma_f32_16x16x32_bf16 v[152:155], a[144:147], a[8:11], v[152:155]\n"
"ds_read_b64_tr_b16 a[168:169], v6 offset:36928\n"
"ds_read_b64_tr_b16 a[170:171], v6 offset:41536\n"
"v_mfma_f32_16x16x32_bf16 v[156:159], a[144:147], a[12:15], v[156:159]\n"
"ds_read_b64_tr_b16 a[172:173], v6 offset:36960\n"
"ds_read_b64_tr_b16 a[174:175], v6 offset:41568\n"
"v_mfma_f32_16x16x32_bf16 v[160:163], a[148:151], a[8:11], v[160:163]\n"
"ds_read_b64_tr_b16 a[144:145], v6 offset:36992\n"
"ds_read_b64_tr_b16 a[146:147], v6 offset:41600\n"
"v_mfma_f32_16x16x32_bf16 v[164:167], a[148:151], a[12:15], v[164:167]\n"
"s_add_u32 s61, s61, 1179648\n"
"s_add_u32 m0, s60, 0\n"
"v_mfma_f32_16x16x32_bf16 v[168:171], a[152:155], a[8:11], v[168:171]\n"
"ds_read_b64_tr_b16 a[148:149], v6 offset:37024\n"
"ds_read_b64_tr_b16 a[150:151], v6 offset:41632\n"
"v_mfma_f32_16x16x32_bf16 v[172:175], a[152:155], a[12:15], v[172:175]\n"
"buffer_load_dwordx4 v8, s[12:15], s61 offen offset:0 lds\n"
"v_add_u32 v241, 0, v0\n"
"v_mfma_f32_16x16x32_bf16 v[176:179], a[156:159], a[8:11], v[176:179]\n"
"ds_read_b64_tr_b16 a[152:153], v6 offset:37056\n"
"ds_read_b64_tr_b16 a[154:155], v6 offset:41664\n"
"v_mfma_f32_16x16x32_bf16 v[180:183], a[156:159], a[12:15], v[180:183]\n"
"buffer_load_dwordx4 v9, s[12:15], s61 offen offset:1024 lds\n"
"s_waitcnt lgkmcnt(8)\n"
"v_mfma_f32_16x16x32_bf16 v[112:115], a[160:163], a[16:19], v[112:115]\n"
"ds_read_b64_tr_b16 a[156:157], v6 offset:37088\n"
"ds_read_b64_tr_b16 a[158:159], v6 offset:41696\n"
"v_mfma_f32_16x16x32_bf16 v[116:119], a[160:163], a[20:23], v[116:119]\n"
"v_mul_u32_u24 v242, 911, v241\n"
"v_lshrrev_b32 v242, 14, v242\n"
"v_mfma_f32_16x16x32_bf16 v[120:123], a[164:167], a[16:19], v[120:123]\n"
"ds_read_b64_tr_b16 a[160:161], v6 offset:37120\n"
"ds_read_b64_tr_b16 a[162:163], v6 offset:41728\n"
"v_mfma_f32_16x16x32_bf16 v[124:127], a[164:167], a[20:23], v[124:127]\n"
"v_mul_u32_u24 v243, 911, v242\n"
"v_lshrrev_b32 v243, 14, v243\n"
"v_mfma_f32_16x16x32_bf16 v[128:131], a[168:171], a[16:19], v[128:131]\n"
"ds_read_b64_tr_b16 a[164:165], v6 offset:46080\n"
"ds_read_b64_tr_b16 a[166:167], v6 offset:50688\n"
"v_mfma_f32_16x16x32_bf16 v[132:135], a[168:171], a[20:23], v[132:135]\n"
"buffer_load_dwordx4 v10, s[12:15], s61 offen offset:2048 lds\n"
"s_waitcnt lgkmcnt(8)\n"
"v_mfma_f32_16x16x32_bf16 v[136:139], a[172:175], a[16:19], v[136:139]\n"
"ds_read_b64_tr_b16 a[168:169], v6 offset:46112\n"
"ds_read_b64_tr_b16 a[170:171], v6 offset:50720\n"
"v_mfma_f32_16x16x32_bf16 v[140:143], a[172:175], a[20:23], v[140:143]\n"
"v_mul_u32_u24 v241, 48, v241\n"
"v_mul_u32_u24 v244, 862, v242\n"
"v_mfma_f32_16x16x32_bf16 v[144:147], a[144:147], a[16:19], v[144:147]\n"
"ds_read_b64_tr_b16 a[172:173], v6 offset:46144\n"
"ds_read_b64_tr_b16 a[174:175], v6 offset:50752\n"
"v_mfma_f32_16x16x32_bf16 v[148:151], a[144:147], a[20:23], v[148:151]\n"
"v_sub_u32 v241, v241, v244\n"
"v_mul_u32_u24 v244, 924, v243\n"
"v_mfma_f32_16x16x32_bf16 v[152:155], a[148:151], a[16:19], v[152:155]\n"
"ds_read_b64_tr_b16 a[144:145], v6 offset:46176\n"
"ds_read_b64_tr_b16 a[146:147], v6 offset:50784\n"
"v_mfma_f32_16x16x32_bf16 v[156:159], a[148:151], a[20:23], v[156:159]\n"
"buffer_load_dwordx4 v11, s[12:15], s61 offen offset:3072 lds\n"
"s_waitcnt lgkmcnt(8)\n"
"v_mfma_f32_16x16x32_bf16 v[160:163], a[152:155], a[16:19], v[160:163]\n"
"ds_read_b64_tr_b16 a[148:149], v6 offset:46208\n"
"ds_read_b64_tr_b16 a[150:151], v6 offset:50816\n"
"v_mfma_f32_16x16x32_bf16 v[164:167], a[152:155], a[20:23], v[164:167]\n"
"s_add_u32 m0, s60, 4096\n"
"v_add_u32 v241, v241, v244\n"
"v_mfma_f32_16x16x32_bf16 v[168:171], a[156:159], a[16:19], v[168:171]\n"
"ds_read_b64_tr_b16 a[152:153], v6 offset:46240\n"
"ds_read_b64_tr_b16 a[154:155], v6 offset:50848\n"
"v_mfma_f32_16x16x32_bf16 v[172:175], a[156:159], a[20:23], v[172:175]\n"
"v_add_u32 v246, 155648, v241\n"
"v_add_u32 v241, 256, v0\n"
"v_mfma_f32_16x16x32_bf16 v[176:179], a[160:163], a[16:19], v[176:179]\n"
"ds_read_b64_tr_b16 a[156:157], v6 offset:46272\n"
"ds_read_b64_tr_b16 a[158:159], v6 offset:50880\n"
"v_mfma_f32_16x16x32_bf16 v[180:183], a[160:163], a[20:23], v[180:183]\n"
"buffer_load_dwordx4 v12, s[12:15], s61 offen offset:0 lds\n"
"s_waitcnt lgkmcnt(8)\n"
"v_mfma_f32_16x16x32_bf16 v[112:115], a[164:167], a[24:27], v[112:115]\n"
"ds_read_b64_tr_b16 a[160:161], v6 offset:46304\n"
"ds_read_b64_tr_b16 a[162:163], v6 offset:50912\n"
"v_mfma_f32_16x16x32_bf16 v[116:119], a[164:167], a[28:31], v[116:119]\n"
"v_mul_u32_u24 v242, 911, v241\n"
"v_lshrrev_b32 v242, 14, v242\n"
"v_mfma_f32_16x16x32_bf16 v[120:123], a[168:171], a[24:27], v[120:123]\n"
"ds_read_b64_tr_b16 a[164:165], v6 offset:46336\n"
"ds_read_b64_tr_b16 a[166:167], v6 offset:50944\n"
"v_mfma_f32_16x16x32_bf16 v[124:127], a[168:171], a[28:31], v[124:127]\n"
"v_mul_u32_u24 v243, 911, v242\n"
"v_lshrrev_b32 v243, 14, v243\n"
"v_mfma_f32_16x16x32_bf16 v[128:131], a[172:175], a[24:27], v[128:131]\n"
"ds_read_b64_tr_b16 a[168:169], v6 offset:55296\n"
"ds_read_b64_tr_b16 a[170:171], v6 offset:59904\n"
"v_mfma_f32_16x16x32_bf16 v[132:135], a[172:175], a[28:31], v[132:135]\n"
"buffer_load_dwordx4 v13, s[12:15], s61 offen offset:1024 lds\n"
"s_waitcnt lgkmcnt(8)\n"
"v_mfma_f32_16x16x32_bf16 v[136:139], a[144:147], a[24:27], v[136:139]\n"
"ds_read_b64_tr_b16 a[172:173], v6 offset:55328\n"
"ds_read_b64_tr_b16 a[174:175], v6 offset:59936\n"
"v_mfma_f32_16x16x32_bf16 v[140:143], a[144:147], a[28:31], v[140:143]\n"
"v_mul_u32_u24 v241, 48, v241\n"
"v_mul_u32_u24 v244, 862, v242\n"
"v_mfma_f32_16x16x32_bf16 v[144:147], a[148:151], a[24:27], v[144:147]\n"
"ds_read_b64_tr_b16 a[144:145], v6 offset:55360\n"
"ds_read_b64_tr_b16 a[146:147], v6 offset:59968\n"
"v_mfma_f32_16x16x32_bf16 v[148:151], a[148:151], a[28:31], v[148:151]\n"
"v_sub_u32 v241, v241, v244\n"
"v_mul_u32_u24 v244, 924, v243\n"
"v_mfma_f32_16x16x32_bf16 v[152:155], a[152:155], a[24:27], v[152:155]\n"
"ds_read_b64_tr_b16 a[148:149], v6 offset:55392\n"
"ds_read_b64_tr_b16 a[150:151], v6 offset:60000\n"
"v_mfma_f32_16x16x32_bf16 v[156:159], a[152:155], a[28:31], v[156:159]\n"
"buffer_load_dwordx4 v14, s[12:15], s61 offen offset:2048 lds\n"
"s_waitcnt lgkmcnt(8)\n"
"v_mfma_f32_16x16x32_bf16 v[160:163], a[156:159], a[24:27], v[160:163]\n"
"ds_read_b64_tr_b16 a[152:153], v6 offset:55424\n"
"ds_read_b64_tr_b16 a[154:155], v6 offset:60032\n"
"v_mfma_f32_16x16x32_bf16 v[164:167], a[156:159], a[28:31], v[164:167]\n"
"v_add_u32 v241, v241, v244\n"
"v_add_u32 v247, 155648, v241\n"
"v_mfma_f32_16x16x32_bf16 v[168:171], a[160:163], a[24:27], v[168:171]\n"
"ds_read_b64_tr_b16 a[156:157], v6 offset:55456\n"
"ds_read_b64_tr_b16 a[158:159], v6 offset:60064\n"
"v_mfma_f32_16x16x32_bf16 v[172:175], a[160:163], a[28:31], v[172:175]\n"
"v_add_u32 v241, 512, v0\n"
"v_mul_u32_u24 v242, 911, v241\n"
"v_mfma_f32_16x16x32_bf16 v[176:179], a[164:167], a[24:27], v[176:179]\n"
"ds_read_b64_tr_b16 a[160:161], v6 offset:55488\n"
"ds_read_b64_tr_b16 a[162:163], v6 offset:60096\n"
"v_mfma_f32_16x16x32_bf16 v[180:183], a[164:167], a[28:31], v[180:183]\n"
"buffer_load_dwordx4 v15, s[12:15], s61 offen offset:3072 lds\n"
"s_waitcnt lgkmcnt(8)\n"
"v_mfma_f32_16x16x32_bf16 v[112:115], a[168:171], a[32:35], v[112:115]\n"
"ds_read_b64_tr_b16 a[164:165], v6 offset:55520\n"
"ds_read_b64_tr_b16 a[166:167], v6 offset:60128\n"
"v_mfma_f32_16x16x32_bf16 v[116:119], a[168:171], a[36:39], v[116:119]\n"
"s_add_u32 m0, s60, 8192\n"
"v_lshrrev_b32 v242, 14, v242\n"
"v_mfma_f32_16x16x32_bf16 v[120:123], a[172:175], a[32:35], v[120:123]\n"
"ds_read_b64_tr_b16 a[168:169], v6 offset:55552\n"
"ds_read_b64_tr_b16 a[170:171], v6 offset:60160\n"
"buffer_load_dwordx4 v[184:187], v2, s[16:19], s69 offen offset:0 sc1\n"
"v_mfma_f32_16x16x32_bf16 v[124:127], a[172:175], a[36:39], v[124:127]\n"
"v_mul_u32_u24 v243, 911, v242\n"
"v_lshrrev_b32 v243, 14, v243\n"
"v_mfma_f32_16x16x32_bf16 v[128:131], a[144:147], a[32:35], v[128:131]\n"
"ds_read_b64_tr_b16 a[172:173], v6 offset:64512\n"
"ds_read_b64_tr_b16 a[174:175], v7 offset:3584\n"
"v_mfma_f32_16x16x32_bf16 v[132:135], a[144:147], a[36:39], v[132:135]\n"
"buffer_load_dwordx4 v[188:191], v2, s[16:19], s69 offen offset:1024 sc1\n"
"s_waitcnt lgkmcnt(8)\n"
"v_mfma_f32_16x16x32_bf16 v[136:139], a[148:151], a[32:35], v[136:139]\n"
"ds_read_b64_tr_b16 a[144:145], v6 offset:64544\n"
"ds_read_b64_tr_b16 a[146:147], v7 offset:3616\n"
"v_mfma_f32_16x16x32_bf16 v[140:143], a[148:151], a[36:39], v[140:143]\n"
"v_mul_u32_u24 v241, 48, v241\n"
"v_mul_u32_u24 v244, 862, v242\n"
"v_mfma_f32_16x16x32_bf16 v[144:147], a[152:155], a[32:35], v[144:147]\n"
"ds_read_b64_tr_b16 a[148:149], v6 offset:64576\n"
"ds_read_b64_tr_b16 a[150:151], v7 offset:3648\n"
"buffer_load_dwordx4 v[192:195], v2, s[16:19], s69 offen offset:2048 sc1\n"
"v_mfma_f32_16x16x32_bf16 v[148:151], a[152:155], a[36:39], v[148:151]\n"
"v_sub_u32 v241, v241, v244\n"
"v_mul_u32_u24 v244, 924, v243\n"
"v_mfma_f32_16x16x32_bf16 v[152:155], a[156:159], a[32:35], v[152:155]\n"
"ds_read_b64_tr_b16 a[152:153], v6 offset:64608\n"
"ds_read_b64_tr_b16 a[154:155], v7 offset:3680\n"
"v_mfma_f32_16x16x32_bf16 v[156:159], a[156:159], a[36:39], v[156:159]\n"
"buffer_load_dwordx4 v[196:199], v2, s[16:19], s69 offen offset:3072 sc1\n"
"s_waitcnt lgkmcnt(8)\n"
"v_mfma_f32_16x16x32_bf16 v[160:163], a[160:163], a[32:35], v[160:163]\n"
"ds_read_b64_tr_b16 a[156:157], v6 offset:64640\n"
"ds_read_b64_tr_b16 a[158:159], v7 offset:3712\n"
"v_mfma_f32_16x16x32_bf16 v[164:167], a[160:163], a[36:39], v[164:167]\n"
"v_add_u32 v241, v241, v244\n"
"v_add_u32 v242, 30, v242\n"
"v_mfma_f32_16x16x32_bf16 v[168:171], a[164:167], a[32:35], v[168:171]\n"
"ds_read_b64_tr_b16 a[160:161], v6 offset:64672\n"
"ds_read_b64_tr_b16 a[162:163], v7 offset:3744\n"
"buffer_load_dwordx4 v[200:203], v3, s[16:19], s69 offen offset:0 sc1\n"
"v_mfma_f32_16x16x32_bf16 v[172:175], a[164:167], a[36:39], v[172:175]\n"
"v_lshrrev_b32 v242, 6, v242\n"
"v_lshl_add_u32 v241, v242, 2, v241\n"
"v_mfma_f32_16x16x32_bf16 v[176:179], a[168:171], a[32:35], v[176:179]\n"
"ds_read_b64_tr_b16 a[164:165], v6 offset:64704\n"
"ds_read_b64_tr_b16 a[166:167], v7 offset:3776\n"
"v_mfma_f32_16x16x32_bf16 v[180:183], a[168:171], a[36:39], v[180:183]\n"
"buffer_load_dwordx4 v[204:207], v3, s[16:19], s69 offen offset:1024 sc1\n"
"s_waitcnt lgkmcnt(8)\n"
"v_mfma_f32_16x16x32_bf16 v[112:115], a[172:175], a[40:43], v[112:115]\n"
"ds_read_b64_tr_b16 a[168:169], v6 offset:64736\n"
"ds_read_b64_tr_b16 a[170:171], v7 offset:3808\n"
"v_mfma_f32_16x16x32_bf16 v[116:119], a[172:175], a[44:47], v[116:119]\n"
"v_min_u32 v241, 1984, v241\n"
"v_add_u32 v248, 155648, v241\n"
"v_mfma_f32_16x16x32_bf16 v[120:123], a[144:147], a[40:43], v[120:123]\n"
"ds_read_b64_tr_b16 a[172:173], v6 offset:64768\n"
"ds_read_b64_tr_b16 a[174:175], v7 offset:3840\n"
"buffer_load_dwordx4 v[208:211], v3, s[16:19], s69 offen offset:2048 sc1\n"
"v_mfma_f32_16x16x32_bf16 v[124:127], a[144:147], a[44:47], v[124:127]\n"
"v_add_u32 v0, 0x20000, v2\n"
"s_lshl_b32 s20, s58, 2\n"
"v_mfma_f32_16x16x32_bf16 v[128:131], a[148:151], a[40:43], v[128:131]\n"
"s_add_u32 s20, s20, s54\n"
"s_mul_i32 s21, s20, 911\n"
"v_mfma_f32_16x16x32_bf16 v[132:135], a[148:151], a[44:47], v[132:135]\n"
"buffer_load_dwordx4 v[212:215], v3, s[16:19], s69 offen offset:3072 sc1\n"
"s_waitcnt lgkmcnt(6)\n"
"v_mfma_f32_16x16x32_bf16 v[136:139], a[152:155], a[40:43], v[136:139]\n"
"buffer_load_dword v34, v33, s[16:19], s71 offen sc1\n"
"buffer_load_dword v36, v35, s[16:19], s65 offen sc1\n"
"v_mfma_f32_16x16x32_bf16 v[140:143], a[152:155], a[44:47], v[140:143]\n"
"s_waitcnt vmcnt(34)\n"
"s_waitcnt lgkmcnt(0)\n"
"s_barrier\n"
"v_mfma_f32_16x16x32_bf16 v[144:147], a[156:159], a[40:43], v[144:147]\n"
"ds_read_b64_tr_b16 a[144:145], v7 offset:8192\n"
"ds_read_b64_tr_b16 a[146:147], v7 offset:12800\n"
"v_mfma_f32_16x16x32_bf16 v[148:151], a[156:159], a[44:47], v[148:151]\n"
"ds_read_b64_tr_b16 a[148:149], v7 offset:8224\n"
"ds_read_b64_tr_b16 a[150:151], v7 offset:12832\n"
"v_mfma_f32_16x16x32_bf16 v[152:155], a[160:163], a[40:43], v[152:155]\n"
"ds_read_b64_tr_b16 a[152:153], v7 offset:8256\n"
"ds_read_b64_tr_b16 a[154:155], v7 offset:12864\n"
"v_mfma_f32_16x16x32_bf16 v[156:159], a[160:163], a[44:47], v[156:159]\n"
"ds_read_b64_tr_b16 a[156:157], v7 offset:8288\n"
"ds_read_b64_tr_b16 a[158:159], v7 offset:12896\n"
"v_mfma_f32_16x16x32_bf16 v[160:163], a[164:167], a[40:43], v[160:163]\n"
"ds_read_b64_tr_b16 a[160:161], v7 offset:8320\n"
"ds_read_b64_tr_b16 a[162:163], v7 offset:12928\n"
"v_mfma_f32_16x16x32_bf16 v[164:167], a[164:167], a[44:47], v[164:167]\n"
"s_lshr_b32 s21, s21, 13\n"
"s_mul_i32 s22, s21, 9\n"
"v_mfma_f32_16x16x32_bf16 v[168:171], a[168:171], a[40:43], v[168:171]\n"
"ds_read_b64_tr_b16 a[164:165], v7 offset:8352\n"
"ds_read_b64_tr_b16 a[166:167], v7 offset:12960\n"
"v_mfma_f32_16x16x32_bf16 v[172:175], a[168:171], a[44:47], v[172:175]\n"
"s_sub_u32 s22, s20, s22\n"
"s_mul_i32 s23, s21, 43\n"
"v_mfma_f32_16x16x32_bf16 v[176:179], a[172:175], a[40:43], v[176:179]\n"
"ds_read_b64_tr_b16 a[168:169], v7 offset:8384\n"
"ds_read_b64_tr_b16 a[170:171], v7 offset:12992\n"
"v_mfma_f32_16x16x32_bf16 v[180:183], a[172:175], a[44:47], v[180:183]\n"
"s_lshr_b32 s23, s23, 8\n"
"s_waitcnt lgkmcnt(8)\n"
"v_mfma_f32_16x16x32_bf16 v[112:115], a[144:147], a[48:51], v[112:115]\n"
"ds_read_b64_tr_b16 a[172:173], v7 offset:8416\n"
"ds_read_b64_tr_b16 a[174:175], v7 offset:13024\n"
"v_mfma_f32_16x16x32_bf16 v[116:119], a[144:147], a[52:55], v[116:119]\n"
"s_mul_i32 s24, s23, 6\n"
"s_sub_u32 s24, s21, s24\n"
"v_mfma_f32_16x16x32_bf16 v[120:123], a[148:151], a[48:51], v[120:123]\n"
"ds_read_b64_tr_b16 a[144:145], v7 offset:8448\n"
"ds_read_b64_tr_b16 a[146:147], v7 offset:13056\n"
"v_mfma_f32_16x16x32_bf16 v[124:127], a[148:151], a[52:55], v[124:127]\n"
"buffer_load_dwordx4 v16, s[12:15], s61 offen offset:0 lds\n"
"s_lshl_b32 s25, s22, 7\n"
"v_mfma_f32_16x16x32_bf16 v[128:131], a[152:155], a[48:51], v[128:131]\n"
"ds_read_b64_tr_b16 a[148:149], v7 offset:17408\n"
"ds_read_b64_tr_b16 a[150:151], v7 offset:22016\n"
"v_mfma_f32_16x16x32_bf16 v[132:135], a[152:155], a[52:55], v[132:135]\n"
"v_and_b32 v240, 15, v1\n"
"s_waitcnt lgkmcnt(8)\n"
"v_mfma_f32_16x16x32_bf16 v[136:139], a[156:159], a[48:51], v[136:139]\n"
"ds_read_b64_tr_b16 a[152:153], v7 offset:17440\n"
"ds_read_b64_tr_b16 a[154:155], v7 offset:22048\n"
"v_mfma_f32_16x16x32_bf16 v[140:143], a[156:159], a[52:55], v[140:143]\n"
"v_lshl_add_u32 v240, v240, 3, s25\n"
"v_lshrrev_b32 v241, 5, v1\n"
"v_mfma_f32_16x16x32_bf16 v[144:147], a[160:163], a[48:51], v[144:147]\n"
"ds_read_b64_tr_b16 a[156:157], v7 offset:17472\n"
"ds_read_b64_tr_b16 a[158:159], v7 offset:22080\n"
"v_mfma_f32_16x16x32_bf16 v[148:151], a[160:163], a[52:55], v[148:151]\n"
"v_bfe_u32 v242, v1, 4, 1\n"
"s_mul_i32 s25, s23, 165888\n"
"v_mfma_f32_16x16x32_bf16 v[152:155], a[164:167], a[48:51], v[152:155]\n"
"ds_read_b64_tr_b16 a[160:161], v7 offset:17504\n"
"ds_read_b64_tr_b16 a[162:163], v7 offset:22112\n"
"v_mfma_f32_16x16x32_bf16 v[156:159], a[164:167], a[52:55], v[156:159]\n"
"s_mul_i32 s26, s24, 36864\n"
"s_waitcnt lgkmcnt(8)\n"
"v_mfma_f32_16x16x32_bf16 v[160:163], a[168:171], a[48:51], v[160:163]\n"
"ds_read_b64_tr_b16 a[164:165], v7 offset:17536\n"
"ds_read_b64_tr_b16 a[166:167], v7 offset:22144\n"
"v_mfma_f32_16x16x32_bf16 v[164:167], a[168:171], a[52:55], v[164:167]\n"
"s_add_u32 s25, s25, s26\n"
"s_mul_i32 s26, s23, 41472\n"
"v_mfma_f32_16x16x32_bf16 v[168:171], a[172:175], a[48:51], v[168:171]\n"
"ds_read_b64_tr_b16 a[168:169], v7 offset:17568\n"
"ds_read_b64_tr_b16 a[170:171], v7 offset:22176\n"
"v_mfma_f32_16x16x32_bf16 v[172:175], a[172:175], a[52:55], v[172:175]\n"
"s_mov_b32 s51, 0x20000\n"
"s_mov_b32 s27, 1073741824\n"
"v_mfma_f32_16x16x32_bf16 v[176:179], a[144:147], a[48:51], v[176:179]\n"
"ds_read_b64_tr_b16 a[172:173], v7 offset:17600\n"
"ds_read_b64_tr_b16 a[174:175], v7 offset:22208\n"
"v_mfma_f32_16x16x32_bf16 v[180:183], a[144:147], a[52:55], v[180:183]\n"
"s_mov_b32 s98, 9216\n"
"s_waitcnt lgkmcnt(8)\n"
"v_mfma_f32_16x16x32_bf16 v[112:115], a[148:151], a[56:59], v[112:115]\n"
"ds_read_b64_tr_b16 a[144:145], v7 offset:17632\n"
"ds_read_b64_tr_b16 a[146:147], v7 offset:22240\n"
"v_mfma_f32_16x16x32_bf16 v[116:119], a[148:151], a[60:63], v[116:119]\n"
"s_mov_b32 s99, 4608\n"
"v_mfma_f32_16x16x32_bf16 v[120:123], a[152:155], a[56:59], v[120:123]\n"
"ds_read_b64_tr_b16 a[148:149], v7 offset:17664\n"
"ds_read_b64_tr_b16 a[150:151], v7 offset:22272\n"
"v_mfma_f32_16x16x32_bf16 v[124:127], a[152:155], a[60:63], v[124:127]\n"
"s_cmp_eq_u32 s24, 5\n"
"s_cselect_b32 s25, s26, s25\n"
"s_cselect_b64 s[48:49], s[32:33], s[28:29]\n"
"v_mfma_f32_16x16x32_bf16 v[128:131], a[156:159], a[56:59], v[128:131]\n"
"ds_read_b64_tr_b16 a[152:153], v7 offset:26624\n"
"ds_read_b64_tr_b16 a[154:155], v7 offset:31232\n"
"v_mfma_f32_16x16x32_bf16 v[132:135], a[156:159], a[60:63], v[132:135]\n"
"s_waitcnt lgkmcnt(8)\n"
"v_mfma_f32_16x16x32_bf16 v[136:139], a[160:163], a[56:59], v[136:139]\n"
"ds_read_b64_tr_b16 a[156:157], v7 offset:26656\n"
"ds_read_b64_tr_b16 a[158:159], v7 offset:31264\n"
"v_mfma_f32_16x16x32_bf16 v[140:143], a[160:163], a[60:63], v[140:143]\n"
"s_cmp_eq_u32 s24, 5\n"
"s_cselect_b32 s50, s34, s30\n"
"s_cselect_b32 s27, 20736, s27\n"
"v_mfma_f32_16x16x32_bf16 v[144:147], a[164:167], a[56:59], v[144:147]\n"
"ds_read_b64_tr_b16 a[160:161], v7 offset:26688\n"
"ds_read_b64_tr_b16 a[162:163], v7 offset:31296\n"
"v_mfma_f32_16x16x32_bf16 v[148:151], a[164:167], a[60:63], v[148:151]\n"
"s_waitcnt vmcnt(10)\n"
"v_and_b32 v216, v185, v187\n"
"v_or3_b32 v217, v185, v187, v17\n"
"s_waitcnt vmcnt(9)\n"
"v_bitop3_b32 v216, v216, v189, v191 bitop3:0x80\n"
"v_or3_b32 v217, v217, v189, v191\n"
"v_mfma_f32_16x16x32_bf16 v[152:155], a[168:171], a[56:59], v[152:155]\n"
"ds_read_b64_tr_b16 a[164:165], v7 offset:26720\n"
"ds_read_b64_tr_b16 a[166:167], v7 offset:31328\n"
"s_waitcnt vmcnt(8)\n"
"v_bitop3_b32 v216, v216, v193, v195 bitop3:0x80\n"
"v_or3_b32 v217, v217, v193, v195\n"
"v_mfma_f32_16x16x32_bf16 v[156:159], a[168:171], a[60:63], v[156:159]\n"
"s_waitcnt vmcnt(2)\n"
"v_xor_b32 v34, s55, v34\n"
"v_bitop3_b32 v216, v216, v197, v199 bitop3:0x80\n"
"v_or3_b32 v217, v217, v197, v199\n"
"s_waitcnt lgkmcnt(8)\n"
"v_mfma_f32_16x16x32_bf16 v[160:163], a[172:175], a[56:59], v[160:163]\n"
"ds_read_b64_tr_b16 a[168:169], v7 offset:26752\n"
"ds_read_b64_tr_b16 a[170:171], v7 offset:31360\n"
"v_bitop3_b32 v216, v216, v201, v203 bitop3:0x80\n"
"v_or3_b32 v217, v217, v201, v203\n"
"v_mfma_f32_16x16x32_bf16 v[164:167], a[172:175], a[60:63], v[164:167]\n"
"s_waitcnt vmcnt(1)\n"
"v_xor_b32 v36, s55, v36\n"
"v_or_b32 v34, v34, v36\n"
"v_bitop3_b32 v216, v216, v205, v207 bitop3:0x80\n"
"v_or3_b32 v217, v217, v205, v207\n"
"v_mfma_f32_16x16x32_bf16 v[168:171], a[144:147], a[56:59], v[168:171]\n"
"ds_read_b64_tr_b16 a[172:173], v7 offset:26784\n"
"ds_read_b64_tr_b16 a[174:175], v7 offset:31392\n"
"v_bitop3_b32 v216, v216, v209, v211 bitop3:0x80\n"
"v_or3_b32 v217, v217, v209, v211\n"
"v_mfma_f32_16x16x32_bf16 v[172:175], a[144:147], a[60:63], v[172:175]\n"
"v_cmp_ne_u32 vcc, 0, v34\n"
"s_cbranch_vccnz .Lmk_SLOW_0\n"
".Lmk_BACK_0:\n"
"v_bitop3_b32 v216, v216, v213, v215 bitop3:0x80\n"
"v_or3_b32 v217, v217, v213, v215\n"
"v_mfma_f32_16x16x32_bf16 v[176:179], a[148:151], a[56:59], v[176:179]\n"
"ds_read_b64_tr_b16 a[144:145], v7 offset:26816\n"
"ds_read_b64_tr_b16 a[146:147], v7 offset:31424\n"
"v_and_b32 v216, v216, v17\n"
"v_mfma_f32_16x16x32_bf16 v[180:183], a[148:151], a[60:63], v[180:183]\n"
"s_mul_i32 s61, s53, 129024\n"
"v_cmp_ne_u32 vcc, v216, v217\n"
"s_cbranch_vccnz .Lmk_SLOW_1\n"
".Lmk_BACK_1:\n"
"s_waitcnt lgkmcnt(8)\n"
"v_mfma_f32_16x16x32_bf16 v[112:115], a[152:155], a[64:67], v[112:115]\n"
"ds_read_b64_tr_b16 a[148:149], v7 offset:26848\n"
"ds_read_b64_tr_b16 a[150:151], v7 offset:31456\n"
"v_add_f32 v184, v184, v188\n"
"v_add_f32 v186, v186, v190\n"
"v_mfma_f32_16x16x32_bf16 v[116:119], a[152:155], a[68:71], v[116:119]\n"
"v_add_f32 v184, v184, v192\n"
"v_add_f32 v186, v186, v194\n"
"v_add_f32 v184, v184, v196\n"
"v_add_f32 v186, v186, v198\n"
"v_mfma_f32_16x16x32_bf16 v[120:123], a[156:159], a[64:67], v[120:123]\n"
"ds_read_b64_tr_b16 a[152:153], v7 offset:26880\n"
"ds_read_b64_tr_b16 a[154:155], v7 offset:31488\n"
"v_add_f32 v184, v184, v200\n"
"v_add_f32 v186, v186, v202\n"
"v_mfma_f32_16x16x32_bf16 v[124:127], a[156:159], a[68:71], v[124:127]\n"
"v_add_f32 v184, v184, v204\n"
"v_add_f32 v186, v186, v206\n"
"v_add_f32 v184, v184, v208\n"
"v_add_f32 v186, v186, v210\n"
"v_mfma_f32_16x16x32_bf16 v[128:131], a[160:163], a[64:67], v[128:131]\n"
"ds_read_b64_tr_b16 a[156:157], v7 offset:35840\n"
"ds_read_b64_tr_b16 a[158:159], v7 offset:40448\n"
"v_add_f32 v184, v184, v212\n"
"v_add_f32 v186, v186, v214\n"
"v_mfma_f32_16x16x32_bf16 v[132:135], a[160:163], a[68:71], v[132:135]\n"
"v_mov_b32 v185, v184\n"
"v_mov_b32 v187, v186\n"
"s_lshl_b32 s72, s54, 8\n"
"s_waitcnt lgkmcnt(8)\n"
"v_mfma_f32_16x16x32_bf16 v[136:139], a[164:167], a[64:67], v[136:139]\n"
"ds_read_b64_tr_b16 a[160:161], v7 offset:35872\n"
"ds_read_b64_tr_b16 a[162:163], v7 offset:40480\n"
"s_add_u32 s72, s72, 162304\n"
"v_mfma_f32_16x16x32_bf16 v[140:143], a[164:167], a[68:71], v[140:143]\n"
"v_permlane32_swap_b32 v184, v185\n"
"v_permlane32_swap_b32 v186, v187\n"
"v_mfma_f32_16x16x32_bf16 v[144:147], a[168:171], a[64:67], v[144:147]\n"
"ds_read_b64_tr_b16 a[164:165], v7 offset:35904\n"
"ds_read_b64_tr_b16 a[166:167], v7 offset:40512\n"
"v_add_f32 v184, v184, v185\n"
"v_add_f32 v186, v186, v187\n"
"v_mfma_f32_16x16x32_bf16 v[148:151], a[168:171], a[68:71], v[148:151]\n"
"v_mul_f32 v184, 0x39e38e39, v184\n"
"v_mul_f32 v186, 0x39e38e39, v186\n"
"v_mul_f32 v185, v184, v184\n"
"v_lshl_add_u32 v188, v1, 2, s72\n"
"v_mfma_f32_16x16x32_bf16 v[152:155], a[172:175], a[64:67], v[152:155]\n"
"ds_read_b64_tr_b16 a[168:169], v7 offset:35936\n"
"ds_read_b64_tr_b16 a[170:171], v7 offset:40544\n"
"v_subrev_f32 v185, 0x3727c5ac, v185\n"
"v_lshrrev_b32 v189, 4, v1\n"
"v_mfma_f32_16x16x32_bf16 v[156:159], a[172:175], a[68:71], v[156:159]\n"
"v_sub_f32 v186, v186, v185\n"
"v_lshl_add_u32 v189, v189, 5, s72\n"
"v_max_f32 v186, 0x3727c5ac, v186\n"
"s_waitcnt lgkmcnt(8)\n"
"v_mfma_f32_16x16x32_bf16 v[160:163], a[144:147], a[64:67], v[160:163]\n"
"ds_read_b64_tr_b16 a[172:173], v7 offset:35968\n"
"ds_read_b64_tr_b16 a[174:175], v7 offset:40576\n"
"s_mov_b32 s86, 0\n"
"s_mov_b32 s87, -1\n"
"v_mfma_f32_16x16x32_bf16 v[164:167], a[144:147], a[68:71], v[164:167]\n"
"v_rsq_f32 v186, v186\n"
"s_mul_i32 s73, s53, 589824\n"
"v_mul_f32 v187, -v184, v186\n"
"v_mfma_f32_16x16x32_bf16 v[168:171], a[148:151], a[64:67], v[168:171]\n"
"ds_read_b64_tr_b16 a[144:145], v7 offset:36000\n"
"ds_read_b64_tr_b16 a[146:147], v7 offset:40608\n"
"s_mul_i32 s74, s63, 9216\n"
"v_cndmask_b32 v186, v186, v187, s[86:87]\n"
"v_mfma_f32_16x16x32_bf16 v[172:175], a[148:151], a[68:71], v[172:175]\n"
"s_add_u32 s73, s73, s74\n"
"ds_write_b32 v188, v186\n"
"ds_read_b128 v[216:219], v189 offset:0\n"
"ds_read_b128 v[224:227], v189 offset:128\n"
"v_mfma_f32_16x16x32_bf16 v[176:179], a[152:155], a[64:67], v[176:179]\n"
"ds_read_b64_tr_b16 a[148:149], v7 offset:36032\n"
"ds_read_b64_tr_b16 a[150:151], v7 offset:40640\n"
"ds_read_b128 v[220:223], v189 offset:16\n"
"ds_read_b128 v[228:231], v189 offset:144\n"
"v_mfma_f32_16x16x32_bf16 v[180:183], a[152:155], a[68:71], v[180:183]\n"
"s_add_u32 s73, s73, 17825792\n"
"s_waitcnt lgkmcnt(13)\n"
"v_mfma_f32_16x16x32_bf16 v[112:115], a[156:159], a[72:75], v[112:115]\n"
"ds_read_b64_tr_b16 a[152:153], v7 offset:36064\n"
"ds_read_b64_tr_b16 a[154:155], v7 offset:40672\n"
"v_mfma_f32_16x16x32_bf16 v[116:119], a[156:159], a[76:79], v[116:119]\n"
"s_add_u32 s61, s61, s60\n"
"s_add_u32 s61, s61, 3145728\n"
"v_mfma_f32_16x16x32_bf16 v[120:123], a[160:163], a[72:75], v[120:123]\n"
"ds_read_b64_tr_b16 a[156:157], v7 offset:36096\n"
"ds_read_b64_tr_b16 a[158:159], v7 offset:40704\n"
"v_mfma_f32_16x16x32_bf16 v[124:127], a[160:163], a[76:79], v[124:127]\n"
"s_add_u32 m0, s60, 36864\n"
"s_nop 0\n"
"buffer_load_dwordx4 v2, s[16:19], s61 offen offset:0 sc1 lds\n"
"v_mfma_f32_16x16x32_bf16 v[128:131], a[164:167], a[72:75], v[128:131]\n"
"s_waitcnt lgkmcnt(8)\n"
"v_fma_f32 v40, v40, v216, v224\n"
"v_fma_f32 v41, v41, v217, v225\n"
"v_fma_f32 v42, v42, v218, v226\n"
"v_fma_f32 v43, v43, v219, v227\n"
"v_mfma_f32_16x16x32_bf16 v[132:135], a[164:167], a[76:79], v[132:135]\n"
"s_waitcnt lgkmcnt(4)\n"
"v_fma_f32 v44, v44, v220, v228\n"
"v_fma_f32 v45, v45, v221, v229\n"
"v_fma_f32 v46, v46, v222, v230\n"
"v_mfma_f32_16x16x32_bf16 v[136:139], a[168:171], a[72:75], v[136:139]\n"
"v_fma_f32 v47, v47, v223, v231\n"
"v_cvt_pk_bf16_f32 v232, v40, v41\n"
"v_cvt_pk_bf16_f32 v233, v42, v43\n"
"v_cvt_pk_bf16_f32 v234, v44, v45\n"
"v_mfma_f32_16x16x32_bf16 v[140:143], a[168:171], a[76:79], v[140:143]\n"
"s_waitcnt lgkmcnt(0)\n"
"s_barrier\n"
"v_cvt_pk_bf16_f32 v235, v46, v47\n"
"v_mfma_f32_16x16x32_bf16 v[144:147], a[172:175], a[72:75], v[144:147]\n"
"ds_read_b64_tr_b16 a[160:161], v7 offset:45056\n"
"ds_read_b64_tr_b16 a[162:163], v7 offset:49664\n"
"buffer_store_dwordx4 v[232:235], v2, s[16:19], s73 offen offset:0 sc1\n"
"v_fma_f32 v48, v48, v216, v224\n"
"v_mfma_f32_16x16x32_bf16 v[148:151], a[172:175], a[76:79], v[148:151]\n"
"ds_read_b64_tr_b16 a[164:165], v7 offset:45088\n"
"ds_read_b64_tr_b16 a[166:167], v7 offset:49696\n"
"v_fma_f32 v49, v49, v217, v225\n"
"v_fma_f32 v50, v50, v218, v226\n"
"v_mfma_f32_16x16x32_bf16 v[152:155], a[144:147], a[72:75], v[152:155]\n"
"ds_read_b64_tr_b16 a[168:169], v7 offset:45120\n"
"ds_read_b64_tr_b16 a[170:171], v7 offset:49728\n"
"v_fma_f32 v51, v51, v219, v227\n"
"v_fma_f32 v52, v52, v220, v228\n"
"v_mfma_f32_16x16x32_bf16 v[156:159], a[144:147], a[76:79], v[156:159]\n"
"ds_read_b64_tr_b16 a[172:173], v7 offset:45152\n"
"ds_read_b64_tr_b16 a[174:175], v7 offset:49760\n"
"v_fma_f32 v53, v53, v221, v229\n"
"v_mfma_f32_16x16x32_bf16 v[160:163], a[148:151], a[72:75], v[160:163]\n"
"ds_read_b64_tr_b16 a[144:145], v7 offset:45184\n"
"ds_read_b64_tr_b16 a[146:147], v7 offset:49792\n"
"v_fma_f32 v54, v54, v222, v230\n"
"v_fma_f32 v55, v55, v223, v231\n"
"v_mfma_f32_16x16x32_bf16 v[164:167], a[148:151], a[76:79], v[164:167]\n"
"v_cvt_pk_bf16_f32 v236, v48, v49\n"
"v_cvt_pk_bf16_f32 v237, v50, v51\n"
"v_cvt_pk_bf16_f32 v238, v52, v53\n"
"v_cvt_pk_bf16_f32 v239, v54, v55\n"
"v_mfma_f32_16x16x32_bf16 v[168:171], a[152:155], a[72:75], v[168:171]\n"
"ds_read_b64_tr_b16 a[148:149], v7 offset:45216\n"
"ds_read_b64_tr_b16 a[150:151], v7 offset:49824\n"
"buffer_store_dwordx4 v[236:239], v2, s[16:19], s73 offen offset:1024 sc1\n"
"v_fma_f32 v56, v56, v216, v224\n"
"v_mfma_f32_16x16x32_bf16 v[172:175], a[152:155], a[76:79], v[172:175]\n"
"v_fma_f32 v57, v57, v217, v225\n"
"v_fma_f32 v58, v58, v218, v226\n"
"v_fma_f32 v59, v59, v219, v227\n"
"v_fma_f32 v60, v60, v220, v228\n"
"v_mfma_f32_16x16x32_bf16 v[176:179], a[156:159], a[72:75], v[176:179]\n"
"ds_read_b64_tr_b16 a[152:153], v7 offset:45248\n"
"ds_read_b64_tr_b16 a[154:155], v7 offset:49856\n"
"v_fma_f32 v61, v61, v221, v229\n"
"v_fma_f32 v62, v62, v222, v230\n"
"v_mfma_f32_16x16x32_bf16 v[180:183], a[156:159], a[76:79], v[180:183]\n"
"v_fma_f32 v63, v63, v223, v231\n"
"v_cvt_pk_bf16_f32 v232, v56, v57\n"
"v_cvt_pk_bf16_f32 v233, v58, v59\n"
"s_waitcnt lgkmcnt(8)\n"
"v_mfma_f32_16x16x32_bf16 v[112:115], a[160:163], a[80:83], v[112:115]\n"
"ds_read_b64_tr_b16 a[156:157], v7 offset:45280\n"
"ds_read_b64_tr_b16 a[158:159], v7 offset:49888\n"
"v_cvt_pk_bf16_f32 v234, v60, v61\n"
"v_cvt_pk_bf16_f32 v235, v62, v63\n"
"v_mfma_f32_16x16x32_bf16 v[116:119], a[160:163], a[84:87], v[116:119]\n"
"buffer_store_dwordx4 v[232:235], v2, s[16:19], s73 offen offset:2048 sc1\n"
"v_fma_f32 v64, v64, v216, v224\n"
"v_fma_f32 v65, v65, v217, v225\n"
"v_fma_f32 v66, v66, v218, v226\n"
"v_mfma_f32_16x16x32_bf16 v[120:123], a[164:167], a[80:83], v[120:123]\n"
"ds_read_b64_tr_b16 a[160:161], v7 offset:45312\n"
"ds_read_b64_tr_b16 a[162:163], v7 offset:49920\n"
"v_fma_f32 v67, v67, v219, v227\n"
"v_fma_f32 v68, v68, v220, v228\n"
"v_mfma_f32_16x16x32_bf16 v[124:127], a[164:167], a[84:87], v[124:127]\n"
"v_fma_f32 v69, v69, v221, v229\n"
"v_fma_f32 v70, v70, v222, v230\n"
"v_fma_f32 v71, v71, v223, v231\n"
"v_cvt_pk_bf16_f32 v236, v64, v65\n"
"v_mfma_f32_16x16x32_bf16 v[128:131], a[168:171], a[80:83], v[128:131]\n"
"ds_read_b64_tr_b16 a[164:165], v7 offset:54272\n"
"ds_read_b64_tr_b16 a[166:167], v7 offset:58880\n"
"v_cvt_pk_bf16_f32 v237, v66, v67\n"
"v_cvt_pk_bf16_f32 v238, v68, v69\n"
"v_mfma_f32_16x16x32_bf16 v[132:135], a[168:171], a[84:87], v[132:135]\n"
"v_cvt_pk_bf16_f32 v239, v70, v71\n"
"s_waitcnt lgkmcnt(8)\n"
"v_mfma_f32_16x16x32_bf16 v[136:139], a[172:175], a[80:83], v[136:139]\n"
"ds_read_b64_tr_b16 a[168:169], v7 offset:54304\n"
"ds_read_b64_tr_b16 a[170:171], v7 offset:58912\n"
"buffer_store_dwordx4 v[236:239], v2, s[16:19], s73 offen offset:3072 sc1\n"
"v_fma_f32 v72, v72, v216, v224\n"
"v_mfma_f32_16x16x32_bf16 v[140:143], a[172:175], a[84:87], v[140:143]\n"
"v_fma_f32 v73, v73, v217, v225\n"
"v_fma_f32 v74, v74, v218, v226\n"
"v_fma_f32 v75, v75, v219, v227\n"
"v_fma_f32 v76, v76, v220, v228\n"
"v_mfma_f32_16x16x32_bf16 v[144:147], a[144:147], a[80:83], v[144:147]\n"
"ds_read_b64_tr_b16 a[172:173], v7 offset:54336\n"
"ds_read_b64_tr_b16 a[174:175], v7 offset:58944\n"
"v_fma_f32 v77, v77, v221, v229\n"
"v_fma_f32 v78, v78, v222, v230\n"
"v_mfma_f32_16x16x32_bf16 v[148:151], a[144:147], a[84:87], v[148:151]\n"
"v_fma_f32 v79, v79, v223, v231\n"
"v_cvt_pk_bf16_f32 v232, v72, v73\n"
"v_cvt_pk_bf16_f32 v233, v74, v75\n"
"v_cvt_pk_bf16_f32 v234, v76, v77\n"
"v_mfma_f32_16x16x32_bf16 v[152:155], a[148:151], a[80:83], v[152:155]\n"
"ds_read_b64_tr_b16 a[144:145], v7 offset:54368\n"
"ds_read_b64_tr_b16 a[146:147], v7 offset:58976\n"
"v_cvt_pk_bf16_f32 v235, v78, v79\n"
"v_mfma_f32_16x16x32_bf16 v[156:159], a[148:151], a[84:87], v[156:159]\n"
"buffer_store_dwordx4 v[232:235], v3, s[16:19], s73 offen offset:0 sc1\n"
"v_fma_f32 v80, v80, v216, v224\n"
"v_fma_f32 v81, v81, v217, v225\n"
"s_waitcnt lgkmcnt(8)\n"
"v_mfma_f32_16x16x32_bf16 v[160:163], a[152:155], a[80:83], v[160:163]\n"
"ds_read_b64_tr_b16 a[148:149], v7 offset:54400\n"
"ds_read_b64_tr_b16 a[150:151], v7 offset:59008\n"
"v_fma_f32 v82, v82, v218, v226\n"
"v_fma_f32 v83, v83, v219, v227\n"
"v_mfma_f32_16x16x32_bf16 v[164:167], a[152:155], a[84:87], v[164:167]\n"
"v_fma_f32 v84, v84, v220, v228\n"
"v_fma_f32 v85, v85, v221, v229\n"
"v_fma_f32 v86, v86, v222, v230\n"
"v_fma_f32 v87, v87, v223, v231\n"
"v_mfma_f32_16x16x32_bf16 v[168:171], a[156:159], a[80:83], v[168:171]\n"
"ds_read_b64_tr_b16 a[152:153], v7 offset:54432\n"
"ds_read_b64_tr_b16 a[154:155], v7 offset:59040\n"
"v_cvt_pk_bf16_f32 v236, v80, v81\n"
"v_cvt_pk_bf16_f32 v237, v82, v83\n"
"v_mfma_f32_16x16x32_bf16 v[172:175], a[156:159], a[84:87], v[172:175]\n"
"v_cvt_pk_bf16_f32 v238, v84, v85\n"
"v_cvt_pk_bf16_f32 v239, v86, v87\n"
"v_mfma_f32_16x16x32_bf16 v[176:179], a[160:163], a[80:83], v[176:179]\n"
"ds_read_b64_tr_b16 a[156:157], v7 offset:54464\n"
"ds_read_b64_tr_b16 a[158:159], v7 offset:59072\n"
"buffer_store_dwordx4 v[236:239], v3, s[16:19], s73 offen offset:1024 sc1\n"
"v_fma_f32 v88, v88, v216, v224\n"
"v_mfma_f32_16x16x32_bf16 v[180:183], a[160:163], a[84:87], v[180:183]\n"
"v_fma_f32 v89, v89, v217, v225\n"
"v_fma_f32 v90, v90, v218, v226\n"
"v_fma_f32 v91, v91, v219, v227\n"
"s_waitcnt lgkmcnt(8)\n"
"v_mfma_f32_16x16x32_bf16 v[112:115], a[164:167], a[88:91], v[112:115]\n"
"ds_read_b64_tr_b16 a[160:161], v7 offset:54496\n"
"ds_read_b64_tr_b16 a[162:163], v7 offset:59104\n"
"v_fma_f32 v92, v92, v220, v228\n"
"v_fma_f32 v93, v93, v221, v229\n"
"v_mfma_f32_16x16x32_bf16 v[116:119], a[164:167], a[92:95], v[116:119]\n"
"v_fma_f32 v94, v94, v222, v230\n"
"v_fma_f32 v95, v95, v223, v231\n"
"v_cvt_pk_bf16_f32 v232, v88, v89\n"
"v_cvt_pk_bf16_f32 v233, v90, v91\n"
"v_mfma_f32_16x16x32_bf16 v[120:123], a[168:171], a[88:91], v[120:123]\n"
"ds_read_b64_tr_b16 a[164:165], v7 offset:54528\n"
"ds_read_b64_tr_b16 a[166:167], v7 offset:59136\n"
"v_cvt_pk_bf16_f32 v234, v92, v93\n"
"v_cvt_pk_bf16_f32 v235, v94, v95\n"
"v_mfma_f32_16x16x32_bf16 v[124:127], a[168:171], a[92:95], v[124:127]\n"
"buffer_store_dwordx4 v[232:235], v3, s[16:19], s73 offen offset:2048 sc1\n"
"v_fma_f32 v96, v96, v216, v224\n"
"v_fma_f32 v97, v97, v217, v225\n"
"v_fma_f32 v98, v98, v218, v226\n"
"v_mfma_f32_16x16x32_bf16 v[128:131], a[172:175], a[88:91], v[128:131]\n"
"ds_read_b64_tr_b16 a[168:169], v7 offset:63488\n"
"ds_read_b64_tr_b16 a[170:171], v22 offset:2560\n"
"v_fma_f32 v99, v99, v219, v227\n"
"v_fma_f32 v100, v100, v220, v228\n"
"v_mfma_f32_16x16x32_bf16 v[132:135], a[172:175], a[92:95], v[132:135]\n"
"v_fma_f32 v101, v101, v221, v229\n"
"v_fma_f32 v102, v102, v222, v230\n"
"v_fma_f32 v103, v103, v223, v231\n"
"s_waitcnt lgkmcnt(8)\n"
"v_mfma_f32_16x16x32_bf16 v[136:139], a[144:147], a[88:91], v[136:139]\n"
"ds_read_b64_tr_b16 a[172:173], v7 offset:63520\n"
"ds_read_b64_tr_b16 a[174:175], v22 offset:2592\n"
"v_cvt_pk_bf16_f32 v236, v96, v97\n"
"v_cvt_pk_bf16_f32 v237, v98, v99\n"
"v_mfma_f32_16x16x32_bf16 v[140:143], a[144:147], a[92:95], v[140:143]\n"
"v_cvt_pk_bf16_f32 v238, v100, v101\n"
"v_cvt_pk_bf16_f32 v239, v102, v103\n"
"v_mfma_f32_16x16x32_bf16 v[144:147], a[148:151], a[88:91], v[144:147]\n"
"ds_read_b64_tr_b16 a[144:145], v7 offset:63552\n"
"ds_read_b64_tr_b16 a[146:147], v22 offset:2624\n"
"buffer_store_dwordx4 v[236:239], v3, s[16:19], s73 offen offset:3072 sc1\n"
"v_fma_f32 v104, v104, v216, v224\n"
"v_mfma_f32_16x16x32_bf16 v[148:151], a[148:151], a[92:95], v[148:151]\n"
"v_fma_f32 v105, v105, v217, v225\n"
"v_fma_f32 v106, v106, v218, v226\n"
"v_fma_f32 v107, v107, v219, v227\n"
"v_fma_f32 v108, v108, v220, v228\n"
"v_mfma_f32_16x16x32_bf16 v[152:155], a[152:155], a[88:91], v[152:155]\n"
"ds_read_b64_tr_b16 a[148:149], v7 offset:63584\n"
"ds_read_b64_tr_b16 a[150:151], v22 offset:2656\n"
"v_fma_f32 v109, v109, v221, v229\n"
"v_fma_f32 v110, v110, v222, v230\n"
"v_mfma_f32_16x16x32_bf16 v[156:159], a[152:155], a[92:95], v[156:159]\n"
"v_fma_f32 v111, v111, v223, v231\n"
"v_cvt_pk_bf16_f32 v232, v104, v105\n"
"v_cvt_pk_bf16_f32 v233, v106, v107\n"
"s_waitcnt lgkmcnt(8)\n"
"v_mfma_f32_16x16x32_bf16 v[160:163], a[156:159], a[88:91], v[160:163]\n"
"ds_read_b64_tr_b16 a[152:153], v7 offset:63616\n"
"ds_read_b64_tr_b16 a[154:155], v22 offset:2688\n"
"v_cvt_pk_bf16_f32 v234, v108, v109\n"
"v_cvt_pk_bf16_f32 v235, v110, v111\n"
"v_mfma_f32_16x16x32_bf16 v[164:167], a[156:159], a[92:95], v[164:167]\n"
"buffer_store_dwordx4 v[232:235], v4, s[16:19], s73 offen offset:0 sc1\n"
"s_add_u32 s68, s67, 16384\n"
"v_mfma_f32_16x16x32_bf16 v[168:171], a[160:163], a[88:91], v[168:171]\n"
"ds_read_b64_tr_b16 a[156:157], v7 offset:63648\n"
"ds_read_b64_tr_b16 a[158:159], v22 offset:2720\n"
"v_mfma_f32_16x16x32_bf16 v[172:175], a[160:163], a[92:95], v[172:175]\n"
"s_cmp_lt_u32 s24, 4\n"
"s_cselect_b32 s27, 9216, s27\n"
"s_cselect_b32 s98, 4608, s98\n"
"s_cselect_b32 s99, 18432, s99\n"
"v_mfma_f32_16x16x32_bf16 v[176:179], a[164:167], a[88:91], v[176:179]\n"
"ds_read_b64_tr_b16 a[160:161], v7 offset:63680\n"
"ds_read_b64_tr_b16 a[162:163], v22 offset:2752\n"
"v_mfma_f32_16x16x32_bf16 v[180:183], a[164:167], a[92:95], v[180:183]\n"
"v_mul_lo_u32 v243, v241, s27\n"
"s_waitcnt lgkmcnt(8)\n"
"v_mfma_f32_16x16x32_bf16 v[112:115], a[168:171], a[96:99], v[112:115]\n"
"ds_read_b64_tr_b16 a[164:165], v7 offset:63712\n"
"ds_read_b64_tr_b16 a[166:167], v22 offset:2784\n"
"v_mfma_f32_16x16x32_bf16 v[116:119], a[168:171], a[100:103], v[116:119]\n"
"buffer_load_dwordx4 v2, s[16:19], s61 offen offset:1024 sc1 lds\n"
"v_mul_lo_u32 v244, v242, s98\n"
"v_mfma_f32_16x16x32_bf16 v[120:123], a[172:175], a[96:99], v[120:123]\n"
"ds_read_b64_tr_b16 a[168:169], v7 offset:63744\n"
"ds_read_b64_tr_b16 a[170:171], v22 offset:2816\n"
"v_mfma_f32_16x16x32_bf16 v[124:127], a[172:175], a[100:103], v[124:127]\n"
"v_add3_u32 v243, v243, v244, v240\n"
"v_add_u32 v29, s25, v243\n"
"v_mfma_f32_16x16x32_bf16 v[128:131], a[144:147], a[96:99], v[128:131]\n"
"ds_read_b64_tr_b16 a[172:173], v22 offset:7168\n"
"ds_read_b64_tr_b16 a[174:175], v22 offset:11776\n"
"v_mfma_f32_16x16x32_bf16 v[132:135], a[144:147], a[100:103], v[132:135]\n"
"buffer_load_dwordx4 v2, s[16:19], s61 offen offset:2048 sc1 lds\n"
"s_waitcnt lgkmcnt(8)\n"
"v_mfma_f32_16x16x32_bf16 v[136:139], a[148:151], a[96:99], v[136:139]\n"
"ds_read_b64_tr_b16 a[144:145], v22 offset:7200\n"
"ds_read_b64_tr_b16 a[146:147], v22 offset:11808\n"
"v_mfma_f32_16x16x32_bf16 v[140:143], a[148:151], a[100:103], v[140:143]\n"
"v_add_u32 v30, s99, v29\n"
"v_mul_u32_u24 v243, 20736, v242\n"
"v_mfma_f32_16x16x32_bf16 v[144:147], a[152:155], a[96:99], v[144:147]\n"
"ds_read_b64_tr_b16 a[148:149], v22 offset:7232\n"
"ds_read_b64_tr_b16 a[150:151], v22 offset:11840\n"
"v_mfma_f32_16x16x32_bf16 v[148:151], a[152:155], a[100:103], v[148:151]\n"
"buffer_load_dwordx4 v2, s[16:19], s61 offen offset:3072 sc1 lds\n"
"s_add_u32 m0, s60, 40960\n"
"v_mfma_f32_16x16x32_bf16 v[152:155], a[156:159], a[96:99], v[152:155]\n"
"ds_read_b64_tr_b16 a[152:153], v22 offset:7264\n"
"ds_read_b64_tr_b16 a[154:155], v22 offset:11872\n"
"v_mfma_f32_16x16x32_bf16 v[156:159], a[156:159], a[100:103], v[156:159]\n"
"v_add3_u32 v243, v243, v240, s26\n"
"s_waitcnt lgkmcnt(8)\n"
"v_mfma_f32_16x16x32_bf16 v[160:163], a[160:163], a[96:99], v[160:163]\n"
"ds_read_b64_tr_b16 a[156:157], v22 offset:7296\n"
"ds_read_b64_tr_b16 a[158:159], v22 offset:11904\n"
"v_mfma_f32_16x16x32_bf16 v[164:167], a[160:163], a[100:103], v[164:167]\n"
"buffer_load_dwordx4 v3, s[16:19], s61 offen offset:0 sc1 lds\n"
"v_add_u32 v243, 18432, v243\n"
"v_mfma_f32_16x16x32_bf16 v[168:171], a[164:167], a[96:99], v[168:171]\n"
"ds_read_b64_tr_b16 a[160:161], v22 offset:7328\n"
"ds_read_b64_tr_b16 a[162:163], v22 offset:11936\n"
"v_mfma_f32_16x16x32_bf16 v[172:175], a[164:167], a[100:103], v[172:175]\n"
"v_mov_b32 v244, 1073741824\n"
"s_mov_b32 s26, 0\n"
"v_mfma_f32_16x16x32_bf16 v[176:179], a[168:171], a[96:99], v[176:179]\n"
"ds_read_b64_tr_b16 a[164:165], v22 offset:7360\n"
"ds_read_b64_tr_b16 a[166:167], v22 offset:11968\n"
"v_mfma_f32_16x16x32_bf16 v[180:183], a[168:171], a[100:103], v[180:183]\n"
"buffer_load_dwordx4 v3, s[16:19], s61 offen offset:1024 sc1 lds\n"
"s_waitcnt lgkmcnt(8)\n"
"v_mfma_f32_16x16x32_bf16 v[112:115], a[172:175], a[104:107], v[112:115]\n"
"ds_read_b64_tr_b16 a[168:169], v22 offset:7392\n"
"ds_read_b64_tr_b16 a[170:171], v22 offset:12000\n"
"v_mfma_f32_16x16x32_bf16 v[116:119], a[172:175], a[108:111], v[116:119]\n"
"s_cmp_eq_u32 s24, 4\n"
"s_cselect_b32 s27, -1, 0\n"
"v_mfma_f32_16x16x32_bf16 v[120:123], a[144:147], a[104:107], v[120:123]\n"
"ds_read_b64_tr_b16 a[172:173], v22 offset:7424\n"
"ds_read_b64_tr_b16 a[174:175], v22 offset:12032\n"
"v_mfma_f32_16x16x32_bf16 v[124:127], a[144:147], a[108:111], v[124:127]\n"
"buffer_load_dwordx4 v3, s[16:19], s61 offen offset:2048 sc1 lds\n"
"v_cndmask_b32 v31, v244, v243, s[26:27]\n"
"v_mfma_f32_16x16x32_bf16 v[128:131], a[148:151], a[104:107], v[128:131]\n"
"s_cmp_lt_u32 s20, 864\n"
"s_cselect_b32 s20, 0, 1073741824\n"
"v_mfma_f32_16x16x32_bf16 v[132:135], a[148:151], a[108:111], v[132:135]\n"
"v_add_u32 v32, s20, v2\n"
"s_waitcnt lgkmcnt(6)\n"
"v_mfma_f32_16x16x32_bf16 v[136:139], a[152:155], a[104:107], v[136:139]\n"
"buffer_load_dwordx4 v3, s[16:19], s61 offen offset:3072 sc1 lds\n"
"s_add_u32 m0, s60, 45056\n"
"v_mfma_f32_16x16x32_bf16 v[140:143], a[152:155], a[108:111], v[140:143]\n"
"s_waitcnt vmcnt(17)\n"
"s_waitcnt lgkmcnt(0)\n"
"s_barrier\n"
"v_mfma_f32_16x16x32_bf16 v[144:147], a[156:159], a[104:107], v[144:147]\n"
"ds_read_b64_tr_b16 a[144:145], v6 offset:0\n"
"ds_read_b64_tr_b16 a[146:147], v6 offset:4608\n"
"v_mfma_f32_16x16x32_bf16 v[148:151], a[156:159], a[108:111], v[148:151]\n"
"ds_read_b64_tr_b16 a[148:149], v6 offset:32\n"
"ds_read_b64_tr_b16 a[150:151], v6 offset:4640\n"
"v_mfma_f32_16x16x32_bf16 v[152:155], a[160:163], a[104:107], v[152:155]\n"
"ds_read_b64_tr_b16 a[152:153], v6 offset:64\n"
"ds_read_b64_tr_b16 a[154:155], v6 offset:4672\n"
"v_mfma_f32_16x16x32_bf16 v[156:159], a[160:163], a[108:111], v[156:159]\n"
"ds_read_b64_tr_b16 a[156:157], v6 offset:96\n"
"ds_read_b64_tr_b16 a[158:159], v6 offset:4704\n"
"v_mfma_f32_16x16x32_bf16 v[160:163], a[164:167], a[104:107], v[160:163]\n"
"ds_read_b64_tr_b16 a[160:161], v6 offset:128\n"
"ds_read_b64_tr_b16 a[162:163], v6 offset:4736\n"
"v_mfma_f32_16x16x32_bf16 v[164:167], a[164:167], a[108:111], v[164:167]\n"
"buffer_load_dwordx4 v4, s[16:19], s61 offen offset:0 sc1 lds\n"
"s_add_u32 s61, s61, 36864\n"
"v_mfma_f32_16x16x32_bf16 v[168:171], a[168:171], a[104:107], v[168:171]\n"
"ds_read_b64_tr_b16 a[164:165], v6 offset:160\n"
"ds_read_b64_tr_b16 a[166:167], v6 offset:4768\n"
"v_mfma_f32_16x16x32_bf16 v[172:175], a[168:171], a[108:111], v[172:175]\n"
"s_add_u32 m0, s60, 73728\n"
"ds_write_b16 v246, v249\n"
"v_mfma_f32_16x16x32_bf16 v[176:179], a[172:175], a[104:107], v[176:179]\n"
"ds_read_b64_tr_b16 a[168:169], v6 offset:192\n"
"ds_read_b64_tr_b16 a[170:171], v6 offset:4800\n"
"v_mfma_f32_16x16x32_bf16 v[180:183], a[172:175], a[108:111], v[180:183]\n"
"ds_write_b16 v247, v250\n"
"s_waitcnt lgkmcnt(10)\n"
"v_mfma_f32_16x16x32_bf16 v[112:115], a[144:147], a[112:115], v[112:115]\n"
"ds_read_b64_tr_b16 a[172:173], v6 offset:224\n"
"ds_read_b64_tr_b16 a[174:175], v6 offset:4832\n"
"v_mfma_f32_16x16x32_bf16 v[116:119], a[144:147], a[116:119], v[116:119]\n"
"ds_write_b16 v248, v251\n"
"v_mfma_f32_16x16x32_bf16 v[120:123], a[148:151], a[112:115], v[120:123]\n"
"ds_read_b64_tr_b16 a[144:145], v6 offset:256\n"
"ds_read_b64_tr_b16 a[146:147], v6 offset:4864\n"
"v_mfma_f32_16x16x32_bf16 v[124:127], a[148:151], a[116:119], v[124:127]\n"
"v_mfma_f32_16x16x32_bf16 v[128:131], a[152:155], a[112:115], v[128:131]\n"
"ds_read_b64_tr_b16 a[148:149], v6 offset:9216\n"
"ds_read_b64_tr_b16 a[150:151], v6 offset:13824\n"
"v_mfma_f32_16x16x32_bf16 v[132:135], a[152:155], a[116:119], v[132:135]\n"
"s_waitcnt lgkmcnt(11)\n"
"v_mfma_f32_16x16x32_bf16 v[136:139], a[156:159], a[112:115], v[136:139]\n"
"ds_read_b64_tr_b16 a[152:153], v6 offset:9248\n"
"ds_read_b64_tr_b16 a[154:155], v6 offset:13856\n"
"v_mfma_f32_16x16x32_bf16 v[140:143], a[156:159], a[116:119], v[140:143]\n"
"v_mfma_f32_16x16x32_bf16 v[144:147], a[160:163], a[112:115], v[144:147]\n"
"ds_read_b64_tr_b16 a[156:157], v6 offset:9280\n"
"ds_read_b64_tr_b16 a[158:159], v6 offset:13888\n"
"v_mfma_f32_16x16x32_bf16 v[148:151], a[160:163], a[116:119], v[148:151]\n"
"v_mfma_f32_16x16x32_bf16 v[152:155], a[164:167], a[112:115], v[152:155]\n"
"ds_read_b64_tr_b16 a[160:161], v6 offset:9312\n"
"ds_read_b64_tr_b16 a[162:163], v6 offset:13920\n"
"v_mfma_f32_16x16x32_bf16 v[156:159], a[164:167], a[116:119], v[156:159]\n"
"s_waitcnt lgkmcnt(8)\n"
"v_mfma_f32_16x16x32_bf16 v[160:163], a[168:171], a[112:115], v[160:163]\n"
"ds_read_b64_tr_b16 a[164:165], v6 offset:9344\n"
"ds_read_b64_tr_b16 a[166:167], v6 offset:13952\n"
"v_mfma_f32_16x16x32_bf16 v[164:167], a[168:171], a[116:119], v[164:167]\n"
"v_mfma_f32_16x16x32_bf16 v[168:171], a[172:175], a[112:115], v[168:171]\n"
"ds_read_b64_tr_b16 a[168:169], v6 offset:9376\n"
"ds_read_b64_tr_b16 a[170:171], v6 offset:13984\n"
"v_mfma_f32_16x16x32_bf16 v[172:175], a[172:175], a[116:119], v[172:175]\n"
"v_mfma_f32_16x16x32_bf16 v[176:179], a[144:147], a[112:115], v[176:179]\n"
"ds_read_b64_tr_b16 a[172:173], v6 offset:9408\n"
"ds_read_b64_tr_b16 a[174:175], v6 offset:14016\n"
"v_mfma_f32_16x16x32_bf16 v[180:183], a[144:147], a[116:119], v[180:183]\n"
"s_waitcnt lgkmcnt(8)\n"
"v_mfma_f32_16x16x32_bf16 v[112:115], a[148:151], a[120:123], v[112:115]\n"
"ds_read_b64_tr_b16 a[144:145], v6 offset:9440\n"
"ds_read_b64_tr_b16 a[146:147], v6 offset:14048\n"
"v_mfma_f32_16x16x32_bf16 v[116:119], a[148:151], a[124:127], v[116:119]\n"
"v_mfma_f32_16x16x32_bf16 v[120:123], a[152:155], a[120:123], v[120:123]\n"
"ds_read_b64_tr_b16 a[148:149], v6 offset:9472\n"
"ds_read_b64_tr_b16 a[150:151], v6 offset:14080\n"
"v_mfma_f32_16x16x32_bf16 v[124:127], a[152:155], a[124:127], v[124:127]\n"
"v_mfma_f32_16x16x32_bf16 v[128:131], a[156:159], a[120:123], v[128:131]\n"
"ds_read_b64_tr_b16 a[152:153], v6 offset:18432\n"
"ds_read_b64_tr_b16 a[154:155], v6 offset:23040\n"
"v_mfma_f32_16x16x32_bf16 v[132:135], a[156:159], a[124:127], v[132:135]\n"
"s_waitcnt lgkmcnt(8)\n"
"v_mfma_f32_16x16x32_bf16 v[136:139], a[160:163], a[120:123], v[136:139]\n"
"ds_read_b64_tr_b16 a[156:157], v6 offset:18464\n"
"ds_read_b64_tr_b16 a[158:159], v6 offset:23072\n"
"v_mfma_f32_16x16x32_bf16 v[140:143], a[160:163], a[124:127], v[140:143]\n"
"v_mfma_f32_16x16x32_bf16 v[144:147], a[164:167], a[120:123], v[144:147]\n"
"ds_read_b64_tr_b16 a[160:161], v6 offset:18496\n"
"ds_read_b64_tr_b16 a[162:163], v6 offset:23104\n"
"v_mfma_f32_16x16x32_bf16 v[148:151], a[164:167], a[124:127], v[148:151]\n"
"v_mfma_f32_16x16x32_bf16 v[152:155], a[168:171], a[120:123], v[152:155]\n"
"ds_read_b64_tr_b16 a[164:165], v6 offset:18528\n"
"ds_read_b64_tr_b16 a[166:167], v6 offset:23136\n"
"v_mfma_f32_16x16x32_bf16 v[156:159], a[168:171], a[124:127], v[156:159]\n"
"s_waitcnt vmcnt(8)\n"
"s_waitcnt lgkmcnt(8)\n"
"v_mfma_f32_16x16x32_bf16 v[160:163], a[172:175], a[120:123], v[160:163]\n"
"ds_read_b64_tr_b16 a[168:169], v6 offset:18560\n"
"ds_read_b64_tr_b16 a[170:171], v6 offset:23168\n"
"v_mfma_f32_16x16x32_bf16 v[164:167], a[172:175], a[124:127], v[164:167]\n"
"s_mov_b64 exec, 1\n"
"buffer_store_dword v17, off, s[16:19], s68 sc1\n"
"s_mov_b64 exec, -1\n"
"v_mfma_f32_16x16x32_bf16 v[168:171], a[144:147], a[120:123], v[168:171]\n"
"ds_read_b64_tr_b16 a[172:173], v6 offset:18592\n"
"ds_read_b64_tr_b16 a[174:175], v6 offset:23200\n"
"v_mfma_f32_16x16x32_bf16 v[172:175], a[144:147], a[124:127], v[172:175]\n"
"s_add_u32 s71, s71, 12288\n"
"v_mfma_f32_16x16x32_bf16 v[176:179], a[148:151], a[120:123], v[176:179]\n"
"ds_read_b64_tr_b16 a[144:145], v6 offset:18624\n"
"ds_read_b64_tr_b16 a[146:147], v6 offset:23232\n"
"v_mfma_f32_16x16x32_bf16 v[180:183], a[148:151], a[124:127], v[180:183]\n"
"s_waitcnt lgkmcnt(8)\n"
"v_mfma_f32_16x16x32_bf16 v[112:115], a[152:155], a[128:131], v[112:115]\n"
"ds_read_b64_tr_b16 a[148:149], v6 offset:18656\n"
"ds_read_b64_tr_b16 a[150:151], v6 offset:23264\n"
"v_mfma_f32_16x16x32_bf16 v[116:119], a[152:155], a[132:135], v[116:119]\n"
"buffer_load_dwordx4 v2, s[16:19], s61 offen offset:0 sc1 lds\n"
"v_mfma_f32_16x16x32_bf16 v[120:123], a[156:159], a[128:131], v[120:123]\n"
"ds_read_b64_tr_b16 a[152:153], v6 offset:18688\n"
"ds_read_b64_tr_b16 a[154:155], v6 offset:23296\n"
"v_mfma_f32_16x16x32_bf16 v[124:127], a[156:159], a[132:135], v[124:127]\n"
"v_mfma_f32_16x16x32_bf16 v[128:131], a[160:163], a[128:131], v[128:131]\n"
"ds_read_b64_tr_b16 a[156:157], v6 offset:27648\n"
"ds_read_b64_tr_b16 a[158:159], v6 offset:32256\n"
"v_mfma_f32_16x16x32_bf16 v[132:135], a[160:163], a[132:135], v[132:135]\n"
"buffer_load_dwordx4 v2, s[16:19], s61 offen offset:1024 sc1 lds\n"
"s_waitcnt lgkmcnt(8)\n"
"v_mfma_f32_16x16x32_bf16 v[136:139], a[164:167], a[128:131], v[136:139]\n"
"ds_read_b64_tr_b16 a[160:161], v6 offset:27680\n"
"ds_read_b64_tr_b16 a[162:163], v6 offset:32288\n"
"v_mfma_f32_16x16x32_bf16 v[140:143], a[164:167], a[132:135], v[140:143]\n"
".Lmk_PCHERE_3:\n"
"s_add_u32 s0, s0, .Lmk_PCHERE_3-.Lmk_PCHERE_2\n"
"s_addc_u32 s1, s1, 0\n"
"s_sub_u32 s2, s2, .Lmk_PCHERE_3-.Lmk_PCHERE_2\n"
"s_max_i32 s2, s2, 0\n"
"buffer_load_dword a255, off, s[0:3], 0\n"
"v_mfma_f32_16x16x32_bf16 v[144:147], a[168:171], a[128:131], v[144:147]\n"
"ds_read_b64_tr_b16 a[164:165], v6 offset:27712\n"
"ds_read_b64_tr_b16 a[166:167], v6 offset:32320\n"
"v_mfma_f32_16x16x32_bf16 v[148:151], a[168:171], a[132:135], v[148:151]\n"
"buffer_load_dwordx4 v2, s[16:19], s61 offen offset:2048 sc1 lds\n"
"v_mfma_f32_16x16x32_bf16 v[152:155], a[172:175], a[128:131], v[152:155]\n"
"ds_read_b64_tr_b16 a[168:169], v6 offset:27744\n"
"ds_read_b64_tr_b16 a[170:171], v6 offset:32352\n"
"v_mfma_f32_16x16x32_bf16 v[156:159], a[172:175], a[132:135], v[156:159]\n"
"s_waitcnt lgkmcnt(8)\n"
"v_mfma_f32_16x16x32_bf16 v[160:163], a[144:147], a[128:131], v[160:163]\n"
"ds_read_b64_tr_b16 a[172:173], v6 offset:27776\n"
"ds_read_b64_tr_b16 a[174:175], v6 offset:32384\n"
"v_mfma_f32_16x16x32_bf16 v[164:167], a[144:147], a[132:135], v[164:167]\n"
"buffer_load_dwordx4 v2, s[16:19], s61 offen offset:3072 sc1 lds\n"
"s_add_u32 m0, s60, 77824\n"
"v_mfma_f32_16x16x32_bf16 v[168:171], a[148:151], a[128:131], v[168:171]\n"
"ds_read_b64_tr_b16 a[144:145], v6 offset:27808\n"
"ds_read_b64_tr_b16 a[146:147], v6 offset:32416\n"
"v_mfma_f32_16x16x32_bf16 v[172:175], a[148:151], a[132:135], v[172:175]\n"
"v_mfma_f32_16x16x32_bf16 v[176:179], a[152:155], a[128:131], v[176:179]\n"
"ds_read_b64_tr_b16 a[148:149], v6 offset:27840\n"
"ds_read_b64_tr_b16 a[150:151], v6 offset:32448\n"
"v_mfma_f32_16x16x32_bf16 v[180:183], a[152:155], a[132:135], v[180:183]\n"
"buffer_load_dwordx4 v3, s[16:19], s61 offen offset:0 sc1 lds\n"
"s_waitcnt lgkmcnt(8)\n"
"v_mfma_f32_16x16x32_bf16 v[112:115], a[156:159], a[136:139], v[112:115]\n"
"ds_read_b64_tr_b16 a[152:153], v6 offset:27872\n"
"ds_read_b64_tr_b16 a[154:155], v6 offset:32480\n"
"v_mfma_f32_16x16x32_bf16 v[116:119], a[156:159], a[140:143], v[116:119]\n"
"v_mfma_f32_16x16x32_bf16 v[120:123], a[160:163], a[136:139], v[120:123]\n"
"ds_read_b64_tr_b16 a[156:157], v6 offset:27904\n"
"ds_read_b64_tr_b16 a[158:159], v6 offset:32512\n"
"v_mfma_f32_16x16x32_bf16 v[124:127], a[160:163], a[140:143], v[124:127]\n"
"buffer_load_dwordx4 v3, s[16:19], s61 offen offset:1024 sc1 lds\n"
"v_mfma_f32_16x16x32_bf16 v[128:131], a[164:167], a[136:139], v[128:131]\n"
"v_mfma_f32_16x16x32_bf16 v[132:135], a[164:167], a[140:143], v[132:135]\n"
"s_waitcnt lgkmcnt(6)\n"
"v_mfma_f32_16x16x32_bf16 v[136:139], a[168:171], a[136:139], v[136:139]\n"
"buffer_load_dwordx4 v3, s[16:19], s61 offen offset:2048 sc1 lds\n"
"v_mfma_f32_16x16x32_bf16 v[140:143], a[168:171], a[140:143], v[140:143]\n"
"s_waitcnt vmcnt(9)\n"
"s_waitcnt lgkmcnt(0)\n"
"s_barrier\n"
"v_mfma_f32_16x16x32_bf16 v[144:147], a[172:175], a[136:139], v[144:147]\n"
"ds_read_b128 a[160:163], v2 offset:36864\n"
"v_mfma_f32_16x16x32_bf16 v[148:151], a[172:175], a[140:143], v[148:151]\n"
"ds_read_b128 a[164:167], v2 offset:37888\n"
"buffer_load_dwordx4 v3, s[16:19], s61 offen offset:3072 sc1 lds\n"
"v_mfma_f32_16x16x32_bf16 v[152:155], a[144:147], a[136:139], v[152:155]\n"
"ds_read_b128 a[168:171], v2 offset:38912\n"
"s_add_u32 m0, s60, 81920\n"
"v_mfma_f32_16x16x32_bf16 v[156:159], a[144:147], a[140:143], v[156:159]\n"
"ds_read_b128 a[172:175], v2 offset:39936\n"
"v_mfma_f32_16x16x32_bf16 v[160:163], a[148:151], a[136:139], v[160:163]\n"
"ds_read_b128 a[144:147], v2 offset:40960\n"
"buffer_load_dwordx4 v4, s[16:19], s61 offen offset:0 sc1 lds\n"
"v_mfma_f32_16x16x32_bf16 v[164:167], a[148:151], a[140:143], v[164:167]\n"
"s_add_u32 s61, s61, 36864\n"
"s_add_u32 m0, s60, 110592\n"
"v_mfma_f32_16x16x32_bf16 v[168:171], a[152:155], a[136:139], v[168:171]\n"
"ds_read_b128 a[148:151], v2 offset:41984\n"
"v_mfma_f32_16x16x32_bf16 v[172:175], a[152:155], a[140:143], v[172:175]\n"
"v_mfma_f32_16x16x32_bf16 v[176:179], a[156:159], a[136:139], v[176:179]\n"
"ds_read_b128 a[152:155], v26 offset:43008\n"
"v_mfma_f32_16x16x32_bf16 v[180:183], a[156:159], a[140:143], v[180:183]\n"
"s_waitcnt lgkmcnt(4)\n"
"v_mfma_f32_16x16x32_bf16 v[184:187], a[160:163], a[0:3], 0\n"
"ds_read_b128 a[156:159], v2 offset:44032\n"
"v_mov_b32 v38, 0xc0135761\n"
"v_mfma_f32_16x16x32_bf16 v[188:191], a[160:163], a[4:7], 0\n"
"v_mul_f32 v7, v112, v112\n"
"v_mul_f32 v16, v113, v113\n"
"v_mfma_f32_16x16x32_bf16 v[192:195], a[164:167], a[0:3], 0\n"
"ds_read_b128 a[160:163], v2 offset:45056\n"
"v_mul_f32 v37, v114, v114\n"
"v_mfma_f32_16x16x32_bf16 v[196:199], a[164:167], a[4:7], 0\n"
"v_fma_f32 v7, v7, s80, v38\n"
"v_fma_f32 v16, v16, s80, v38\n"
"v_mfma_f32_16x16x32_bf16 v[200:203], a[168:171], a[0:3], 0\n"
"ds_read_b128 a[164:167], v2 offset:46080\n"
"v_fma_f32 v37, v37, s80, v38\n"
"v_mfma_f32_16x16x32_bf16 v[204:207], a[168:171], a[4:7], 0\n"
"buffer_load_dwordx2 v[240:241], v29, s[48:51], 0 offen offset:0\n"
"s_waitcnt lgkmcnt(4)\n"
"v_mfma_f32_16x16x32_bf16 v[208:211], a[172:175], a[0:3], 0\n"
"ds_read_b128 a[168:171], v2 offset:47104\n"
"v_mul_f32 v7, v7, v112\n"
"v_mfma_f32_16x16x32_bf16 v[212:215], a[172:175], a[4:7], 0\n"
"v_mul_f32 v16, v16, v113\n"
"v_mul_f32 v37, v37, v114\n"
"v_mfma_f32_16x16x32_bf16 v[216:219], a[144:147], a[0:3], 0\n"
"ds_read_b128 a[172:175], v2 offset:48128\n"
"v_mfma_f32_16x16x32_bf16 v[220:223], a[144:147], a[4:7], 0\n"
"buffer_load_dwordx2 v[242:243], v29, s[48:51], 0 offen offset:1152\n"
"v_mfma_f32_16x16x32_bf16 v[224:227], a[148:151], a[0:3], 0\n"
"ds_read_b128 a[144:147], v2 offset:49152\n"
"v_mfma_f32_16x16x32_bf16 v[228:231], a[148:151], a[4:7], 0\n"
"s_waitcnt lgkmcnt(4)\n"
"v_mfma_f32_16x16x32_bf16 v[232:235], a[152:155], a[0:3], 0\n"
"ds_read_b128 a[148:151], v26 offset:50176\n"
"v_mfma_f32_16x16x32_bf16 v[236:239], a[152:155], a[4:7], 0\n"
"buffer_load_dwordx2 v[244:245], v29, s[48:51], 0 offen offset:2304\n"
"v_mfma_f32_16x16x32_bf16 v[184:187], a[156:159], a[8:11], v[184:187]\n"
"ds_read_b128 a[152:155], v2 offset:51200\n"
"v_mfma_f32_16x16x32_bf16 v[188:191], a[156:159], a[12:15], v[188:191]\n"
"v_exp_f32 v7, v7\n"
"v_mfma_f32_16x16x32_bf16 v[192:195], a[160:163], a[8:11], v[192:195]\n"
"ds_read_b128 a[156:159], v2 offset:52224\n"
"buffer_load_dwordx2 v[246:247], v29, s[48:51], 0 offen offset:3456\n"
"v_mfma_f32_16x16x32_bf16 v[196:199], a[160:163], a[12:15], v[196:199]\n"
"s_waitcnt lgkmcnt(4)\n"
"v_mfma_f32_16x16x32_bf16 v[200:203], a[164:167], a[8:11], v[200:203]\n"
"ds_read_b128 a[160:163], v2 offset:53248\n"
"v_mfma_f32_16x16x32_bf16 v[204:207], a[164:167], a[12:15], v[204:207]\n"
"v_exp_f32 v16, v16\n"
"v_mfma_f32_16x16x32_bf16 v[208:211], a[168:171], a[8:11], v[208:211]\n"
"ds_read_b128 a[164:167], v2 offset:54272\n"
"buffer_load_dwordx2 v[248:249], v30, s[48:51], 0 offen offset:0\n"
"v_mfma_f32_16x16x32_bf16 v[212:215], a[168:171], a[12:15], v[212:215]\n"
"v_exp_f32 v37, v37\n"
"v_mfma_f32_16x16x32_bf16 v[216:219], a[172:175], a[8:11], v[216:219]\n"
"ds_read_b128 a[168:171], v2 offset:55296\n"
"v_add_f32 v7, 1.0, v7\n"
"v_mfma_f32_16x16x32_bf16 v[220:223], a[172:175], a[12:15], v[220:223]\n"
"v_add_f32 v16, 1.0, v16\n"
"s_waitcnt lgkmcnt(4)\n"
"v_mfma_f32_16x16x32_bf16 v[224:227], a[144:147], a[8:11], v[224:227]\n"
"ds_read_b128 a[172:175], v2 offset:56320\n"
"buffer_load_dwordx2 v[250:251], v30, s[48:51], 0 offen offset:1152\n"
"v_mfma_f32_16x16x32_bf16 v[228:231], a[144:147], a[12:15], v[228:231]\n"
"v_add_f32 v37, 1.0, v37\n"
"v_mfma_f32_16x16x32_bf16 v[232:235], a[148:151], a[8:11], v[232:235]\n"
"ds_read_b128 a[144:147], v26 offset:57344\n"
"v_mfma_f32_16x16x32_bf16 v[236:239], a[148:151], a[12:15], v[236:239]\n"
"v_rcp_f32 v7, v7\n"
"v_mfma_f32_16x16x32_bf16 v[184:187], a[152:155], a[16:19], v[184:187]\n"
"ds_read_b128 a[148:151], v2 offset:58368\n"
"buffer_load_dwordx2 v[252:253], v30, s[48:51], 0 offen offset:2304\n"
"v_mfma_f32_16x16x32_bf16 v[188:191], a[152:155], a[20:23], v[188:191]\n"
"s_waitcnt lgkmcnt(4)\n"
"v_mfma_f32_16x16x32_bf16 v[192:195], a[156:159], a[16:19], v[192:195]\n"
"ds_read_b128 a[152:155], v2 offset:59392\n"
"v_mfma_f32_16x16x32_bf16 v[196:199], a[156:159], a[20:23], v[196:199]\n"
"buffer_load_dwordx2 v[254:255], v30, s[48:51], 0 offen offset:3456\n"
"v_mfma_f32_16x16x32_bf16 v[200:203], a[160:163], a[16:19], v[200:203]\n"
"ds_read_b128 a[156:159], v2 offset:60416\n"
"v_mfma_f32_16x16x32_bf16 v[204:207], a[160:163], a[20:23], v[204:207]\n"
"v_rcp_f32 v16, v16\n"
"v_mfma_f32_16x16x32_bf16 v[208:211], a[164:167], a[16:19], v[208:211]\n"
"ds_read_b128 a[160:163], v2 offset:61440\n"
"v_mfma_f32_16x16x32_bf16 v[212:215], a[164:167], a[20:23], v[212:215]\n"
"buffer_load_dwordx2 v[18:19], v31, s[32:35], 0 offen offset:0\n"
"s_waitcnt lgkmcnt(4)\n"
"v_mfma_f32_16x16x32_bf16 v[216:219], a[168:171], a[16:19], v[216:219]\n"
"ds_read_b128 a[164:167], v2 offset:62464\n"
"buffer_load_dwordx2 v[20:21], v31, s[32:35], 0 offen offset:1152\n"
"v_mfma_f32_16x16x32_bf16 v[220:223], a[168:171], a[20:23], v[220:223]\n"
"s_lshr_b32 s25, s24, 1\n"
"s_lshl_b32 s25, s25, 2\n"
"v_mfma_f32_16x16x32_bf16 v[224:227], a[172:175], a[16:19], v[224:227]\n"
"ds_read_b128 a[168:171], v2 offset:63488\n"
"buffer_load_dwordx4 v2, s[16:19], s61 offen offset:0 sc1 lds\n"
"v_mfma_f32_16x16x32_bf16 v[228:231], a[172:175], a[20:23], v[228:231]\n"
"s_add_u32 s25, s25, s24\n"
"s_add_u32 s25, s25, 6\n"
"v_mfma_f32_16x16x32_bf16 v[232:235], a[144:147], a[16:19], v[232:235]\n"
"ds_read_b128 a[172:175], v26 offset:64512\n"
"v_mfma_f32_16x16x32_bf16 v[236:239], a[144:147], a[20:23], v[236:239]\n"
"s_waitcnt lgkmcnt(4)\n"
"v_mfma_f32_16x16x32_bf16 v[184:187], a[148:151], a[24:27], v[184:187]\n"
"ds_read_b128 a[144:147], v5 offset:0\n"
"buffer_load_dwordx4 v2, s[16:19], s61 offen offset:1024 sc1 lds\n"
"v_mfma_f32_16x16x32_bf16 v[188:191], a[148:151], a[28:31], v[188:191]\n"
"s_cmp_eq_u32 s24, 4\n"
"s_cselect_b32 s25, 20, s25\n"
"v_mfma_f32_16x16x32_bf16 v[192:195], a[152:155], a[24:27], v[192:195]\n"
"ds_read_b128 a[148:151], v5 offset:1024\n"
"v_mfma_f32_16x16x32_bf16 v[196:199], a[152:155], a[28:31], v[196:199]\n"
"s_cmp_eq_u32 s24, 5\n"
"s_cselect_b32 s25, 4, s25\n"
"v_mfma_f32_16x16x32_bf16 v[200:203], a[156:159], a[24:27], v[200:203]\n"
"ds_read_b128 a[152:155], v5 offset:2048\n"
"buffer_load_dwordx4 v2, s[16:19], s61 offen offset:2048 sc1 lds\n"
"v_mfma_f32_16x16x32_bf16 v[204:207], a[156:159], a[28:31], v[204:207]\n"
"s_waitcnt lgkmcnt(4)\n"
"v_mfma_f32_16x16x32_bf16 v[208:211], a[160:163], a[24:27], v[208:211]\n"
"ds_read_b128 a[156:159], v5 offset:3072\n"
"v_mfma_f32_16x16x32_bf16 v[212:215], a[160:163], a[28:31], v[212:215]\n"
"s_cmp_lt_u32 s24, 4\n"
"s_cselect_b32 s26, 2, 1\n"
"s_cselect_b32 s24, s24, s25\n"
"v_mfma_f32_16x16x32_bf16 v[216:219], a[164:167], a[24:27], v[216:219]\n"
"ds_read_b128 a[160:163], v5 offset:4096\n"
"buffer_load_dwordx4 v2, s[16:19], s61 offen offset:3072 sc1 lds\n"
"v_mfma_f32_16x16x32_bf16 v[220:223], a[164:167], a[28:31], v[220:223]\n"
"s_add_u32 m0, s60, 114688\n"
"s_lshr_b32 s27, s22, 1\n"
"v_mfma_f32_16x16x32_bf16 v[224:227], a[168:171], a[24:27], v[224:227]\n"
"ds_read_b128 a[164:167], v5 offset:5120\n"
"s_and_b32 s98, s22, 1\n"
"v_mfma_f32_16x16x32_bf16 v[228:231], a[168:171], a[28:31], v[228:231]\n"
"s_lshl_b32 s98, s98, 12\n"
"s_waitcnt lgkmcnt(4)\n"
"v_mfma_f32_16x16x32_bf16 v[232:235], a[172:175], a[24:27], v[232:235]\n"
"ds_read_b128 a[168:171], v27 offset:6144\n"
"buffer_load_dwordx4 v3, s[16:19], s61 offen offset:0 sc1 lds\n"
"v_mfma_f32_16x16x32_bf16 v[236:239], a[172:175], a[28:31], v[236:239]\n"
"s_and_b32 s99, s23, 12\n"
"s_mul_i32 s99, s99, 221184\n"
"v_mfma_f32_16x16x32_bf16 v[184:187], a[144:147], a[32:35], v[184:187]\n"
"ds_read_b128 a[172:175], v5 offset:7168\n"
"s_add_u32 s99, s99, 5242880\n"
"v_mfma_f32_16x16x32_bf16 v[188:191], a[144:147], a[36:39], v[188:191]\n"
"s_add_u32 s21, s98, 0\n"
"buffer_load_dwordx4 v3, s[16:19], s61 offen offset:1024 sc1 lds\n"
"v_mfma_f32_16x16x32_bf16 v[192:195], a[148:151], a[32:35], v[192:195]\n"
"s_cmp_lt_u32 s22, 8\n"
"s_cselect_b32 s20, s27, 0\n"
"s_cselect_b32 s21, s21, 8192\n"
"v_mfma_f32_16x16x32_bf16 v[196:199], a[148:151], a[36:39], v[196:199]\n"
"s_sub_u32 s94, s23, s20\n"
"s_waitcnt lgkmcnt(3)\n"
"v_mfma_f32_16x16x32_bf16 v[200:203], a[152:155], a[32:35], v[200:203]\n"
"buffer_load_dwordx4 v3, s[16:19], s61 offen offset:2048 sc1 lds\n"
"s_mul_i32 s20, s20, 24\n"
"v_mfma_f32_16x16x32_bf16 v[204:207], a[152:155], a[36:39], v[204:207]\n"
"s_waitcnt vmcnt(17)\n"
"s_waitcnt lgkmcnt(0)\n"
"s_barrier\n"
"v_mfma_f32_16x16x32_bf16 v[208:211], a[156:159], a[32:35], v[208:211]\n"
"ds_read_b128 a[144:147], v5 offset:8192\n"
"s_and_b32 s94, s94, 3\n"
"v_mfma_f32_16x16x32_bf16 v[212:215], a[156:159], a[36:39], v[212:215]\n"
"ds_read_b128 a[148:151], v5 offset:9216\n"
"buffer_load_dwordx4 v3, s[16:19], s61 offen offset:3072 sc1 lds\n"
"v_mfma_f32_16x16x32_bf16 v[216:219], a[160:163], a[32:35], v[216:219]\n"
"ds_read_b128 a[152:155], v5 offset:10240\n"
"s_add_u32 m0, s60, 118784\n"
"v_mfma_f32_16x16x32_bf16 v[220:223], a[160:163], a[36:39], v[220:223]\n"
"ds_read_b128 a[156:159], v5 offset:11264\n"
"v_mfma_f32_16x16x32_bf16 v[224:227], a[164:167], a[32:35], v[224:227]\n"
"ds_read_b128 a[160:163], v5 offset:12288\n"
"buffer_load_dwordx4 v4, s[16:19], s61 offen offset:0 sc1 lds\n"
"v_mfma_f32_16x16x32_bf16 v[228:231], a[164:167], a[36:39], v[228:231]\n"
"s_lshr_b32 s93, s60, 1\n"
"s_add_u32 s61, s61, 36864\n"
"v_mfma_f32_16x16x32_bf16 v[232:235], a[168:171], a[32:35], v[232:235]\n"
"ds_read_b128 a[164:167], v27 offset:13312\n"
"s_sub_u32 s61, s61, s93\n"
"v_mfma_f32_16x16x32_bf16 v[236:239], a[168:171], a[36:39], v[236:239]\n"
"s_add_u32 m0, s93, 0\n"
"s_mul_i32 s94, s94, s26\n"
"v_mfma_f32_16x16x32_bf16 v[184:187], a[172:175], a[40:43], v[184:187]\n"
"ds_read_b128 a[168:171], v5 offset:14336\n"
"v_mfma_f32_16x16x32_bf16 v[188:191], a[172:175], a[44:47], v[188:191]\n"
"s_waitcnt lgkmcnt(4)\n"
"v_mfma_f32_16x16x32_bf16 v[192:195], a[144:147], a[40:43], v[192:195]\n"
"ds_read_b128 a[172:175], v5 offset:15360\n"
"v_mfma_f32_16x16x32_bf16 v[196:199], a[144:147], a[44:47], v[196:199]\n"
"s_cmp_eq_u32 s94, 0\n"
"s_cselect_b32 s36, s24, s25\n"
"v_mfma_f32_16x16x32_bf16 v[200:203], a[148:151], a[40:43], v[200:203]\n"
"ds_read_b128 a[144:147], v5 offset:16384\n"
"s_add_u32 s94, s94, s36\n"
"v_mfma_f32_16x16x32_bf16 v[204:207], a[148:151], a[44:47], v[204:207]\n"
"s_add_u32 s94, s94, s20\n"
"s_mul_i32 s94, s94, 9216\n"
"v_mfma_f32_16x16x32_bf16 v[208:211], a[152:155], a[40:43], v[208:211]\n"
"ds_read_b128 a[148:151], v5 offset:17408\n"
"buffer_load_dwordx4 v2, s[16:19], s61 offen offset:0 sc1 lds\n"
"v_mfma_f32_16x16x32_bf16 v[212:215], a[152:155], a[44:47], v[212:215]\n"
"s_add_u32 s94, s94, s21\n"
"s_waitcnt lgkmcnt(4)\n"
"v_mfma_f32_16x16x32_bf16 v[216:219], a[156:159], a[40:43], v[216:219]\n"
"ds_read_b128 a[152:155], v5 offset:18432\n"
"s_add_u32 s94, s94, s99\n"
"v_mfma_f32_16x16x32_bf16 v[220:223], a[156:159], a[44:47], v[220:223]\n"
"s_add_u32 s21, s98, 1024\n"
"v_mfma_f32_16x16x32_bf16 v[224:227], a[160:163], a[40:43], v[224:227]\n"
"ds_read_b128 a[156:159], v5 offset:19456\n"
"buffer_load_dwordx4 v2, s[16:19], s61 offen offset:1024 sc1 lds\n"
"v_mfma_f32_16x16x32_bf16 v[228:231], a[160:163], a[44:47], v[228:231]\n"
"s_cmp_lt_u32 s22, 8\n"
"s_cselect_b32 s20, s27, 1\n"
"s_cselect_b32 s21, s21, 8192\n"
"v_mfma_f32_16x16x32_bf16 v[232:235], a[164:167], a[40:43], v[232:235]\n"
"ds_read_b128 a[160:163], v27 offset:20480\n"
"s_sub_u32 s95, s23, s20\n"
"v_mfma_f32_16x16x32_bf16 v[236:239], a[164:167], a[44:47], v[236:239]\n"
"s_mul_i32 s20, s20, 24\n"
"s_waitcnt lgkmcnt(4)\n"
"v_mfma_f32_16x16x32_bf16 v[184:187], a[168:171], a[48:51], v[184:187]\n"
"ds_read_b128 a[164:167], v5 offset:21504\n"
"buffer_load_dwordx4 v2, s[16:19], s61 offen offset:2048 sc1 lds\n"
"v_mfma_f32_16x16x32_bf16 v[188:191], a[168:171], a[52:55], v[188:191]\n"
"s_and_b32 s95, s95, 3\n"
"s_mul_i32 s95, s95, s26\n"
"v_mfma_f32_16x16x32_bf16 v[192:195], a[172:175], a[48:51], v[192:195]\n"
"ds_read_b128 a[168:171], v5 offset:22528\n"
"v_mfma_f32_16x16x32_bf16 v[196:199], a[172:175], a[52:55], v[196:199]\n"
"s_cmp_eq_u32 s95, 0\n"
"s_cselect_b32 s36, s24, s25\n"
"v_mfma_f32_16x16x32_bf16 v[200:203], a[144:147], a[48:51], v[200:203]\n"
"ds_read_b128 a[172:175], v5 offset:23552\n"
"buffer_load_dwordx4 v2, s[16:19], s61 offen offset:3072 sc1 lds\n"
"v_mfma_f32_16x16x32_bf16 v[204:207], a[144:147], a[52:55], v[204:207]\n"
"s_add_u32 m0, s93, 4096\n"
"s_waitcnt lgkmcnt(4)\n"
"v_mfma_f32_16x16x32_bf16 v[208:211], a[148:151], a[48:51], v[208:211]\n"
"ds_read_b128 a[144:147], v5 offset:24576\n"
"s_add_u32 s95, s95, s36\n"
"v_mfma_f32_16x16x32_bf16 v[212:215], a[148:151], a[52:55], v[212:215]\n"
"s_add_u32 s95, s95, s20\n"
"s_mul_i32 s95, s95, 9216\n"
"v_mfma_f32_16x16x32_bf16 v[216:219], a[152:155], a[48:51], v[216:219]\n"
"ds_read_b128 a[148:151], v5 offset:25600\n"
"s_add_u32 s95, s95, s21\n"
"v_mfma_f32_16x16x32_bf16 v[220:223], a[152:155], a[52:55], v[220:223]\n"
"s_mov_b32 exec_hi, 0\n"
"buffer_load_dwordx4 v3, s[16:19], s61 offen offset:0 sc1 lds\n"
"s_mov_b32 exec_hi, -1\n"
"v_mfma_f32_16x16x32_bf16 v[224:227], a[156:159], a[48:51], v[224:227]\n"
"ds_read_b128 a[152:155], v5 offset:26624\n"
"s_add_u32 s95, s95, s99\n"
"v_mfma_f32_16x16x32_bf16 v[228:231], a[156:159], a[52:55], v[228:231]\n"
"s_add_u32 s21, s98, 2048\n"
"s_waitcnt lgkmcnt(4)\n"
"v_mfma_f32_16x16x32_bf16 v[232:235], a[160:163], a[48:51], v[232:235]\n"
"ds_read_b128 a[156:159], v27 offset:27648\n"
"buffer_load_dwordx4 a[176:179], v2, s[16:19], s62 offen offset:0 sc1\n"
"v_mfma_f32_16x16x32_bf16 v[236:239], a[160:163], a[52:55], v[236:239]\n"
"s_cmp_lt_u32 s22, 8\n"
"s_cselect_b32 s20, s27, 2\n"
"s_cselect_b32 s21, s21, 8192\n"
"v_mfma_f32_16x16x32_bf16 v[184:187], a[164:167], a[56:59], v[184:187]\n"
"ds_read_b128 a[160:163], v5 offset:28672\n"
"s_sub_u32 s96, s23, s20\n"
"v_mfma_f32_16x16x32_bf16 v[188:191], a[164:167], a[60:63], v[188:191]\n"
"s_mul_i32 s20, s20, 24\n"
"s_and_b32 s96, s96, 3\n"
"v_mfma_f32_16x16x32_bf16 v[192:195], a[168:171], a[56:59], v[192:195]\n"
"ds_read_b128 a[164:167], v5 offset:29696\n"
"buffer_load_dwordx4 a[180:183], v2, s[16:19], s62 offen offset:1024 sc1\n"
"v_mfma_f32_16x16x32_bf16 v[196:199], a[168:171], a[60:63], v[196:199]\n"
"s_mul_i32 s96, s96, s26\n"
"s_waitcnt lgkmcnt(4)\n"
"v_mfma_f32_16x16x32_bf16 v[200:203], a[172:175], a[56:59], v[200:203]\n"
"ds_read_b128 a[168:171], v5 offset:30720\n"
"v_mfma_f32_16x16x32_bf16 v[204:207], a[172:175], a[60:63], v[204:207]\n"
"s_cmp_eq_u32 s96, 0\n"
"s_cselect_b32 s36, s24, s25\n"
"v_mfma_f32_16x16x32_bf16 v[208:211], a[144:147], a[56:59], v[208:211]\n"
"ds_read_b128 a[172:175], v5 offset:31744\n"
"s_add_u32 s96, s96, s36\n"
"v_mfma_f32_16x16x32_bf16 v[212:215], a[144:147], a[60:63], v[212:215]\n"
"buffer_load_dwordx4 a[184:187], v2, s[16:19], s62 offen offset:2048 sc1\n"
"s_add_u32 s96, s96, s20\n"
"v_mfma_f32_16x16x32_bf16 v[216:219], a[148:151], a[56:59], v[216:219]\n"
"ds_read_b128 a[144:147], v5 offset:32768\n"
"s_mul_i32 s96, s96, 9216\n"
"v_mfma_f32_16x16x32_bf16 v[220:223], a[148:151], a[60:63], v[220:223]\n"
"s_add_u32 s96, s96, s21\n"
"s_waitcnt lgkmcnt(4)\n"
"v_mfma_f32_16x16x32_bf16 v[224:227], a[152:155], a[56:59], v[224:227]\n"
"ds_read_b128 a[148:151], v5 offset:33792\n"
"s_add_u32 s96, s96, s99\n"
"v_mfma_f32_16x16x32_bf16 v[228:231], a[152:155], a[60:63], v[228:231]\n"
"buffer_load_dwordx4 a[188:191], v2, s[16:19], s62 offen offset:3072 sc1\n"
"s_add_u32 s21, s98, 3072\n"
"v_mfma_f32_16x16x32_bf16 v[232:235], a[156:159], a[56:59], v[232:235]\n"
"ds_read_b128 a[152:155], v27 offset:34816\n"
"v_mfma_f32_16x16x32_bf16 v[236:239], a[156:159], a[60:63], v[236:239]\n"
"buffer_load_dword v34, v33, s[16:19], s71 offen sc1\n"
"v_mfma_f32_16x16x32_bf16 v[184:187], a[160:163], a[64:67], v[184:187]\n"
"ds_read_b128 a[156:159], v5 offset:35840\n"
"v_mfma_f32_16x16x32_bf16 v[188:191], a[160:163], a[68:71], v[188:191]\n"
"s_waitcnt lgkmcnt(4)\n"
"v_mfma_f32_16x16x32_bf16 v[192:195], a[164:167], a[64:67], v[192:195]\n"
"ds_read_b128 a[160:163], v5 offset:36864\n"
"v_mfma_f32_16x16x32_bf16 v[196:199], a[164:167], a[68:71], v[196:199]\n"
"buffer_load_dwordx4 a[192:195], v3, s[16:19], s62 offen offset:0 sc1\n"
"v_mfma_f32_16x16x32_bf16 v[200:203], a[168:171], a[64:67], v[200:203]\n"
"ds_read_b128 a[164:167], v5 offset:37888\n"
"v_mfma_f32_16x16x32_bf16 v[204:207], a[168:171], a[68:71], v[204:207]\n"
"s_cmp_lt_u32 s22, 8\n"
"s_cselect_b32 s20, s27, 3\n"
"s_cselect_b32 s21, s21, 8192\n"
"v_mfma_f32_16x16x32_bf16 v[208:211], a[172:175], a[64:67], v[208:211]\n"
"ds_read_b128 a[168:171], v5 offset:38912\n"
"s_sub_u32 s97, s23, s20\n"
"v_mfma_f32_16x16x32_bf16 v[212:215], a[172:175], a[68:71], v[212:215]\n"
"buffer_load_dwordx4 a[196:199], v3, s[16:19], s62 offen offset:1024 sc1\n"
"s_waitcnt lgkmcnt(4)\n"
"v_mfma_f32_16x16x32_bf16 v[216:219], a[144:147], a[64:67], v[216:219]\n"
"ds_read_b128 a[172:175], v5 offset:39936\n"
"s_mul_i32 s20, s20, 24\n"
"v_mfma_f32_16x16x32_bf16 v[220:223], a[144:147], a[68:71], v[220:223]\n"
"s_and_b32 s97, s97, 3\n"
"s_mul_i32 s97, s97, s26\n"
"v_mfma_f32_16x16x32_bf16 v[224:227], a[148:151], a[64:67], v[224:227]\n"
"ds_read_b128 a[144:147], v5 offset:40960\n"
"v_mfma_f32_16x16x32_bf16 v[228:231], a[148:151], a[68:71], v[228:231]\n"
"buffer_load_dwordx4 a[200:203], v3, s[16:19], s62 offen offset:2048 sc1\n"
"v_mfma_f32_16x16x32_bf16 v[232:235], a[152:155], a[64:67], v[232:235]\n"
"ds_read_b128 a[148:151], v27 offset:41984\n"
"v_mfma_f32_16x16x32_bf16 v[236:239], a[152:155], a[68:71], v[236:239]\n"
"s_waitcnt lgkmcnt(4)\n"
"v_mfma_f32_16x16x32_bf16 v[184:187], a[156:159], a[72:75], v[184:187]\n"
"ds_read_b128 a[152:155], v5 offset:43008\n"
"v_mfma_f32_16x16x32_bf16 v[188:191], a[156:159], a[76:79], v[188:191]\n"
"buffer_load_dwordx4 a[204:207], v3, s[16:19], s62 offen offset:3072 sc1\n"
"s_add_u32 s62, s62, 8192\n"
"v_mfma_f32_16x16x32_bf16 v[192:195], a[160:163], a[72:75], v[192:195]\n"
"ds_read_b128 a[156:159], v5 offset:44032\n"
"v_mfma_f32_16x16x32_bf16 v[196:199], a[160:163], a[76:79], v[196:199]\n"
"s_cmp_eq_u32 s97, 0\n"
"s_cselect_b32 s36, s24, s25\n"
"v_mfma_f32_16x16x32_bf16 v[200:203], a[164:167], a[72:75], v[200:203]\n"
"s_add_u32 s97, s97, s36\n"
"s_add_u32 s97, s97, s20\n"
"v_mfma_f32_16x16x32_bf16 v[204:207], a[164:167], a[76:79], v[204:207]\n"
"s_mul_i32 s97, s97, 9216\n"
"s_waitcnt lgkmcnt(3)\n"
"v_mfma_f32_16x16x32_bf16 v[208:211], a[168:171], a[72:75], v[208:211]\n"
"buffer_load_dwordx4 a[208:211], v2, s[16:19], s62 offen offset:0 sc1\n"
"s_add_u32 s97, s97, s21\n"
"v_mfma_f32_16x16x32_bf16 v[212:215], a[168:171], a[76:79], v[212:215]\n"
"s_waitcnt vmcnt(15)\n"
"s_waitcnt lgkmcnt(0)\n"
"s_barrier\n"
"v_mfma_f32_16x16x32_bf16 v[216:219], a[172:175], a[72:75], v[216:219]\n"
"ds_read_b128 a[160:163], v5 offset:45056\n"
"s_add_u32 s97, s97, s99\n"
"v_mfma_f32_16x16x32_bf16 v[220:223], a[172:175], a[76:79], v[220:223]\n"
"ds_read_b128 a[164:167], v5 offset:46080\n"
"buffer_load_dwordx4 a[212:215], v2, s[16:19], s62 offen offset:1024 sc1\n"
"v_mfma_f32_16x16x32_bf16 v[224:227], a[144:147], a[72:75], v[224:227]\n"
"ds_read_b128 a[168:171], v5 offset:47104\n"
"v_and_b32 v76, 15, v1\n"
"v_mfma_f32_16x16x32_bf16 v[228:231], a[144:147], a[76:79], v[228:231]\n"
"ds_read_b128 a[172:175], v5 offset:48128\n"
"v_mfma_f32_16x16x32_bf16 v[232:235], a[148:151], a[72:75], v[232:235]\n"
"ds_read_b128 a[144:147], v27 offset:49152\n"
"v_and_b32 v77, 12, v76\n"
"v_mfma_f32_16x16x32_bf16 v[236:239], a[148:151], a[76:79], v[236:239]\n"
"v_add_u32 v77, v77, v76\n"
"v_lshlrev_b32 v77, 1, v77\n"
"v_mfma_f32_16x16x32_bf16 v[184:187], a[152:155], a[80:83], v[184:187]\n"
"ds_read_b128 a[148:151], v5 offset:50176\n"
"buffer_load_dwordx4 a[216:219], v2, s[16:19], s62 offen offset:2048 sc1\n"
"v_mfma_f32_16x16x32_bf16 v[188:191], a[152:155], a[84:87], v[188:191]\n"
"s_lshl_b32 s73, s63, 6\n"
"buffer_load_ushort v8, v77, s[40:43], s73 offen offset:0\n"
"v_mfma_f32_16x16x32_bf16 v[192:195], a[156:159], a[80:83], v[192:195]\n"
"ds_read_b128 a[152:155], v5 offset:51200\n"
"buffer_load_ushort v12, v77, s[40:43], s73 offen offset:8\n"
"v_mfma_f32_16x16x32_bf16 v[196:199], a[156:159], a[84:87], v[196:199]\n"
"v_lshrrev_b32 v78, 4, v1\n"
"s_waitcnt lgkmcnt(4)\n"
"v_mfma_f32_16x16x32_bf16 v[200:203], a[160:163], a[80:83], v[200:203]\n"
"ds_read_b128 a[156:159], v5 offset:52224\n"
"v_lshrrev_b32 v79, 2, v76\n"
"v_mfma_f32_16x16x32_bf16 v[204:207], a[160:163], a[84:87], v[204:207]\n"
"v_and_b32 v80, 1, v79\n"
"v_and_b32 v81, 3, v76\n"
"v_mfma_f32_16x16x32_bf16 v[208:211], a[164:167], a[80:83], v[208:211]\n"
"ds_read_b128 a[160:163], v5 offset:53248\n"
"v_mul_u32_u24 v82, 20, v80\n"
"v_mfma_f32_16x16x32_bf16 v[212:215], a[164:167], a[84:87], v[212:215]\n"
"buffer_load_dwordx4 a[220:223], v2, s[16:19], s62 offen offset:3072 sc1\n"
"v_add_u32 v81, v81, v82\n"
"v_mfma_f32_16x16x32_bf16 v[216:219], a[168:171], a[80:83], v[216:219]\n"
"ds_read_b128 a[164:167], v5 offset:54272\n"
"v_mul_u32_u24 v81, 48, v81\n"
"v_mfma_f32_16x16x32_bf16 v[220:223], a[168:171], a[84:87], v[220:223]\n"
"v_add_u32 v81, 155648, v81\n"
"s_waitcnt lgkmcnt(4)\n"
"v_mfma_f32_16x16x32_bf16 v[224:227], a[172:175], a[80:83], v[224:227]\n"
"ds_read_b128 a[168:171], v5 offset:55296\n"
"v_and_b32 v82, 1, v78\n"
"v_mfma_f32_16x16x32_bf16 v[228:231], a[172:175], a[84:87], v[228:231]\n"
"v_lshl_add_u32 v82, v82, 4, v81\n"
"v_mov_b32 v83, 157696\n"
"v_mfma_f32_16x16x32_bf16 v[232:235], a[144:147], a[80:83], v[232:235]\n"
"ds_read_b128 a[172:175], v27 offset:56320\n"
"buffer_load_dwordx4 a[224:227], v3, s[16:19], s62 offen offset:0 sc1\n"
"v_mfma_f32_16x16x32_bf16 v[236:239], a[144:147], a[84:87], v[236:239]\n"
"v_lshrrev_b32 v84, 1, v78\n"
"v_cmp_eq_u32 s[82:83], v84, v79\n"
"v_mfma_f32_16x16x32_bf16 v[184:187], a[148:151], a[88:91], v[184:187]\n"
"ds_read_b128 a[144:147], v5 offset:57344\n"
"v_add_u32 v84, 2, v84\n"
"v_mfma_f32_16x16x32_bf16 v[188:191], a[148:151], a[92:95], v[188:191]\n"
"v_cmp_eq_u32 s[84:85], v84, v79\n"
"s_waitcnt lgkmcnt(4)\n"
"v_mfma_f32_16x16x32_bf16 v[192:195], a[152:155], a[88:91], v[192:195]\n"
"ds_read_b128 a[148:151], v5 offset:58368\n"
"buffer_load_dwordx4 a[228:231], v3, s[16:19], s62 offen offset:1024 sc1\n"
"v_mfma_f32_16x16x32_bf16 v[196:199], a[152:155], a[92:95], v[196:199]\n"
"v_lshrrev_b32 v79, 1, v79\n"
"v_xor_b32 v79, 1, v79\n"
"v_mfma_f32_16x16x32_bf16 v[200:203], a[156:159], a[88:91], v[200:203]\n"
"ds_read_b128 a[152:155], v5 offset:59392\n"
"v_cndmask_b32 v90, v83, v82, s[82:83]\n"
"v_mfma_f32_16x16x32_bf16 v[204:207], a[156:159], a[92:95], v[204:207]\n"
"v_cndmask_b32 v91, v83, v82, s[84:85]\n"
"v_cmp_eq_u32 s[82:83], v78, v79\n"
"v_mfma_f32_16x16x32_bf16 v[208:211], a[160:163], a[88:91], v[208:211]\n"
"ds_read_b128 a[156:159], v5 offset:60416\n"
"buffer_load_dwordx4 a[232:235], v3, s[16:19], s62 offen offset:2048 sc1\n"
"v_mfma_f32_16x16x32_bf16 v[212:215], a[160:163], a[92:95], v[212:215]\n"
"v_add_u32 v81, 32, v81\n"
"s_waitcnt lgkmcnt(4)\n"
"v_mfma_f32_16x16x32_bf16 v[216:219], a[164:167], a[88:91], v[216:219]\n"
"ds_read_b128 a[160:163], v5 offset:61440\n"
"v_cndmask_b32 v92, v83, v81, s[82:83]\n"
"v_mfma_f32_16x16x32_bf16 v[220:223], a[164:167], a[92:95], v[220:223]\n"
"s_waitcnt vmcnt(13)\n"
"v_cmp_ne_u32 vcc, s55, v34\n"
"s_cbranch_vccnz .Lmk_SLOW_2\n"
".Lmk_BACK_2:\n"
"v_mfma_f32_16x16x32_bf16 v[224:227], a[168:171], a[88:91], v[224:227]\n"
"ds_read_b128 a[164:167], v5 offset:62464\n"
"s_mul_i32 s61, s53, 589824\n"
"v_mfma_f32_16x16x32_bf16 v[228:231], a[168:171], a[92:95], v[228:231]\n"
"s_add_u32 s61, s61, s60\n"
"s_add_u32 s61, s61, 17825792\n"
"v_mfma_f32_16x16x32_bf16 v[232:235], a[172:175], a[88:91], v[232:235]\n"
"ds_read_b128 a[168:171], v27 offset:63488\n"
"s_add_u32 m0, s60, 36864\n"
"v_mfma_f32_16x16x32_bf16 v[236:239], a[172:175], a[92:95], v[236:239]\n"
"buffer_load_dwordx4 v2, s[16:19], s61 offen offset:0 sc1 lds\n"
"s_waitcnt lgkmcnt(4)\n"
"v_mfma_f32_16x16x32_bf16 v[184:187], a[144:147], a[96:99], v[184:187]\n"
"ds_read_b128 a[172:175], v5 offset:64512\n"
"buffer_load_dwordx4 v2, s[16:19], s61 offen offset:1024 sc1 lds\n"
"v_mfma_f32_16x16x32_bf16 v[188:191], a[144:147], a[100:103], v[188:191]\n"
"s_or_b32 s72, s58, s54\n"
"s_mul_i32 s74, s54, 8448\n"
"v_mfma_f32_16x16x32_bf16 v[192:195], a[148:151], a[96:99], v[192:195]\n"
"ds_read_b128 a[144:147], v0 offset:0\n"
"s_add_u32 s74, s74, 110592\n"
"v_mfma_f32_16x16x32_bf16 v[196:199], a[148:151], a[100:103], v[196:199]\n"
"s_cmp_eq_u32 s72, 0\n"
"s_cselect_b32 s72, 0, 1073741824\n"
"v_mfma_f32_16x16x32_bf16 v[200:203], a[152:155], a[96:99], v[200:203]\n"
"ds_read_b128 a[148:151], v0 offset:1024\n"
"buffer_load_dwordx4 v2, s[16:19], s61 offen offset:2048 sc1 lds\n"
"v_mfma_f32_16x16x32_bf16 v[204:207], a[152:155], a[100:103], v[204:207]\n"
"v_mov_b32 v34, s72\n"
"s_waitcnt lgkmcnt(4)\n"
"v_mfma_f32_16x16x32_bf16 v[208:211], a[156:159], a[96:99], v[208:211]\n"
"ds_read_b128 a[152:155], v0 offset:2048\n"
"v_mul_u32_u24 v79, 264, v76\n"
"v_mfma_f32_16x16x32_bf16 v[212:215], a[156:159], a[100:103], v[212:215]\n"
"s_mov_b64 exec, 1\n"
"buffer_store_dword v17, v34, s[16:19], 0 offen sc1\n"
"s_mov_b64 exec, -1\n"
"v_mfma_f32_16x16x32_bf16 v[216:219], a[160:163], a[96:99], v[216:219]\n"
"ds_read_b128 a[156:159], v0 offset:3072\n"
"v_add_u32 v79, s74, v79\n"
"v_mfma_f32_16x16x32_bf16 v[220:223], a[160:163], a[100:103], v[220:223]\n"
"v_lshl_add_u32 v23, v78, 4, v79\n"
"v_lshl_add_u32 v24, v78, 3, v79\n"
"v_mfma_f32_16x16x32_bf16 v[224:227], a[164:167], a[96:99], v[224:227]\n"
"ds_read_b128 a[160:163], v0 offset:4096\n"
"buffer_load_dwordx4 v2, s[16:19], s61 offen offset:3072 sc1 lds\n"
"v_mfma_f32_16x16x32_bf16 v[228:231], a[164:167], a[100:103], v[228:231]\n"
"s_add_u32 m0, s60, 40960\n"
"s_waitcnt lgkmcnt(4)\n"
"v_mfma_f32_16x16x32_bf16 v[232:235], a[168:171], a[96:99], v[232:235]\n"
"ds_read_b128 a[164:167], v28 offset:5120\n"
"v_lshrrev_b32 v80, 1, v78\n"
"v_mfma_f32_16x16x32_bf16 v[236:239], a[168:171], a[100:103], v[236:239]\n"
"v_mul_u32_u24 v80, 4224, v80\n"
"v_add_u32 v79, v79, v80\n"
"v_mfma_f32_16x16x32_bf16 v[184:187], a[172:175], a[104:107], v[184:187]\n"
"ds_read_b128 a[168:171], v0 offset:6144\n"
"buffer_load_dwordx4 v3, s[16:19], s61 offen offset:0 sc1 lds\n"
"v_mfma_f32_16x16x32_bf16 v[188:191], a[172:175], a[108:111], v[188:191]\n"
"v_and_b32 v80, 1, v78\n"
"v_mul_u32_u24 v80, 80, v80\n"
"v_mfma_f32_16x16x32_bf16 v[192:195], a[144:147], a[104:107], v[192:195]\n"
"ds_read_b128 a[172:175], v0 offset:7168\n"
"v_add_u32 v22, v79, v80\n"
"v_mfma_f32_16x16x32_bf16 v[196:199], a[144:147], a[108:111], v[196:199]\n"
"s_mov_b32 s82, 0\n"
"s_waitcnt lgkmcnt(4)\n"
"v_mfma_f32_16x16x32_bf16 v[200:203], a[148:151], a[104:107], v[200:203]\n"
"ds_read_b128 a[144:147], v0 offset:8192\n"
"buffer_load_dwordx4 v3, s[16:19], s61 offen offset:1024 sc1 lds\n"
"v_mfma_f32_16x16x32_bf16 v[204:207], a[148:151], a[108:111], v[204:207]\n"
"s_mov_b32 s83, 0xffff0000\n"
"v_mov_b32 v25, s76\n"
"v_mfma_f32_16x16x32_bf16 v[208:211], a[152:155], a[104:107], v[208:211]\n"
"ds_read_b128 a[148:151], v0 offset:9216\n"
"ds_read_b128 v[40:43], v90 offset:0\n"
"v_mfma_f32_16x16x32_bf16 v[212:215], a[152:155], a[108:111], v[212:215]\n"
"ds_read_b128 v[44:47], v91 offset:0\n"
"buffer_load_dwordx4 v3, s[16:19], s61 offen offset:2048 sc1 lds\n"
"v_mfma_f32_16x16x32_bf16 v[216:219], a[156:159], a[104:107], v[216:219]\n"
"ds_read_b128 a[152:155], v0 offset:10240\n"
"ds_read_b64 v[48:49], v92 offset:0\n"
"v_mfma_f32_16x16x32_bf16 v[220:223], a[156:159], a[108:111], v[220:223]\n"
"ds_read_b128 v[50:53], v90 offset:192\n"
"s_waitcnt lgkmcnt(8)\n"
"v_mfma_f32_16x16x32_bf16 v[224:227], a[160:163], a[104:107], v[224:227]\n"
"ds_read_b128 a[156:159], v0 offset:11264\n"
"buffer_load_dwordx4 v3, s[16:19], s61 offen offset:3072 sc1 lds\n"
"v_mfma_f32_16x16x32_bf16 v[228:231], a[160:163], a[108:111], v[228:231]\n"
"s_add_u32 m0, s60, 45056\n"
"ds_read_b128 v[54:57], v91 offset:192\n"
"v_mfma_f32_16x16x32_bf16 v[232:235], a[164:167], a[104:107], v[232:235]\n"
"ds_read_b128 a[160:163], v28 offset:12288\n"
"ds_read_b64 v[58:59], v92 offset:192\n"
"v_mfma_f32_16x16x32_bf16 v[236:239], a[164:167], a[108:111], v[236:239]\n"
"ds_read_b128 v[60:63], v90 offset:384\n"
"buffer_load_dwordx4 v4, s[16:19], s61 offen offset:0 sc1 lds\n"
"v_mfma_f32_16x16x32_bf16 v[184:187], a[168:171], a[112:115], v[184:187]\n"
"ds_read_b128 a[164:167], v0 offset:13312\n"
"s_add_u32 s61, s61, 36864\n"
"v_mfma_f32_16x16x32_bf16 v[188:191], a[168:171], a[116:119], v[188:191]\n"
"s_add_u32 m0, s60, 73728\n"
"s_waitcnt lgkmcnt(11)\n"
"v_mfma_f32_16x16x32_bf16 v[192:195], a[172:175], a[112:115], v[192:195]\n"
"ds_read_b128 a[168:171], v0 offset:14336\n"
"ds_read_b128 v[64:67], v91 offset:384\n"
"v_mfma_f32_16x16x32_bf16 v[196:199], a[172:175], a[116:119], v[196:199]\n"
"ds_read_b64 v[68:69], v92 offset:384\n"
"ds_read_b128 v[70:73], v90 offset:576\n"
"v_mfma_f32_16x16x32_bf16 v[200:203], a[144:147], a[112:115], v[200:203]\n"
"ds_read_b128 a[172:175], v0 offset:15360\n"
"ds_read_b128 v[74:77], v91 offset:576\n"
"v_mfma_f32_16x16x32_bf16 v[204:207], a[144:147], a[116:119], v[204:207]\n"
"ds_read_b64 v[78:79], v92 offset:576\n"
"ds_read_b128 v[80:83], v90 offset:768\n"
"v_mfma_f32_16x16x32_bf16 v[208:211], a[148:151], a[112:115], v[208:211]\n"
"ds_read_b128 v[84:87], v91 offset:768\n"
"ds_read_b64 v[88:89], v92 offset:768\n"
"v_mfma_f32_16x16x32_bf16 v[212:215], a[148:151], a[116:119], v[212:215]\n"
"v_or_b32 v240, v240, v18\n"
"s_waitcnt lgkmcnt(13)\n"
"v_mfma_f32_16x16x32_bf16 v[216:219], a[152:155], a[112:115], v[216:219]\n"
"buffer_load_dwordx4 v2, s[16:19], s61 offen offset:0 sc1 lds\n"
"v_or_b32 v241, v241, v19\n"
"v_mfma_f32_16x16x32_bf16 v[220:223], a[152:155], a[116:119], v[220:223]\n"
"s_waitcnt lgkmcnt(5)\n"
"s_barrier\n"
"v_mfma_f32_16x16x32_bf16 v[224:227], a[156:159], a[112:115], v[224:227]\n"
"ds_read_b128 a[144:147], v2 offset:0\n"
"v_or_b32 v242, v242, v20\n"
"v_mfma_f32_16x16x32_bf16 v[228:231], a[156:159], a[116:119], v[228:231]\n"
"ds_read_b128 a[148:151], v2 offset:1024\n"
"v_or_b32 v243, v243, v21\n"
"v_mfma_f32_16x16x32_bf16 v[232:235], a[160:163], a[112:115], v[232:235]\n"
"ds_read_b128 a[152:155], v2 offset:2048\n"
"buffer_load_dwordx4 v2, s[16:19], s61 offen offset:1024 sc1 lds\n"
"v_mfma_f32_16x16x32_bf16 v[236:239], a[160:163], a[116:119], v[236:239]\n"
"ds_read_b128 a[156:159], v26 offset:3072\n"
"v_mfma_f32_16x16x32_bf16 v[184:187], a[164:167], a[120:123], v[184:187]\n"
"ds_read_b128 a[160:163], v2 offset:4096\n"
"v_perm_b32 v96, v242, v240, s78\n"
"v_mfma_f32_16x16x32_bf16 v[188:191], a[164:167], a[124:127], v[188:191]\n"
"v_perm_b32 v97, v246, v244, s78\n"
"v_perm_b32 v98, v250, v248, s78\n"
"v_mfma_f32_16x16x32_bf16 v[192:195], a[168:171], a[120:123], v[192:195]\n"
"ds_read_b128 a[164:167], v2 offset:5120\n"
"buffer_load_dwordx4 v2, s[16:19], s61 offen offset:2048 sc1 lds\n"
"v_mfma_f32_16x16x32_bf16 v[196:199], a[168:171], a[124:127], v[196:199]\n"
"v_perm_b32 v99, v254, v252, s78\n"
"v_perm_b32 v100, v242, v240, s79\n"
"v_mfma_f32_16x16x32_bf16 v[200:203], a[172:175], a[120:123], v[200:203]\n"
"ds_read_b128 a[168:171], v2 offset:6144\n"
"v_perm_b32 v101, v246, v244, s79\n"
"v_mfma_f32_16x16x32_bf16 v[204:207], a[172:175], a[124:127], v[204:207]\n"
"v_perm_b32 v102, v250, v248, s79\n"
"s_waitcnt lgkmcnt(4)\n"
"v_mfma_f32_16x16x32_bf16 v[208:211], a[144:147], a[120:123], v[208:211]\n"
"ds_read_b128 a[172:175], v2 offset:7168\n"
"buffer_load_dwordx4 v2, s[16:19], s61 offen offset:3072 sc1 lds\n"
"v_mfma_f32_16x16x32_bf16 v[212:215], a[144:147], a[124:127], v[212:215]\n"
"s_add_u32 m0, s60, 77824\n"
"v_perm_b32 v103, v254, v252, s79\n"
"v_mfma_f32_16x16x32_bf16 v[216:219], a[148:151], a[120:123], v[216:219]\n"
"ds_read_b128 a[144:147], v2 offset:8192\n"
"v_perm_b32 v104, v243, v241, s78\n"
"v_mfma_f32_16x16x32_bf16 v[220:223], a[148:151], a[124:127], v[220:223]\n"
"v_perm_b32 v105, v247, v245, s78\n"
"v_perm_b32 v106, v251, v249, s78\n"
"v_mfma_f32_16x16x32_bf16 v[224:227], a[152:155], a[120:123], v[224:227]\n"
"ds_read_b128 a[148:151], v2 offset:9216\n"
"buffer_load_dwordx4 v3, s[16:19], s61 offen offset:0 sc1 lds\n"
"v_mfma_f32_16x16x32_bf16 v[228:231], a[152:155], a[124:127], v[228:231]\n"
"v_perm_b32 v107, v255, v253, s78\n"
"s_waitcnt lgkmcnt(4)\n"
"v_mfma_f32_16x16x32_bf16 v[232:235], a[156:159], a[120:123], v[232:235]\n"
"ds_read_b128 a[152:155], v26 offset:10240\n"
"v_perm_b32 v108, v243, v241, s79\n"
"v_mfma_f32_16x16x32_bf16 v[236:239], a[156:159], a[124:127], v[236:239]\n"
"v_perm_b32 v109, v247, v245, s79\n"
"v_perm_b32 v110, v251, v249, s79\n"
"v_mfma_f32_16x16x32_bf16 v[184:187], a[160:163], a[128:131], v[184:187]\n"
"ds_read_b128 a[156:159], v2 offset:11264\n"
"buffer_load_dwordx4 v3, s[16:19], s61 offen offset:1024 sc1 lds\n"
"v_mfma_f32_16x16x32_bf16 v[188:191], a[160:163], a[132:135], v[188:191]\n"
"v_perm_b32 v111, v255, v253, s79\n"
"v_mfma_f32_16x16x32_bf16 v[192:195], a[164:167], a[128:131], v[192:195]\n"
"ds_read_b128 a[160:163], v2 offset:12288\n"
"buffer_store_dwordx4 v[96:99], v32, s[16:19], s94 offen sc1\n"
"v_mfma_f32_16x16x32_bf16 v[196:199], a[164:167], a[132:135], v[196:199]\n"
"s_waitcnt lgkmcnt(4)\n"
"v_mfma_f32_16x16x32_bf16 v[200:203], a[168:171], a[128:131], v[200:203]\n"
"ds_read_b128 a[164:167], v2 offset:13312\n"
"v_mfma_f32_16x16x32_bf16 v[204:207], a[168:171], a[132:135], v[204:207]\n"
"v_rcp_f32 v37, v37\n"
"v_mfma_f32_16x16x32_bf16 v[208:211], a[172:175], a[128:131], v[208:211]\n"
"ds_read_b128 a[168:171], v2 offset:14336\n"
"buffer_load_dwordx4 v3, s[16:19], s61 offen offset:2048 sc1 lds\n"
"v_mfma_f32_16x16x32_bf16 v[212:215], a[172:175], a[132:135], v[212:215]\n"
"v_mul_f32 v112, v112, v7\n"
"v_mul_f32 v113, v113, v16\n"
"v_mfma_f32_16x16x32_bf16 v[216:219], a[144:147], a[128:131], v[216:219]\n"
"ds_read_b128 a[172:175], v2 offset:15360\n"
"v_mul_f32 v114, v114, v37\n"
"v_mfma_f32_16x16x32_bf16 v[220:223], a[144:147], a[132:135], v[220:223]\n"
"v_mul_f32 v7, v115, v115\n"
"s_waitcnt lgkmcnt(4)\n"
"v_mfma_f32_16x16x32_bf16 v[224:227], a[148:151], a[128:131], v[224:227]\n"
"ds_read_b128 a[144:147], v2 offset:16384\n"
"buffer_load_dwordx4 v3, s[16:19], s61 offen offset:3072 sc1 lds\n"
"v_mfma_f32_16x16x32_bf16 v[228:231], a[148:151], a[132:135], v[228:231]\n"
"s_add_u32 m0, s60, 81920\n"
"v_mul_f32 v16, v116, v116\n"
"v_mfma_f32_16x16x32_bf16 v[232:235], a[152:155], a[128:131], v[232:235]\n"
"ds_read_b128 a[148:151], v26 offset:17408\n"
"v_mul_f32 v37, v117, v117\n"
"v_mfma_f32_16x16x32_bf16 v[236:239], a[152:155], a[132:135], v[236:239]\n"
"v_fma_f32 v7, v7, s80, v38\n"
"v_fma_f32 v16, v16, s80, v38\n"
"v_mfma_f32_16x16x32_bf16 v[184:187], a[156:159], a[136:139], v[184:187]\n"
"buffer_load_dwordx4 v4, s[16:19], s61 offen offset:0 sc1 lds\n"
"buffer_store_dwordx4 v[100:103], v32, s[16:19], s95 offen sc1\n"
"v_mfma_f32_16x16x32_bf16 v[188:191], a[156:159], a[140:143], v[188:191]\n"
"v_fma_f32 v37, v37, s80, v38\n"
"s_waitcnt lgkmcnt(3)\n"
"v_mfma_f32_16x16x32_bf16 v[192:195], a[160:163], a[136:139], v[192:195]\n"
"v_mul_f32 v7, v7, v115\n"
"v_mul_f32 v16, v16, v116\n"
"v_mfma_f32_16x16x32_bf16 v[196:199], a[160:163], a[140:143], v[196:199]\n"
"v_mul_f32 v37, v37, v117\n"
"v_mfma_f32_16x16x32_bf16 v[200:203], a[164:167], a[136:139], v[200:203]\n"
"v_exp_f32 v7, v7\n"
"v_mfma_f32_16x16x32_bf16 v[204:207], a[164:167], a[140:143], v[204:207]\n"
"v_exp_f32 v16, v16\n"
"v_mfma_f32_16x16x32_bf16 v[208:211], a[168:171], a[136:139], v[208:211]\n"
"v_exp_f32 v37, v37\n"
"v_mfma_f32_16x16x32_bf16 v[212:215], a[168:171], a[140:143], v[212:215]\n"
"v_add_f32 v7, 1.0, v7\n"
"s_waitcnt lgkmcnt(0)\n"
"v_mfma_f32_16x16x32_bf16 v[216:219], a[172:175], a[136:139], v[216:219]\n"
"v_add_f32 v16, 1.0, v16\n"
"v_add_f32 v37, 1.0, v37\n"
"v_mfma_f32_16x16x32_bf16 v[220:223], a[172:175], a[140:143], v[220:223]\n"
"v_rcp_f32 v7, v7\n"
"v_mfma_f32_16x16x32_bf16 v[224:227], a[144:147], a[136:139], v[224:227]\n"
"v_rcp_f32 v16, v16\n"
"v_mfma_f32_16x16x32_bf16 v[228:231], a[144:147], a[140:143], v[228:231]\n"
"v_rcp_f32 v37, v37\n"
"v_mfma_f32_16x16x32_bf16 v[232:235], a[148:151], a[136:139], v[232:235]\n"
"buffer_store_dwordx4 v[104:107], v32, s[16:19], s96 offen sc1\n"
"v_mul_f32 v115, v115, v7\n"
"v_mfma_f32_16x16x32_bf16 v[236:239], a[148:151], a[140:143], v[236:239]\n"
"v_mul_f32 v116, v116, v16\n"
"v_mul_f32 v117, v117, v37\n"
"buffer_store_dwordx4 v[108:111], v32, s[16:19], s97 offen sc1\n"
"s_waitcnt vmcnt(28)\n"
"v_lshlrev_b32 v8, 16, v8\n"
"s_waitcnt vmcnt(27)\n"
"v_lshlrev_b32 v12, 16, v12\n"
"v_mov_b32 v9, v8\n"
"v_mov_b32 v10, v8\n"
"v_mov_b32 v11, v8\n"
"v_mov_b32 v13, v12\n"
"v_mov_b32 v14, v12\n"
"v_mov_b32 v15, v12\n"
"v_cvt_pk_bf16_f32 v200, v200, v208\n"
"v_cvt_pk_bf16_f32 v201, v201, v209\n"
"v_cvt_pk_bf16_f32 v202, v202, v210\n"
"v_cvt_pk_bf16_f32 v203, v203, v211\n"
"v_cvt_pk_bf16_f32 v204, v204, v212\n"
"v_cvt_pk_bf16_f32 v205, v205, v213\n"
"v_cvt_pk_bf16_f32 v206, v206, v214\n"
"v_cvt_pk_bf16_f32 v207, v207, v215\n"
"v_mfma_f32_16x16x32_bf16 v[208:211], v[40:43], v[200:203], 0\n"
"v_cvt_pk_bf16_f32 v184, v184, v192\n"
"v_cvt_pk_bf16_f32 v185, v185, v193\n"
"v_mfma_f32_16x16x32_bf16 v[92:95], v[40:43], v[204:207], 0\n"
"v_cvt_pk_bf16_f32 v186, v186, v194\n"
"v_cvt_pk_bf16_f32 v187, v187, v195\n"
"v_mfma_f32_16x16x32_bf16 v[240:243], v[50:53], v[200:203], 0\n"
"v_cvt_pk_bf16_f32 v188, v188, v196\n"
"v_cvt_pk_bf16_f32 v189, v189, v197\n"
"v_mfma_f32_16x16x32_bf16 v[96:99], v[50:53], v[204:207], 0\n"
"v_cvt_pk_bf16_f32 v190, v190, v198\n"
"v_cvt_pk_bf16_f32 v191, v191, v199\n"
"v_mfma_f32_16x16x32_bf16 v[244:247], v[60:63], v[200:203], 0\n"
"v_cvt_pk_bf16_f32 v232, v232, v233\n"
"v_cvt_pk_bf16_f32 v233, v234, v235\n"
"v_mfma_f32_16x16x32_bf16 v[100:103], v[60:63], v[204:207], 0\n"
"v_cvt_pk_bf16_f32 v236, v236, v237\n"
"v_cvt_pk_bf16_f32 v237, v238, v239\n"
"v_mfma_f32_16x16x32_bf16 v[248:251], v[70:73], v[200:203], 0\n"
"v_cndmask_b32 v234, v232, v25, s[82:83]\n"
"v_cndmask_b32 v235, v233, v25, s[82:83]\n"
"v_mfma_f32_16x16x32_bf16 v[104:107], v[70:73], v[204:207], 0\n"
"v_cndmask_b32 v238, v236, v25, s[82:83]\n"
"v_cndmask_b32 v239, v237, v25, s[82:83]\n"
"v_mfma_f32_16x16x32_bf16 v[252:255], v[80:83], v[200:203], 0\n"
"v_cvt_pk_bf16_f32 v216, v216, v224\n"
"v_cvt_pk_bf16_f32 v217, v217, v225\n"
"v_mfma_f32_16x16x32_bf16 v[108:111], v[80:83], v[204:207], 0\n"
"v_cvt_pk_bf16_f32 v218, v218, v226\n"
"v_cvt_pk_bf16_f32 v219, v219, v227\n"
"v_mfma_f32_16x16x32_bf16 v[208:211], v[44:47], v[184:187], v[208:211]\n"
"v_cvt_pk_bf16_f32 v220, v220, v228\n"
"v_cvt_pk_bf16_f32 v221, v221, v229\n"
"v_mfma_f32_16x16x32_bf16 v[92:95], v[44:47], v[188:191], v[92:95]\n"
"buffer_load_dwordx4 a[236:239], v3, s[16:19], s62 offen offset:3072 sc1\n"
"s_add_u32 s62, s62, 8192\n"
"v_mfma_f32_16x16x16_bf16 v[208:211], v[48:49], v[234:235], v[208:211]\n"
"v_cvt_pk_bf16_f32 v222, v222, v230\n"
"v_cvt_pk_bf16_f32 v223, v223, v231\n"
"v_mfma_f32_16x16x16_bf16 v[92:95], v[48:49], v[238:239], v[92:95]\n"
"ds_write_b64 v23, v[216:217] offset:160\n"
"v_mfma_f32_16x16x32_bf16 v[240:243], v[54:57], v[184:187], v[240:243]\n"
"ds_write_b64 v23, v[218:219] offset:168\n"
"v_mfma_f32_16x16x32_bf16 v[96:99], v[54:57], v[188:191], v[96:99]\n"
"buffer_load_dwordx4 a[240:243], v2, s[16:19], s62 offen offset:0 sc1\n"
"ds_write_b64 v24, v[234:235] offset:224\n"
"v_mfma_f32_16x16x16_bf16 v[240:243], v[58:59], v[234:235], v[240:243]\n"
"ds_write_b64 v23, v[220:221] offset:4384\n"
"v_mfma_f32_16x16x16_bf16 v[96:99], v[58:59], v[238:239], v[96:99]\n"
"ds_write_b64 v23, v[222:223] offset:4392\n"
"v_mfma_f32_16x16x32_bf16 v[244:247], v[64:67], v[184:187], v[244:247]\n"
"ds_write_b64 v24, v[238:239] offset:4448\n"
"v_mfma_f32_16x16x32_bf16 v[100:103], v[64:67], v[188:191], v[100:103]\n"
"buffer_load_dwordx4 a[244:247], v2, s[16:19], s62 offen offset:1024 sc1\n"
"v_mfma_f32_16x16x16_bf16 v[244:247], v[68:69], v[234:235], v[244:247]\n"
"v_permlane32_swap_b32 v208, v92\n"
"v_mfma_f32_16x16x16_bf16 v[100:103], v[68:69], v[238:239], v[100:103]\n"
"v_permlane32_swap_b32 v209, v93\n"
"v_mfma_f32_16x16x32_bf16 v[248:251], v[74:77], v[184:187], v[248:251]\n"
"v_permlane32_swap_b32 v210, v94\n"
"v_mfma_f32_16x16x32_bf16 v[104:107], v[74:77], v[188:191], v[104:107]\n"
"buffer_load_dwordx4 a[248:251], v2, s[16:19], s62 offen offset:2048 sc1\n"
"v_mfma_f32_16x16x16_bf16 v[248:251], v[78:79], v[234:235], v[248:251]\n"
"v_permlane32_swap_b32 v211, v95\n"
"v_mfma_f32_16x16x16_bf16 v[104:107], v[78:79], v[238:239], v[104:107]\n"
"v_permlane32_swap_b32 v240, v96\n"
"v_mfma_f32_16x16x32_bf16 v[252:255], v[84:87], v[184:187], v[252:255]\n"
"v_permlane32_swap_b32 v241, v97\n"
"v_mfma_f32_16x16x32_bf16 v[108:111], v[84:87], v[188:191], v[108:111]\n"
"v_permlane32_swap_b32 v242, v98\n"
"v_mfma_f32_16x16x16_bf16 v[252:255], v[88:89], v[234:235], v[252:255]\n"
"buffer_load_dwordx4 a[0:3], v2, s[16:19], s62 offen offset:3072 sc1\n"
"v_mfma_f32_16x16x16_bf16 v[108:111], v[88:89], v[238:239], v[108:111]\n"
"v_permlane32_swap_b32 v243, v99\n"
"v_permlane32_swap_b32 v244, v100\n"
"v_permlane32_swap_b32 v245, v101\n"
"v_permlane32_swap_b32 v246, v102\n"
"v_permlane32_swap_b32 v247, v103\n"
"v_permlane32_swap_b32 v248, v104\n"
"v_permlane32_swap_b32 v249, v105\n"
"v_permlane32_swap_b32 v250, v106\n"
"v_permlane32_swap_b32 v251, v107\n"
"v_permlane32_swap_b32 v252, v108\n"
"v_permlane32_swap_b32 v253, v109\n"
"s_mov_b32 s74, 0x3fb8aa3b\n"
"buffer_load_dwordx4 a[4:7], v3, s[16:19], s62 offen offset:0 sc1\n"
"s_mov_b32 s75, 0xbfb8aa3b\n"
"buffer_load_dwordx4 a[8:11], v3, s[16:19], s62 offen offset:1024 sc1\n"
"v_max3_f32 v80, v92, v93, v94\n"
"v_min3_f32 v82, v92, v93, v94\n"
"v_max3_f32 v80, v80, v95, v96\n"
"v_min3_f32 v82, v82, v95, v96\n"
"v_max3_f32 v80, v80, v97, v98\n"
"v_min3_f32 v82, v82, v97, v98\n"
"v_max3_f32 v80, v80, v99, v100\n"
"v_min3_f32 v82, v82, v99, v100\n"
"v_max3_f32 v80, v80, v101, v102\n"
"v_min3_f32 v82, v82, v101, v102\n"
"v_max3_f32 v80, v80, v103, v104\n"
"v_min3_f32 v82, v82, v103, v104\n"
"v_max3_f32 v80, v80, v105, v106\n"
"buffer_load_dwordx4 a[12:15], v3, s[16:19], s62 offen offset:2048 sc1\n"
"v_min3_f32 v82, v82, v105, v106\n"
"v_max3_f32 v80, v80, v107, v108\n"
"v_min3_f32 v82, v82, v107, v108\n"
"v_max_f32 v80, v80, v109\n"
"v_min_f32 v82, v82, v109\n"
"v_mul_f32 v80, s75, v80\n"
"v_mul_f32 v82, s74, v82\n"
"v_pk_fma_f32 v[40:41], v[92:93], s[74:75], v[80:81] op_sel:[0,0,0] op_sel_hi:[1,0,0]\n"
"v_pk_fma_f32 v[92:93], v[92:93], s[74:75], v[82:83] op_sel:[0,1,0] op_sel_hi:[1,1,0]\n"
"v_exp_f32 v40, v40\n"
"v_exp_f32 v41, v41\n"
"buffer_load_dwordx4 a[16:19], v3, s[16:19], s62 offen offset:3072 sc1\n"
"s_add_u32 s62, s62, 8192\n"
"v_exp_f32 v92, v92\n"
"v_exp_f32 v93, v93\n"
"v_pk_fma_f32 v[42:43], v[94:95], s[74:75], v[80:81] op_sel:[0,0,0] op_sel_hi:[1,0,0]\n"
"v_pk_fma_f32 v[94:95], v[94:95], s[74:75], v[82:83] op_sel:[0,1,0] op_sel_hi:[1,1,0]\n"
"v_exp_f32 v42, v42\n"
"v_exp_f32 v43, v43\n"
"v_exp_f32 v94, v94\n"
"v_exp_f32 v95, v95\n"
"buffer_load_dwordx4 a[20:23], v2, s[16:19], s62 offen offset:0 sc1\n"
"v_pk_fma_f32 v[44:45], v[96:97], s[74:75], v[80:81] op_sel:[0,0,0] op_sel_hi:[1,0,0]\n"
"v_pk_fma_f32 v[96:97], v[96:97], s[74:75], v[82:83] op_sel:[0,1,0] op_sel_hi:[1,1,0]\n"
"v_exp_f32 v44, v44\n"
"v_exp_f32 v45, v45\n"
"v_exp_f32 v96, v96\n"
"v_exp_f32 v97, v97\n"
"v_pk_add_f32 v[84:85], v[40:41], v[42:43]\n"
"v_pk_add_f32 v[86:87], v[92:93], v[94:95]\n"
"v_pk_fma_f32 v[46:47], v[98:99], s[74:75], v[80:81] op_sel:[0,0,0] op_sel_hi:[1,0,0]\n"
"buffer_load_dwordx4 a[24:27], v2, s[16:19], s62 offen offset:1024 sc1\n"
"v_pk_fma_f32 v[98:99], v[98:99], s[74:75], v[82:83] op_sel:[0,1,0] op_sel_hi:[1,1,0]\n"
"v_exp_f32 v46, v46\n"
"v_exp_f32 v47, v47\n"
"v_exp_f32 v98, v98\n"
"v_exp_f32 v99, v99\n"
"v_pk_add_f32 v[84:85], v[84:85], v[44:45]\n"
"v_pk_add_f32 v[86:87], v[86:87], v[96:97]\n"
"v_pk_fma_f32 v[48:49], v[100:101], s[74:75], v[80:81] op_sel:[0,0,0] op_sel_hi:[1,0,0]\n"
"v_pk_fma_f32 v[100:101], v[100:101], s[74:75], v[82:83] op_sel:[0,1,0] op_sel_hi:[1,1,0]\n"
"v_exp_f32 v48, v48\n"
"buffer_load_dwordx4 a[28:31], v2, s[16:19], s62 offen offset:2048 sc1\n"
"v_exp_f32 v49, v49\n"
"v_exp_f32 v100, v100\n"
"v_exp_f32 v101, v101\n"
"v_pk_add_f32 v[84:85], v[84:85], v[46:47]\n"
"v_pk_add_f32 v[86:87], v[86:87], v[98:99]\n"
"v_pk_fma_f32 v[50:51], v[102:103], s[74:75], v[80:81] op_sel:[0,0,0] op_sel_hi:[1,0,0]\n"
"v_pk_fma_f32 v[102:103], v[102:103], s[74:75], v[82:83] op_sel:[0,1,0] op_sel_hi:[1,1,0]\n"
"v_exp_f32 v50, v50\n"
"v_exp_f32 v51, v51\n"
"buffer_load_dwordx4 a[32:35], v2, s[16:19], s62 offen offset:3072 sc1\n"
"v_exp_f32 v102, v102\n"
"v_exp_f32 v103, v103\n"
"v_pk_add_f32 v[84:85], v[84:85], v[48:49]\n"
"v_pk_add_f32 v[86:87], v[86:87], v[100:101]\n"
"v_pk_fma_f32 v[52:53], v[104:105], s[74:75], v[80:81] op_sel:[0,0,0] op_sel_hi:[1,0,0]\n"
"v_pk_fma_f32 v[104:105], v[104:105], s[74:75], v[82:83] op_sel:[0,1,0] op_sel_hi:[1,1,0]\n"
"v_exp_f32 v52, v52\n"
"v_exp_f32 v53, v53\n"
"buffer_load_dwordx4 a[36:39], v3, s[16:19], s62 offen offset:0 sc1\n"
"v_exp_f32 v104, v104\n"
"v_exp_f32 v105, v105\n"
"v_pk_add_f32 v[84:85], v[84:85], v[50:51]\n"
"v_pk_add_f32 v[86:87], v[86:87], v[102:103]\n"
"v_pk_fma_f32 v[54:55], v[106:107], s[74:75], v[80:81] op_sel:[0,0,0] op_sel_hi:[1,0,0]\n"
"v_pk_fma_f32 v[106:107], v[106:107], s[74:75], v[82:83] op_sel:[0,1,0] op_sel_hi:[1,1,0]\n"
"v_exp_f32 v54, v54\n"
"v_exp_f32 v55, v55\n"
"v_exp_f32 v106, v106\n"
"buffer_load_dwordx4 a[40:43], v3, s[16:19], s62 offen offset:1024 sc1\n"
"v_exp_f32 v107, v107\n"
"v_pk_add_f32 v[84:85], v[84:85], v[52:53]\n"
"v_pk_add_f32 v[86:87], v[86:87], v[104:105]\n"
"v_pk_fma_f32 v[56:57], v[108:109], s[74:75], v[80:81] op_sel:[0,0,0] op_sel_hi:[1,0,0]\n"
"v_pk_fma_f32 v[108:109], v[108:109], s[74:75], v[82:83] op_sel:[0,1,0] op_sel_hi:[1,1,0]\n"
"v_exp_f32 v56, v56\n"
"v_exp_f32 v57, v57\n"
"v_exp_f32 v108, v108\n"
"v_exp_f32 v109, v109\n"
"buffer_load_dwordx4 a[44:47], v3, s[16:19], s62 offen offset:2048 sc1\n"
"v_pk_add_f32 v[84:85], v[84:85], v[54:55]\n"
"v_pk_add_f32 v[86:87], v[86:87], v[106:107]\n"
"v_pk_add_f32 v[84:85], v[84:85], v[56:57]\n"
"v_pk_add_f32 v[86:87], v[86:87], v[108:109]\n"
"v_add_f32 v84, v84, v85\n"
"v_add_f32 v86, v86, v87\n"
"v_rcp_f32 v84, v84\n"
"v_rcp_f32 v86, v86\n"
"v_pk_mul_f32 v[40:41], v[40:41], v[84:85] op_sel_hi:[1,0]\n"
"v_pk_mul_f32 v[42:43], v[42:43], v[84:85] op_sel_hi:[1,0]\n"
"v_pk_mul_f32 v[44:45], v[44:45], v[84:85] op_sel_hi:[1,0]\n"
"buffer_load_dwordx4 a[48:51], v3, s[16:19], s62 offen offset:3072 sc1\n"
"s_add_u32 s62, s62, 8192\n"
"v_pk_mul_f32 v[46:47], v[46:47], v[84:85] op_sel_hi:[1,0]\n"
"v_pk_mul_f32 v[48:49], v[48:49], v[84:85] op_sel_hi:[1,0]\n"
"v_pk_mul_f32 v[50:51], v[50:51], v[84:85] op_sel_hi:[1,0]\n"
"v_pk_mul_f32 v[52:53], v[52:53], v[84:85] op_sel_hi:[1,0]\n"
"v_pk_mul_f32 v[54:55], v[54:55], v[84:85] op_sel_hi:[1,0]\n"
"v_pk_mul_f32 v[56:57], v[56:57], v[84:85] op_sel_hi:[1,0]\n"
"v_pk_mul_f32 v[92:93], v[92:93], v[86:87] op_sel_hi:[1,0]\n"
"v_pk_mul_f32 v[94:95], v[94:95], v[86:87] op_sel_hi:[1,0]\n"
"v_pk_mul_f32 v[96:97], v[96:97], v[86:87] op_sel_hi:[1,0]\n"
"v_pk_mul_f32 v[98:99], v[98:99], v[86:87] op_sel_hi:[1,0]\n"
"v_pk_mul_f32 v[100:101], v[100:101], v[86:87] op_sel_hi:[1,0]\n"
"v_pk_mul_f32 v[102:103], v[102:103], v[86:87] op_sel_hi:[1,0]\n"
"buffer_load_dwordx4 a[52:55], v2, s[16:19], s62 offen offset:0 sc1\n"
"v_pk_mul_f32 v[104:105], v[104:105], v[86:87] op_sel_hi:[1,0]\n"
"v_pk_mul_f32 v[106:107], v[106:107], v[86:87] op_sel_hi:[1,0]\n"
"v_pk_mul_f32 v[108:109], v[108:109], v[86:87] op_sel_hi:[1,0]\n"
"v_cvt_pk_bf16_f32 v60, v40, v41\n"
"v_cvt_pk_bf16_f32 v61, v42, v43\n"
"v_cvt_pk_bf16_f32 v62, v44, v45\n"
"v_cvt_pk_bf16_f32 v63, v46, v47\n"
"ds_write_b64 v22, v[60:61] offset:0\n"
"v_cvt_pk_bf16_f32 v64, v48, v49\n"
"v_cvt_pk_bf16_f32 v65, v50, v51\n"
"ds_write_b64 v22, v[62:63] offset:8\n"
"v_cvt_pk_bf16_f32 v66, v52, v53\n"
"v_cvt_pk_bf16_f32 v67, v54, v55\n"
"buffer_load_dwordx4 a[56:59], v2, s[16:19], s62 offen offset:1024 sc1\n"
"ds_write_b64 v22, v[64:65] offset:16\n"
"v_cvt_pk_bf16_f32 v68, v56, v57\n"
"v_cvt_pk_bf16_f32 v69, v92, v93\n"
"ds_write_b64 v22, v[66:67] offset:24\n"
"v_cvt_pk_bf16_f32 v70, v94, v95\n"
"v_cvt_pk_bf16_f32 v71, v96, v97\n"
"ds_write_b64 v22, v[68:69] offset:32\n"
"v_cvt_pk_bf16_f32 v72, v98, v99\n"
"v_cvt_pk_bf16_f32 v73, v100, v101\n"
"ds_write_b64 v22, v[70:71] offset:40\n"
"v_cvt_pk_bf16_f32 v74, v102, v103\n"
"v_cvt_pk_bf16_f32 v75, v104, v105\n"
"ds_write_b64 v22, v[72:73] offset:48\n"
"v_cvt_pk_bf16_f32 v76, v106, v107\n"
"buffer_load_dwordx4 a[60:63], v2, s[16:19], s62 offen offset:2048 sc1\n"
"v_cvt_pk_bf16_f32 v77, v108, v109\n"
"ds_write_b64 v22, v[74:75] offset:56\n"
"ds_write_b64 v22, v[76:77] offset:64\n"
"s_waitcnt vmcnt(33)\n"
"s_waitcnt lgkmcnt(0)\n"
"s_barrier\n"
"ds_read_b128 a[152:155], v2 offset:36864\n"
"ds_read_b128 a[156:159], v2 offset:37888\n"
"ds_read_b128 a[160:163], v2 offset:38912\n"
"ds_read_b128 a[164:167], v2 offset:39936\n"
"ds_read_b128 a[168:171], v2 offset:40960\n"
"ds_read_b128 a[172:175], v2 offset:41984\n"
"ds_read_b128 a[144:147], v2 offset:43008\n"
"s_waitcnt lgkmcnt(6)\n"
"v_mfma_f32_16x16x32_bf16 v[40:43], a[152:155], a[176:179], v[8:11]\n"
".Lmk_PCHERE_4:\n"
"s_add_u32 s0, s0, .Lmk_PCHERE_4-.Lmk_PCHERE_3\n"
"s_addc_u32 s1, s1, 0\n"
"s_sub_u32 s2, s2, .Lmk_PCHERE_4-.Lmk_PCHERE_3\n"
"s_max_i32 s2, s2, 0\n"
"buffer_load_dword a255, off, s[0:3], 0\n"
"ds_read_b128 a[148:151], v2 offset:44032\n"
"s_add_u32 s61, s61, 36864\n"
"v_and_b32 v33, 15, v1\n"
"v_mfma_f32_16x16x32_bf16 v[44:47], a[152:155], a[180:183], v[12:15]\n"
"s_add_u32 m0, s60, 0\n"
"s_nop 0\n"
"buffer_load_dwordx4 v2, s[16:19], s61 offen offset:0 sc1 lds\n"
"s_waitcnt lgkmcnt(6)\n"
"v_mfma_f32_16x16x32_bf16 v[48:51], a[156:159], a[176:179], v[8:11]\n"
"ds_read_b128 a[152:155], v2 offset:45056\n"
"v_lshrrev_b32 v34, 4, v1\n"
"v_mfma_f32_16x16x32_bf16 v[52:55], a[156:159], a[180:183], v[12:15]\n"
"v_bfe_u32 v35, v1, 2, 3\n"
"v_lshrrev_b32 v36, 5, v1\n"
"s_waitcnt lgkmcnt(6)\n"
"v_mfma_f32_16x16x32_bf16 v[56:59], a[160:163], a[176:179], v[8:11]\n"
"ds_read_b128 a[156:159], v2 offset:46080\n"
"v_lshl_add_u32 v35, v35, 2, v36\n"
"v_mul_u32_u24 v35, 264, v35\n"
"v_mfma_f32_16x16x32_bf16 v[60:63], a[160:163], a[180:183], v[12:15]\n"
"buffer_load_dwordx4 v2, s[16:19], s61 offen offset:1024 sc1 lds\n"
"s_waitcnt lgkmcnt(4)\n"
"v_mfma_f32_16x16x32_bf16 v[64:67], a[164:167], a[176:179], v[8:11]\n"
"ds_read_b128 a[160:163], v2 offset:47104\n"
"v_and_b32 v36, 3, v1\n"
"v_mfma_f32_16x16x32_bf16 v[68:71], a[164:167], a[180:183], v[12:15]\n"
"v_lshl_add_u32 v6, v36, 3, v35\n"
"s_and_b32 s64, s54, 1\n"
"v_mfma_f32_16x16x32_bf16 v[72:75], a[168:171], a[176:179], v[8:11]\n"
"ds_read_b128 a[164:167], v2 offset:48128\n"
"s_mul_i32 s65, s64, 80\n"
"s_add_u32 s65, s65, 110592\n"
"v_mfma_f32_16x16x32_bf16 v[76:79], a[168:171], a[180:183], v[12:15]\n"
"buffer_load_dwordx4 v2, s[16:19], s61 offen offset:2048 sc1 lds\n"
"v_add_u32 v18, s65, v6\n"
"v_mfma_f32_16x16x32_bf16 v[80:83], a[172:175], a[176:179], v[8:11]\n"
"ds_read_b128 a[168:171], v2 offset:49152\n"
"s_lshl_b32 s65, s64, 5\n"
"v_mfma_f32_16x16x32_bf16 v[84:87], a[172:175], a[180:183], v[12:15]\n"
"s_add_u32 s65, s65, 110752\n"
"s_waitcnt lgkmcnt(4)\n"
"v_mfma_f32_16x16x32_bf16 v[88:91], a[144:147], a[176:179], v[8:11]\n"
"ds_read_b128 a[172:175], v2 offset:50176\n"
"v_mul_f32 v7, v118, v118\n"
"v_mul_f32 v16, v119, v119\n"
"v_mfma_f32_16x16x32_bf16 v[92:95], a[144:147], a[180:183], v[12:15]\n"
"buffer_load_dwordx4 v2, s[16:19], s61 offen offset:3072 sc1 lds\n"
"s_add_u32 m0, s60, 4096\n"
"v_mfma_f32_16x16x32_bf16 v[96:99], a[148:151], a[176:179], v[8:11]\n"
"ds_read_b128 a[144:147], v2 offset:51200\n"
"v_mul_f32 v37, v120, v120\n"
"v_mfma_f32_16x16x32_bf16 v[100:103], a[148:151], a[180:183], v[12:15]\n"
"s_cmp_lt_u32 s54, 2\n"
"s_cselect_b32 s65, s65, 110816\n"
"v_mfma_f32_16x16x32_bf16 v[104:107], a[152:155], a[176:179], v[8:11]\n"
"ds_read_b128 a[148:151], v2 offset:52224\n"
"v_add_u32 v19, s65, v6\n"
"s_lshl_b32 s65, s64, 1\n"
"v_mfma_f32_16x16x32_bf16 v[108:111], a[152:155], a[180:183], v[12:15]\n"
"buffer_load_dwordx4 v3, s[16:19], s61 offen offset:0 sc1 lds\n"
"s_waitcnt lgkmcnt(4)\n"
"v_mfma_f32_16x16x32_bf16 v[40:43], a[156:159], a[184:187], v[40:43]\n"
"ds_read_b128 a[152:155], v2 offset:53248\n"
"s_add_u32 s65, s65, 8\n"
"v_mfma_f32_16x16x32_bf16 v[44:47], a[156:159], a[188:191], v[44:47]\n"
"s_lshl_b32 s69, s58, 4\n"
"s_lshl_b32 s70, s54, 2\n"
"v_mfma_f32_16x16x32_bf16 v[48:51], a[160:163], a[184:187], v[48:51]\n"
"ds_read_b128 a[156:159], v2 offset:54272\n"
"s_add_u32 s69, s69, s70\n"
"s_add_u32 s69, s69, 12288\n"
"v_mfma_f32_16x16x32_bf16 v[52:55], a[160:163], a[188:191], v[52:55]\n"
"s_waitcnt vmcnt(26)\n"
"buffer_load_dwordx4 v3, s[16:19], s61 offen offset:1024 sc1 lds\n"
"v_mfma_f32_16x16x32_bf16 v[56:59], a[164:167], a[184:187], v[56:59]\n"
"ds_read_b128 a[160:163], v2 offset:55296\n"
"v_subrev_u32 v35, s65, v33\n"
"v_mfma_f32_16x16x32_bf16 v[60:63], a[164:167], a[188:191], v[60:63]\n"
"v_cmp_gt_u32 s[82:83], 2, v35\n"
"s_waitcnt lgkmcnt(4)\n"
"v_mfma_f32_16x16x32_bf16 v[64:67], a[168:171], a[184:187], v[64:67]\n"
"ds_read_b128 a[164:167], v2 offset:56320\n"
"v_cmp_eq_u32 s[84:85], 12, v33\n"
"v_add_u32 v35, 16, v35\n"
"v_mfma_f32_16x16x32_bf16 v[68:71], a[168:171], a[188:191], v[68:71]\n"
"s_mov_b64 exec, 1\n"
"buffer_store_dword v17, off, s[16:19], s69 sc1\n"
"s_mov_b64 exec, -1\n"
"v_mfma_f32_16x16x32_bf16 v[72:75], a[172:175], a[184:187], v[72:75]\n"
"ds_read_b128 a[168:171], v2 offset:57344\n"
"s_lshl_b32 s69, s54, 10\n"
"v_mfma_f32_16x16x32_bf16 v[76:79], a[172:175], a[188:191], v[76:79]\n"
"buffer_load_dwordx4 v3, s[16:19], s61 offen offset:2048 sc1 lds\n"
"v_add_u32 v224, s69, v2\n"
"v_mfma_f32_16x16x32_bf16 v[80:83], a[144:147], a[184:187], v[80:83]\n"
"ds_read_b128 a[172:175], v2 offset:58368\n"
"v_add_u32 v225, 147456, v224\n"
"v_lshrrev_b32 v226, 9, v224\n"
"v_mfma_f32_16x16x32_bf16 v[84:87], a[144:147], a[188:191], v[84:87]\n"
"v_mul_u32_u24 v226, 43, v226\n"
"s_waitcnt lgkmcnt(4)\n"
"v_mfma_f32_16x16x32_bf16 v[88:91], a[148:151], a[184:187], v[88:91]\n"
"ds_read_b128 a[144:147], v2 offset:59392\n"
"v_lshrrev_b32 v226, 7, v226\n"
"v_mfma_f32_16x16x32_bf16 v[92:95], a[148:151], a[188:191], v[92:95]\n"
"v_lshl_add_u32 v226, v226, 28, v224\n"
"s_lshl_b32 s69, s53, 4\n"
"v_mfma_f32_16x16x32_bf16 v[96:99], a[152:155], a[184:187], v[96:99]\n"
"ds_read_b128 a[148:151], v2 offset:60416\n"
"buffer_load_dwordx4 v3, s[16:19], s61 offen offset:3072 sc1 lds\n"
"s_add_u32 s69, s69, s52\n"
"v_mfma_f32_16x16x32_bf16 v[100:103], a[152:155], a[188:191], v[100:103]\n"
"s_add_u32 m0, s60, 8192\n"
"s_mul_i32 s69, s69, 5632\n"
"v_mfma_f32_16x16x32_bf16 v[104:107], a[156:159], a[184:187], v[104:107]\n"
"ds_read_b128 a[152:155], v2 offset:61440\n"
"s_add_u32 s69, s69, 1048576\n"
"v_mfma_f32_16x16x32_bf16 v[108:111], a[156:159], a[188:191], v[108:111]\n"
"s_add_u32 s70, s69, 4096\n"
"s_waitcnt lgkmcnt(4)\n"
"v_mfma_f32_16x16x32_bf16 v[40:43], a[160:163], a[192:195], v[40:43]\n"
"ds_read_b128 a[156:159], v2 offset:62464\n"
"buffer_load_dwordx4 v4, s[16:19], s61 offen offset:0 sc1 lds\n"
"v_mov_b32 v36, 0x100000\n"
"v_mfma_f32_16x16x32_bf16 v[44:47], a[160:163], a[196:199], v[44:47]\n"
"v_cndmask_b32 v35, v36, v35, s[82:83]\n"
"v_cndmask_b32 v35, v35, 18, s[84:85]\n"
"v_mfma_f32_16x16x32_bf16 v[48:51], a[164:167], a[192:195], v[48:51]\n"
"ds_read_b128 a[160:163], v2 offset:63488\n"
"v_fma_f32 v7, v7, s80, v38\n"
"v_mfma_f32_16x16x32_bf16 v[52:55], a[164:167], a[196:199], v[52:55]\n"
"s_cmp_lt_u32 s54, 2\n"
"s_cselect_b64 s[82:83], -1, 0\n"
"v_mfma_f32_16x16x32_bf16 v[56:59], a[168:171], a[192:195], v[56:59]\n"
"ds_read_b128 a[164:167], v2 offset:64512\n"
"buffer_load_dwordx4 a[64:67], v2, s[16:19], s62 offen offset:3072 sc1\n"
"v_cndmask_b32 v35, v35, v33, s[82:83]\n"
"v_mfma_f32_16x16x32_bf16 v[60:63], a[168:171], a[196:199], v[60:63]\n"
"s_mul_i32 s64, s64, 2736\n"
"s_waitcnt lgkmcnt(4)\n"
"v_mfma_f32_16x16x32_bf16 v[64:67], a[172:175], a[192:195], v[64:67]\n"
"ds_read_b128 a[168:171], v5 offset:0\n"
"s_add_u32 s64, s64, 147456\n"
"v_mfma_f32_16x16x32_bf16 v[68:71], a[172:175], a[196:199], v[68:71]\n"
"v_mul_u32_u24 v35, 144, v35\n"
"v_add_u32 v35, s64, v35\n"
"v_mfma_f32_16x16x32_bf16 v[72:75], a[144:147], a[192:195], v[72:75]\n"
"ds_read_b128 a[172:175], v5 offset:1024\n"
"buffer_load_dwordx4 a[68:71], v3, s[16:19], s62 offen offset:0 sc1\n"
"v_lshl_add_u32 v20, v34, 4, v35\n"
"v_mfma_f32_16x16x32_bf16 v[76:79], a[144:147], a[196:199], v[76:79]\n"
"v_lshl_add_u32 v21, v34, 16, v35\n"
"v_min_u32 v20, 153088, v20\n"
"v_mfma_f32_16x16x32_bf16 v[80:83], a[148:151], a[192:195], v[80:83]\n"
"ds_read_b128 a[144:147], v5 offset:2048\n"
"v_min_u32 v21, 153088, v21\n"
"v_mfma_f32_16x16x32_bf16 v[84:87], a[148:151], a[196:199], v[84:87]\n"
"ds_read_b64_tr_b16 v[204:205], v19 offset:0\n"
"s_waitcnt lgkmcnt(5)\n"
"v_mfma_f32_16x16x32_bf16 v[88:91], a[152:155], a[192:195], v[88:91]\n"
"ds_read_b128 a[148:151], v5 offset:3072\n"
"buffer_load_dwordx4 a[72:75], v3, s[16:19], s62 offen offset:1024 sc1\n"
"ds_read_b64_tr_b16 v[206:207], v19 offset:528\n"
"v_mfma_f32_16x16x32_bf16 v[92:95], a[152:155], a[196:199], v[92:95]\n"
"ds_read_b64_tr_b16 v[184:185], v18 offset:0\n"
"ds_read_b64_tr_b16 v[186:187], v18 offset:528\n"
"v_mfma_f32_16x16x32_bf16 v[96:99], a[156:159], a[192:195], v[96:99]\n"
"ds_read_b128 a[152:155], v5 offset:4096\n"
"ds_read_b64_tr_b16 v[188:189], v18 offset:32\n"
"v_mfma_f32_16x16x32_bf16 v[100:103], a[156:159], a[196:199], v[100:103]\n"
"ds_read_b64_tr_b16 v[190:191], v18 offset:560\n"
"ds_read_b64_tr_b16 v[200:201], v18 offset:64\n"
"v_mfma_f32_16x16x32_bf16 v[104:107], a[160:163], a[192:195], v[104:107]\n"
"ds_read_b128 a[156:159], v5 offset:5120\n"
"ds_read_b64_tr_b16 v[202:203], v18 offset:592\n"
"v_fma_f32 v16, v16, s80, v38\n"
"v_mfma_f32_16x16x32_bf16 v[108:111], a[160:163], a[196:199], v[108:111]\n"
"buffer_load_dwordx4 a[76:79], v3, s[16:19], s62 offen offset:2048 sc1\n"
"s_waitcnt lgkmcnt(12)\n"
"v_mfma_f32_16x16x32_bf16 v[40:43], a[164:167], a[200:203], v[40:43]\n"
"ds_read_b128 a[160:163], v5 offset:6144\n"
"v_fma_f32 v37, v37, s80, v38\n"
"v_mfma_f32_16x16x32_bf16 v[44:47], a[164:167], a[204:207], v[44:47]\n"
"v_mul_f32 v7, v7, v118\n"
"v_mul_f32 v16, v16, v119\n"
"v_mfma_f32_16x16x32_bf16 v[48:51], a[168:171], a[200:203], v[48:51]\n"
"ds_read_b128 a[164:167], v5 offset:7168\n"
"v_mul_f32 v37, v37, v120\n"
"s_waitcnt lgkmcnt(8)\n"
"v_mfma_f32_16x16x32_bf16 v[192:195], v[184:187], v[204:207], 0\n"
"buffer_load_dwordx4 a[80:83], v3, s[16:19], s62 offen offset:3072 sc1\n"
"s_add_u32 s62, s62, 8192\n"
"s_waitcnt lgkmcnt(5)\n"
"v_mfma_f32_16x16x32_bf16 v[196:199], v[188:191], v[204:207], 0\n"
"v_exp_f32 v7, v7\n"
"s_waitcnt lgkmcnt(2)\n"
"v_mfma_f32_16x16x32_bf16 v[212:215], v[200:203], v[204:207], 0\n"
"ds_read_b64_tr_b16 v[236:237], v19 offset:8448\n"
"ds_read_b64_tr_b16 v[238:239], v19 offset:8976\n"
"v_mfma_f32_16x16x32_bf16 v[52:55], a[168:171], a[204:207], v[52:55]\n"
"ds_read_b64_tr_b16 v[216:217], v18 offset:8448\n"
"ds_read_b64_tr_b16 v[218:219], v18 offset:8976\n"
"ds_read_b64_tr_b16 v[220:221], v18 offset:8480\n"
"v_mfma_f32_16x16x32_bf16 v[56:59], a[172:175], a[200:203], v[56:59]\n"
"buffer_load_dwordx4 a[84:87], v2, s[16:19], s62 offen offset:0 sc1\n"
"ds_read_b64_tr_b16 v[222:223], v18 offset:9008\n"
"v_mfma_f32_16x16x32_bf16 v[60:63], a[172:175], a[204:207], v[60:63]\n"
"ds_read_b64_tr_b16 v[232:233], v18 offset:8512\n"
"v_mfma_f32_16x16x32_bf16 v[64:67], a[144:147], a[200:203], v[64:67]\n"
"ds_read_b64_tr_b16 v[234:235], v18 offset:9040\n"
"v_mfma_f32_16x16x32_bf16 v[68:71], a[144:147], a[204:207], v[68:71]\n"
"s_waitcnt lgkmcnt(8)\n"
"s_barrier\n"
"v_mfma_f32_16x16x32_bf16 v[72:75], a[148:151], a[200:203], v[72:75]\n"
"ds_read_b128 a[168:171], v5 offset:8192\n"
"s_add_u32 s61, s61, 36864\n"
"v_mfma_f32_16x16x32_bf16 v[76:79], a[148:151], a[204:207], v[76:79]\n"
"ds_read_b128 a[172:175], v5 offset:9216\n"
"s_add_u32 m0, s60, 36864\n"
"v_mfma_f32_16x16x32_bf16 v[80:83], a[152:155], a[200:203], v[80:83]\n"
"ds_read_b128 a[144:147], v5 offset:10240\n"
"buffer_load_dwordx4 v2, s[16:19], s61 offen offset:0 sc1 lds\n"
"s_waitcnt lgkmcnt(7)\n"
"v_mfma_f32_16x16x32_bf16 v[192:195], v[216:219], v[236:239], v[192:195]\n"
"v_exp_f32 v16, v16\n"
"s_waitcnt lgkmcnt(5)\n"
"v_mfma_f32_16x16x32_bf16 v[196:199], v[220:223], v[236:239], v[196:199]\n"
"v_exp_f32 v37, v37\n"
"s_waitcnt lgkmcnt(3)\n"
"v_mfma_f32_16x16x32_bf16 v[212:215], v[232:235], v[236:239], v[212:215]\n"
"buffer_load_dwordx4 v2, s[16:19], s61 offen offset:1024 sc1 lds\n"
"ds_read_b64_tr_b16 v[204:205], v19 offset:16896\n"
"v_mfma_f32_16x16x32_bf16 v[84:87], a[152:155], a[204:207], v[84:87]\n"
"ds_read_b128 a[148:151], v5 offset:11264\n"
"v_mfma_f32_16x16x32_bf16 v[88:91], a[156:159], a[200:203], v[88:91]\n"
"ds_read_b128 a[152:155], v5 offset:12288\n"
"ds_read_b64_tr_b16 v[206:207], v19 offset:17424\n"
"ds_read_b64_tr_b16 v[184:185], v18 offset:16896\n"
"v_mfma_f32_16x16x32_bf16 v[92:95], a[156:159], a[204:207], v[92:95]\n"
"ds_read_b64_tr_b16 v[186:187], v18 offset:17424\n"
"ds_read_b64_tr_b16 v[188:189], v18 offset:16928\n"
"v_mfma_f32_16x16x32_bf16 v[96:99], a[160:163], a[200:203], v[96:99]\n"
"ds_read_b128 a[156:159], v5 offset:13312\n"
"buffer_load_dwordx4 v2, s[16:19], s61 offen offset:2048 sc1 lds\n"
"v_mfma_f32_16x16x32_bf16 v[100:103], a[160:163], a[204:207], v[100:103]\n"
"ds_read_b64_tr_b16 v[190:191], v18 offset:17456\n"
"ds_read_b64_tr_b16 v[200:201], v18 offset:16960\n"
"v_mfma_f32_16x16x32_bf16 v[104:107], a[164:167], a[200:203], v[104:107]\n"
"ds_read_b128 a[160:163], v5 offset:14336\n"
"ds_read_b64_tr_b16 v[202:203], v18 offset:17488\n"
"v_add_f32 v7, 1.0, v7\n"
"v_mfma_f32_16x16x32_bf16 v[108:111], a[164:167], a[204:207], v[108:111]\n"
"v_add_f32 v16, 1.0, v16\n"
"s_waitcnt lgkmcnt(12)\n"
"v_mfma_f32_16x16x32_bf16 v[40:43], a[168:171], a[208:211], v[40:43]\n"
"ds_read_b128 a[164:167], v5 offset:15360\n"
"buffer_load_dwordx4 v2, s[16:19], s61 offen offset:3072 sc1 lds\n"
"v_mfma_f32_16x16x32_bf16 v[44:47], a[168:171], a[212:215], v[44:47]\n"
"s_add_u32 m0, s60, 40960\n"
"v_add_f32 v37, 1.0, v37\n"
"v_mfma_f32_16x16x32_bf16 v[48:51], a[172:175], a[208:211], v[48:51]\n"
"ds_read_b128 a[168:171], v5 offset:16384\n"
"v_rcp_f32 v7, v7\n"
"s_waitcnt lgkmcnt(8)\n"
"v_mfma_f32_16x16x32_bf16 v[192:195], v[184:187], v[204:207], v[192:195]\n"
"v_rcp_f32 v16, v16\n"
"s_waitcnt lgkmcnt(5)\n"
"v_mfma_f32_16x16x32_bf16 v[196:199], v[188:191], v[204:207], v[196:199]\n"
"v_rcp_f32 v37, v37\n"
"s_waitcnt lgkmcnt(2)\n"
"v_mfma_f32_16x16x32_bf16 v[212:215], v[200:203], v[204:207], v[212:215]\n"
"buffer_load_dwordx4 v3, s[16:19], s61 offen offset:0 sc1 lds\n"
"ds_read_b64_tr_b16 v[236:237], v19 offset:25344\n"
"v_mfma_f32_16x16x32_bf16 v[52:55], a[172:175], a[212:215], v[52:55]\n"
"ds_read_b64_tr_b16 v[238:239], v19 offset:25872\n"
"ds_read_b64_tr_b16 v[216:217], v18 offset:25344\n"
"ds_read_b64_tr_b16 v[218:219], v18 offset:25872\n"
"v_mfma_f32_16x16x32_bf16 v[56:59], a[144:147], a[208:211], v[56:59]\n"
"ds_read_b128 a[172:175], v5 offset:17408\n"
"ds_read_b64_tr_b16 v[220:221], v18 offset:25376\n"
"v_mfma_f32_16x16x32_bf16 v[60:63], a[144:147], a[212:215], v[60:63]\n"
"ds_read_b64_tr_b16 v[222:223], v18 offset:25904\n"
"v_mfma_f32_16x16x32_bf16 v[64:67], a[148:151], a[208:211], v[64:67]\n"
"ds_read_b128 a[144:147], v5 offset:18432\n"
"buffer_load_dwordx4 v3, s[16:19], s61 offen offset:1024 sc1 lds\n"
"v_mfma_f32_16x16x32_bf16 v[68:71], a[148:151], a[212:215], v[68:71]\n"
"ds_read_b64_tr_b16 v[232:233], v18 offset:25408\n"
"ds_read_b64_tr_b16 v[234:235], v18 offset:25936\n"
"v_mul_f32 v118, v118, v7\n"
"v_mfma_f32_16x16x32_bf16 v[72:75], a[152:155], a[208:211], v[72:75]\n"
"ds_read_b128 a[148:151], v5 offset:19456\n"
"v_mul_f32 v119, v119, v16\n"
"v_mfma_f32_16x16x32_bf16 v[76:79], a[152:155], a[212:215], v[76:79]\n"
"v_mul_f32 v120, v120, v37\n"
"v_mul_f32 v7, v121, v121\n"
"v_mfma_f32_16x16x32_bf16 v[80:83], a[156:159], a[208:211], v[80:83]\n"
"ds_read_b128 a[152:155], v5 offset:20480\n"
"buffer_load_dwordx4 v3, s[16:19], s61 offen offset:2048 sc1 lds\n"
"v_mfma_f32_16x16x32_bf16 v[84:87], a[156:159], a[212:215], v[84:87]\n"
"v_mul_f32 v16, v122, v122\n"
"v_mul_f32 v37, v123, v123\n"
"s_waitcnt lgkmcnt(12)\n"
"s_waitcnt lgkmcnt(8)\n"
"v_mfma_f32_16x16x32_bf16 v[192:195], v[216:219], v[236:239], v[192:195]\n"
"v_fma_f32 v7, v7, s80, v38\n"
"v_fma_f32 v16, v16, s80, v38\n"
"s_waitcnt lgkmcnt(5)\n"
"v_mfma_f32_16x16x32_bf16 v[196:199], v[220:223], v[236:239], v[196:199]\n"
"v_fma_f32 v37, v37, s80, v38\n"
"v_mul_f32 v7, v7, v121\n"
"s_waitcnt lgkmcnt(2)\n"
"v_mfma_f32_16x16x32_bf16 v[212:215], v[232:235], v[236:239], v[212:215]\n"
"v_mul_f32 v16, v16, v122\n"
"v_mul_f32 v37, v37, v123\n"
"v_mfma_f32_16x16x32_bf16 v[88:91], a[160:163], a[208:211], v[88:91]\n"
"ds_read_b128 a[156:159], v5 offset:21504\n"
"buffer_load_dwordx4 v3, s[16:19], s61 offen offset:3072 sc1 lds\n"
"v_mfma_f32_16x16x32_bf16 v[92:95], a[160:163], a[212:215], v[92:95]\n"
"s_add_u32 m0, s60, 45056\n"
"v_mfma_f32_16x16x32_bf16 v[96:99], a[164:167], a[208:211], v[96:99]\n"
"ds_read_b128 a[160:163], v5 offset:22528\n"
"v_mfma_f32_16x16x32_bf16 v[100:103], a[164:167], a[212:215], v[100:103]\n"
"ds_write_b64 v20, v[192:193] offset:0\n"
"v_mfma_f32_16x16x32_bf16 v[104:107], a[168:171], a[208:211], v[104:107]\n"
"ds_read_b128 a[164:167], v5 offset:23552\n"
"buffer_load_dwordx4 v4, s[16:19], s61 offen offset:0 sc1 lds\n"
"ds_write_b64 v20, v[194:195] offset:8\n"
"v_mfma_f32_16x16x32_bf16 v[108:111], a[168:171], a[212:215], v[108:111]\n"
"ds_write_b64 v20, v[196:197] offset:64\n"
"s_waitcnt lgkmcnt(7)\n"
"v_mfma_f32_16x16x32_bf16 v[40:43], a[172:175], a[216:219], v[40:43]\n"
"ds_read_b128 a[168:171], v5 offset:24576\n"
"ds_write_b64 v20, v[198:199] offset:72\n"
"v_mfma_f32_16x16x32_bf16 v[44:47], a[172:175], a[220:223], v[44:47]\n"
"ds_write_b64 v21, v[212:213] offset:128\n"
"v_mfma_f32_16x16x32_bf16 v[48:51], a[144:147], a[216:219], v[48:51]\n"
"ds_read_b128 a[172:175], v5 offset:25600\n"
"buffer_load_dwordx4 a[88:91], v2, s[16:19], s62 offen offset:1024 sc1\n"
"ds_write_b64 v21, v[214:215] offset:136\n"
"v_mfma_f32_16x16x32_bf16 v[52:55], a[144:147], a[220:223], v[52:55]\n"
"v_max3_f32 v220, v208, v209, v210\n"
"v_min3_f32 v221, v208, v209, v210\n"
"v_mfma_f32_16x16x32_bf16 v[56:59], a[148:151], a[216:219], v[56:59]\n"
"ds_read_b128 a[144:147], v5 offset:26624\n"
"v_max3_f32 v220, v220, v211, v240\n"
"v_mfma_f32_16x16x32_bf16 v[60:63], a[148:151], a[220:223], v[60:63]\n"
"v_min3_f32 v221, v221, v211, v240\n"
"s_waitcnt lgkmcnt(10)\n"
"v_mfma_f32_16x16x32_bf16 v[64:67], a[152:155], a[216:219], v[64:67]\n"
"ds_read_b128 a[148:151], v5 offset:27648\n"
"buffer_load_dwordx4 a[92:95], v2, s[16:19], s62 offen offset:2048 sc1\n"
"v_max3_f32 v220, v220, v241, v242\n"
"v_mfma_f32_16x16x32_bf16 v[68:71], a[152:155], a[220:223], v[68:71]\n"
"v_min3_f32 v221, v221, v241, v242\n"
"v_max3_f32 v220, v220, v243, v244\n"
"v_mfma_f32_16x16x32_bf16 v[72:75], a[156:159], a[216:219], v[72:75]\n"
"ds_read_b128 a[152:155], v5 offset:28672\n"
"v_min3_f32 v221, v221, v243, v244\n"
"v_mfma_f32_16x16x32_bf16 v[76:79], a[156:159], a[220:223], v[76:79]\n"
"v_max3_f32 v220, v220, v245, v246\n"
"v_min3_f32 v221, v221, v245, v246\n"
"v_mfma_f32_16x16x32_bf16 v[80:83], a[160:163], a[216:219], v[80:83]\n"
"ds_read_b128 a[156:159], v5 offset:29696\n"
"v_max3_f32 v220, v220, v247, v248\n"
"v_min3_f32 v221, v221, v247, v248\n"
"v_mfma_f32_16x16x32_bf16 v[84:87], a[160:163], a[220:223], v[84:87]\n"
"buffer_load_dwordx4 a[96:99], v2, s[16:19], s62 offen offset:3072 sc1\n"
"s_waitcnt lgkmcnt(5)\n"
"v_mfma_f32_16x16x32_bf16 v[88:91], a[164:167], a[216:219], v[88:91]\n"
"ds_read_b128 a[160:163], v5 offset:30720\n"
"v_max3_f32 v220, v220, v249, v250\n"
"v_mfma_f32_16x16x32_bf16 v[92:95], a[164:167], a[220:223], v[92:95]\n"
"v_min3_f32 v221, v221, v249, v250\n"
"v_max3_f32 v220, v220, v251, v252\n"
"v_mfma_f32_16x16x32_bf16 v[96:99], a[168:171], a[216:219], v[96:99]\n"
"ds_read_b128 a[164:167], v5 offset:31744\n"
"v_min3_f32 v221, v221, v251, v252\n"
"v_max_f32 v220, v220, v253\n"
"v_mfma_f32_16x16x32_bf16 v[100:103], a[168:171], a[220:223], v[100:103]\n"
"buffer_load_dwordx4 a[100:103], v3, s[16:19], s62 offen offset:0 sc1\n"
"v_min_f32 v221, v221, v253\n"
"v_mfma_f32_16x16x32_bf16 v[104:107], a[172:175], a[216:219], v[104:107]\n"
"ds_read_b128 a[168:171], v5 offset:32768\n"
"v_mul_f32 v220, 0xbfb8aa3b, v220\n"
"v_mfma_f32_16x16x32_bf16 v[108:111], a[172:175], a[220:223], v[108:111]\n"
"v_mul_f32 v221, 0x3fb8aa3b, v221\n"
"s_waitcnt lgkmcnt(4)\n"
"v_mfma_f32_16x16x32_bf16 v[40:43], a[144:147], a[224:227], v[40:43]\n"
"ds_read_b128 a[172:175], v5 offset:33792\n"
"v_fmamk_f32 v184, v208, 0x3fb8aa3b, v220\n"
"v_fmamk_f32 v208, v208, 0xbfb8aa3b, v221\n"
"v_mfma_f32_16x16x32_bf16 v[44:47], a[144:147], a[228:231], v[44:47]\n"
"buffer_load_dwordx4 a[104:107], v3, s[16:19], s62 offen offset:1024 sc1\n"
"v_mfma_f32_16x16x32_bf16 v[48:51], a[148:151], a[224:227], v[48:51]\n"
"ds_read_b128 a[144:147], v5 offset:34816\n"
"v_mfma_f32_16x16x32_bf16 v[52:55], a[148:151], a[228:231], v[52:55]\n"
"v_exp_f32 v184, v184\n"
"v_mfma_f32_16x16x32_bf16 v[56:59], a[152:155], a[224:227], v[56:59]\n"
"ds_read_b128 a[148:151], v5 offset:35840\n"
"v_exp_f32 v208, v208\n"
"v_mfma_f32_16x16x32_bf16 v[60:63], a[152:155], a[228:231], v[60:63]\n"
"buffer_load_dwordx4 a[108:111], v3, s[16:19], s62 offen offset:2048 sc1\n"
"s_waitcnt lgkmcnt(4)\n"
"v_mfma_f32_16x16x32_bf16 v[64:67], a[156:159], a[224:227], v[64:67]\n"
"ds_read_b128 a[152:155], v5 offset:36864\n"
"v_fmamk_f32 v185, v209, 0x3fb8aa3b, v220\n"
"v_mfma_f32_16x16x32_bf16 v[68:71], a[156:159], a[228:231], v[68:71]\n"
"v_fmamk_f32 v209, v209, 0xbfb8aa3b, v221\n"
"v_mfma_f32_16x16x32_bf16 v[72:75], a[160:163], a[224:227], v[72:75]\n"
"ds_read_b128 a[156:159], v5 offset:37888\n"
"v_exp_f32 v185, v185\n"
"v_mfma_f32_16x16x32_bf16 v[76:79], a[160:163], a[228:231], v[76:79]\n"
"v_exp_f32 v209, v209\n"
"v_mfma_f32_16x16x32_bf16 v[80:83], a[164:167], a[224:227], v[80:83]\n"
"ds_read_b128 a[160:163], v5 offset:38912\n"
"buffer_load_dwordx4 a[112:115], v3, s[16:19], s62 offen offset:3072 sc1\n"
"v_mfma_f32_16x16x32_bf16 v[84:87], a[164:167], a[228:231], v[84:87]\n"
"s_add_u32 s62, s62, 8192\n"
"s_waitcnt lgkmcnt(4)\n"
"v_mfma_f32_16x16x32_bf16 v[88:91], a[168:171], a[224:227], v[88:91]\n"
"ds_read_b128 a[164:167], v5 offset:39936\n"
"v_fmamk_f32 v186, v210, 0x3fb8aa3b, v220\n"
"v_fmamk_f32 v210, v210, 0xbfb8aa3b, v221\n"
"v_mfma_f32_16x16x32_bf16 v[92:95], a[168:171], a[228:231], v[92:95]\n"
"v_exp_f32 v186, v186\n"
"v_mfma_f32_16x16x32_bf16 v[96:99], a[172:175], a[224:227], v[96:99]\n"
"ds_read_b128 a[168:171], v5 offset:40960\n"
"buffer_load_dwordx4 a[116:119], v2, s[16:19], s62 offen offset:0 sc1\n"
"v_mfma_f32_16x16x32_bf16 v[100:103], a[172:175], a[228:231], v[100:103]\n"
"v_exp_f32 v210, v210\n"
"v_mfma_f32_16x16x32_bf16 v[104:107], a[144:147], a[224:227], v[104:107]\n"
"ds_read_b128 a[172:175], v5 offset:41984\n"
"v_add_f32 v222, v184, v185\n"
"v_add_f32 v223, v208, v209\n"
"v_mfma_f32_16x16x32_bf16 v[108:111], a[144:147], a[228:231], v[108:111]\n"
"v_fmamk_f32 v187, v211, 0x3fb8aa3b, v220\n"
"s_waitcnt vmcnt(53) lgkmcnt(4)\n"
"v_mfma_f32_16x16x32_bf16 v[40:43], a[148:151], a[232:235], v[40:43]\n"
"ds_read_b128 a[144:147], v5 offset:43008\n"
"buffer_load_dwordx4 a[120:123], v2, s[16:19], s62 offen offset:1024 sc1\n"
"v_mfma_f32_16x16x32_bf16 v[44:47], a[148:151], a[236:239], v[44:47]\n"
"v_fmamk_f32 v211, v211, 0xbfb8aa3b, v221\n"
"v_mfma_f32_16x16x32_bf16 v[48:51], a[152:155], a[232:235], v[48:51]\n"
"ds_read_b128 a[148:151], v5 offset:44032\n"
"v_exp_f32 v187, v187\n"
"v_mfma_f32_16x16x32_bf16 v[52:55], a[152:155], a[236:239], v[52:55]\n"
"v_exp_f32 v211, v211\n"
"v_mfma_f32_16x16x32_bf16 v[56:59], a[156:159], a[232:235], v[56:59]\n"
"buffer_load_dwordx4 a[124:127], v2, s[16:19], s62 offen offset:2048 sc1\n"
"v_add_f32 v222, v222, v186\n"
"v_mfma_f32_16x16x32_bf16 v[60:63], a[156:159], a[236:239], v[60:63]\n"
"v_add_f32 v223, v223, v210\n"
"s_waitcnt lgkmcnt(3)\n"
"v_mfma_f32_16x16x32_bf16 v[64:67], a[160:163], a[232:235], v[64:67]\n"
"v_fmamk_f32 v188, v240, 0x3fb8aa3b, v220\n"
"v_fmamk_f32 v240, v240, 0xbfb8aa3b, v221\n"
"v_mfma_f32_16x16x32_bf16 v[68:71], a[160:163], a[236:239], v[68:71]\n"
"s_waitcnt vmcnt(25)\n"
"s_waitcnt lgkmcnt(0)\n"
"s_barrier\n"
"v_mfma_f32_16x16x32_bf16 v[72:75], a[164:167], a[232:235], v[72:75]\n"
"ds_read_b128 a[152:155], v2 offset:0\n"
"s_add_u32 s61, s61, 36864\n"
"v_mfma_f32_16x16x32_bf16 v[76:79], a[164:167], a[236:239], v[76:79]\n"
"ds_read_b128 a[156:159], v2 offset:1024\n"
"s_add_u32 m0, s60, 110592\n"
"v_mfma_f32_16x16x32_bf16 v[80:83], a[168:171], a[232:235], v[80:83]\n"
"ds_read_b128 a[160:163], v2 offset:2048\n"
"buffer_load_dwordx4 v2, s[16:19], s61 offen offset:0 sc1 lds\n"
"ds_read_b128 v[192:195], v225\n"
"v_mfma_f32_16x16x32_bf16 v[84:87], a[168:171], a[236:239], v[84:87]\n"
"ds_read_b128 a[164:167], v2 offset:3072\n"
"v_mfma_f32_16x16x32_bf16 v[88:91], a[172:175], a[232:235], v[88:91]\n"
"ds_read_b128 a[168:171], v2 offset:4096\n"
"ds_read_b128 v[196:199], v225 offset:4096\n"
"v_mfma_f32_16x16x32_bf16 v[92:95], a[172:175], a[236:239], v[92:95]\n"
"buffer_load_dwordx4 v2, s[16:19], s61 offen offset:1024 sc1 lds\n"
"v_mfma_f32_16x16x32_bf16 v[96:99], a[144:147], a[232:235], v[96:99]\n"
"ds_read_b128 a[172:175], v2 offset:5120\n"
"v_exp_f32 v188, v188\n"
"v_mfma_f32_16x16x32_bf16 v[100:103], a[144:147], a[236:239], v[100:103]\n"
"v_exp_f32 v240, v240\n"
"v_mfma_f32_16x16x32_bf16 v[104:107], a[148:151], a[232:235], v[104:107]\n"
"ds_read_b128 a[144:147], v2 offset:6144\n"
"v_add_f32 v222, v222, v187\n"
"v_mfma_f32_16x16x32_bf16 v[108:111], a[148:151], a[236:239], v[108:111]\n"
"buffer_load_dwordx4 v2, s[16:19], s61 offen offset:2048 sc1 lds\n"
"s_waitcnt lgkmcnt(6)\n"
"v_mfma_f32_16x16x32_bf16 v[40:43], a[152:155], a[240:243], v[40:43]\n"
"ds_read_b128 a[148:151], v2 offset:7168\n"
"s_waitcnt lgkmcnt(6)\n"
"buffer_store_dwordx4 v[192:195], v224, s[16:19], s69 offen sc1\n"
"s_waitcnt lgkmcnt(3)\n"
"buffer_store_dwordx4 v[196:199], v226, s[16:19], s70 offen sc1\n"
"v_mfma_f32_16x16x32_bf16 v[44:47], a[152:155], a[244:247], v[44:47]\n"
"s_add_u32 s68, s67, 20480\n"
"v_add_f32 v223, v223, v211\n"
"v_mfma_f32_16x16x32_bf16 v[48:51], a[156:159], a[240:243], v[48:51]\n"
"ds_read_b128 a[152:155], v2 offset:8192\n"
"v_fmamk_f32 v189, v241, 0x3fb8aa3b, v220\n"
"v_mfma_f32_16x16x32_bf16 v[52:55], a[156:159], a[244:247], v[52:55]\n"
"v_fmamk_f32 v241, v241, 0xbfb8aa3b, v221\n"
"v_mfma_f32_16x16x32_bf16 v[56:59], a[160:163], a[240:243], v[56:59]\n"
"ds_read_b128 a[156:159], v2 offset:9216\n"
"v_exp_f32 v189, v189\n"
"v_mfma_f32_16x16x32_bf16 v[60:63], a[160:163], a[244:247], v[60:63]\n"
"s_waitcnt lgkmcnt(4)\n"
"v_mfma_f32_16x16x32_bf16 v[64:67], a[164:167], a[240:243], v[64:67]\n"
"ds_read_b128 a[160:163], v2 offset:10240\n"
"v_mfma_f32_16x16x32_bf16 v[68:71], a[164:167], a[244:247], v[68:71]\n"
"v_exp_f32 v241, v241\n"
"v_mfma_f32_16x16x32_bf16 v[72:75], a[168:171], a[240:243], v[72:75]\n"
"ds_read_b128 a[164:167], v2 offset:11264\n"
"v_add_f32 v222, v222, v188\n"
"v_add_f32 v223, v223, v240\n"
"v_mfma_f32_16x16x32_bf16 v[76:79], a[168:171], a[244:247], v[76:79]\n"
"v_fmamk_f32 v190, v242, 0x3fb8aa3b, v220\n"
"v_fmamk_f32 v242, v242, 0xbfb8aa3b, v221\n"
"v_mfma_f32_16x16x32_bf16 v[80:83], a[172:175], a[240:243], v[80:83]\n"
"ds_read_b128 a[168:171], v2 offset:12288\n"
"v_mfma_f32_16x16x32_bf16 v[84:87], a[172:175], a[244:247], v[84:87]\n"
"s_waitcnt lgkmcnt(4)\n"
"v_mfma_f32_16x16x32_bf16 v[88:91], a[144:147], a[240:243], v[88:91]\n"
"ds_read_b128 a[172:175], v2 offset:13312\n"
"buffer_load_dwordx4 v2, s[16:19], s61 offen offset:3072 sc1 lds\n"
"v_mfma_f32_16x16x32_bf16 v[92:95], a[144:147], a[244:247], v[92:95]\n"
"s_add_u32 m0, s60, 114688\n"
"v_mfma_f32_16x16x32_bf16 v[96:99], a[148:151], a[240:243], v[96:99]\n"
"ds_read_b128 a[144:147], v2 offset:14336\n"
"v_mfma_f32_16x16x32_bf16 v[100:103], a[148:151], a[244:247], v[100:103]\n"
"v_exp_f32 v190, v190\n"
"v_mfma_f32_16x16x32_bf16 v[104:107], a[152:155], a[240:243], v[104:107]\n"
"ds_read_b128 a[148:151], v2 offset:15360\n"
"buffer_load_dwordx4 v3, s[16:19], s61 offen offset:0 sc1 lds\n"
"v_mfma_f32_16x16x32_bf16 v[108:111], a[152:155], a[244:247], v[108:111]\n"
"s_waitcnt lgkmcnt(4)\n"
"v_mfma_f32_16x16x32_bf16 v[40:43], a[156:159], a[248:251], v[40:43]\n"
"ds_read_b128 a[152:155], v2 offset:16384\n"
"v_mfma_f32_16x16x32_bf16 v[44:47], a[156:159], a[0:3], v[44:47]\n"
"v_exp_f32 v242, v242\n"
"v_mfma_f32_16x16x32_bf16 v[48:51], a[160:163], a[248:251], v[48:51]\n"
"ds_read_b128 a[156:159], v2 offset:17408\n"
"v_add_f32 v222, v222, v189\n"
"v_add_f32 v223, v223, v241\n"
"v_mfma_f32_16x16x32_bf16 v[52:55], a[160:163], a[0:3], v[52:55]\n"
"buffer_load_dwordx4 v3, s[16:19], s61 offen offset:1024 sc1 lds\n"
"v_fmamk_f32 v191, v243, 0x3fb8aa3b, v220\n"
"v_mfma_f32_16x16x32_bf16 v[56:59], a[164:167], a[248:251], v[56:59]\n"
"ds_read_b128 a[160:163], v2 offset:18432\n"
"v_fmamk_f32 v243, v243, 0xbfb8aa3b, v221\n"
"v_mfma_f32_16x16x32_bf16 v[60:63], a[164:167], a[0:3], v[60:63]\n"
"s_waitcnt lgkmcnt(4)\n"
"v_mfma_f32_16x16x32_bf16 v[64:67], a[168:171], a[248:251], v[64:67]\n"
"ds_read_b128 a[164:167], v2 offset:19456\n"
"v_exp_f32 v191, v191\n"
"v_mfma_f32_16x16x32_bf16 v[68:71], a[168:171], a[0:3], v[68:71]\n"
"buffer_load_dwordx4 v3, s[16:19], s61 offen offset:2048 sc1 lds\n"
"v_mfma_f32_16x16x32_bf16 v[72:75], a[172:175], a[248:251], v[72:75]\n"
"ds_read_b128 a[168:171], v2 offset:20480\n"
"v_mfma_f32_16x16x32_bf16 v[76:79], a[172:175], a[0:3], v[76:79]\n"
"v_exp_f32 v243, v243\n"
"v_mfma_f32_16x16x32_bf16 v[80:83], a[144:147], a[248:251], v[80:83]\n"
"ds_read_b128 a[172:175], v2 offset:21504\n"
"v_add_f32 v222, v222, v190\n"
"v_add_f32 v223, v223, v242\n"
"v_mfma_f32_16x16x32_bf16 v[84:87], a[144:147], a[0:3], v[84:87]\n"
"buffer_load_dwordx4 v3, s[16:19], s61 offen offset:3072 sc1 lds\n"
"s_waitcnt lgkmcnt(4)\n"
"v_mfma_f32_16x16x32_bf16 v[88:91], a[148:151], a[248:251], v[88:91]\n"
"ds_read_b128 a[144:147], v2 offset:22528\n"
"s_add_u32 m0, s60, 118784\n"
"v_mfma_f32_16x16x32_bf16 v[92:95], a[148:151], a[0:3], v[92:95]\n"
"v_fmamk_f32 v200, v244, 0x3fb8aa3b, v220\n"
"v_fmamk_f32 v244, v244, 0xbfb8aa3b, v221\n"
"v_mfma_f32_16x16x32_bf16 v[96:99], a[152:155], a[248:251], v[96:99]\n"
"ds_read_b128 a[148:151], v2 offset:23552\n"
"v_exp_f32 v200, v200\n"
"v_mfma_f32_16x16x32_bf16 v[100:103], a[152:155], a[0:3], v[100:103]\n"
"buffer_load_dwordx4 v4, s[16:19], s61 offen offset:0 sc1 lds\n"
"s_add_u32 s61, s61, 36864\n"
"v_mfma_f32_16x16x32_bf16 v[104:107], a[156:159], a[248:251], v[104:107]\n"
"ds_read_b128 a[152:155], v2 offset:24576\n"
"s_add_u32 m0, s60, 73728\n"
"v_mfma_f32_16x16x32_bf16 v[108:111], a[156:159], a[0:3], v[108:111]\n"
"s_waitcnt lgkmcnt(4)\n"
"v_mfma_f32_16x16x32_bf16 v[40:43], a[160:163], a[4:7], v[40:43]\n"
"ds_read_b128 a[156:159], v2 offset:25600\n"
"v_exp_f32 v244, v244\n"
"v_mfma_f32_16x16x32_bf16 v[44:47], a[160:163], a[8:11], v[44:47]\n"
"v_add_f32 v222, v222, v191\n"
"v_add_f32 v223, v223, v243\n"
"v_mfma_f32_16x16x32_bf16 v[48:51], a[164:167], a[4:7], v[48:51]\n"
"ds_read_b128 a[160:163], v2 offset:26624\n"
"buffer_load_dwordx4 v2, s[16:19], s61 offen offset:0 sc1 lds\n"
"v_mfma_f32_16x16x32_bf16 v[52:55], a[164:167], a[8:11], v[52:55]\n"
"v_fmamk_f32 v201, v245, 0x3fb8aa3b, v220\n"
"v_fmamk_f32 v245, v245, 0xbfb8aa3b, v221\n"
"v_mfma_f32_16x16x32_bf16 v[56:59], a[168:171], a[4:7], v[56:59]\n"
"ds_read_b128 a[164:167], v2 offset:27648\n"
"v_exp_f32 v201, v201\n"
"v_mfma_f32_16x16x32_bf16 v[60:63], a[168:171], a[8:11], v[60:63]\n"
"s_waitcnt lgkmcnt(4)\n"
"v_mfma_f32_16x16x32_bf16 v[64:67], a[172:175], a[4:7], v[64:67]\n"
"ds_read_b128 a[168:171], v2 offset:28672\n"
"buffer_load_dwordx4 v2, s[16:19], s61 offen offset:1024 sc1 lds\n"
"v_mfma_f32_16x16x32_bf16 v[68:71], a[172:175], a[8:11], v[68:71]\n"
"v_exp_f32 v245, v245\n"
"v_mfma_f32_16x16x32_bf16 v[72:75], a[144:147], a[4:7], v[72:75]\n"
"ds_read_b128 a[172:175], v2 offset:29696\n"
"v_add_f32 v222, v222, v200\n"
"v_add_f32 v223, v223, v244\n"
"v_mfma_f32_16x16x32_bf16 v[76:79], a[144:147], a[8:11], v[76:79]\n"
"v_fmamk_f32 v202, v246, 0x3fb8aa3b, v220\n"
"v_fmamk_f32 v246, v246, 0xbfb8aa3b, v221\n"
"v_mfma_f32_16x16x32_bf16 v[80:83], a[148:151], a[4:7], v[80:83]\n"
"ds_read_b128 a[144:147], v2 offset:30720\n"
"buffer_load_dwordx4 v2, s[16:19], s61 offen offset:2048 sc1 lds\n"
"v_mfma_f32_16x16x32_bf16 v[84:87], a[148:151], a[8:11], v[84:87]\n"
"s_waitcnt lgkmcnt(4)\n"
"v_mfma_f32_16x16x32_bf16 v[88:91], a[152:155], a[4:7], v[88:91]\n"
"ds_read_b128 a[148:151], v2 offset:31744\n"
"v_exp_f32 v202, v202\n"
"v_mfma_f32_16x16x32_bf16 v[92:95], a[152:155], a[8:11], v[92:95]\n"
"v_exp_f32 v246, v246\n"
"v_mfma_f32_16x16x32_bf16 v[96:99], a[156:159], a[4:7], v[96:99]\n"
"ds_read_b128 a[152:155], v2 offset:32768\n"
"buffer_load_dwordx4 v2, s[16:19], s61 offen offset:3072 sc1 lds\n"
"v_mfma_f32_16x16x32_bf16 v[100:103], a[156:159], a[8:11], v[100:103]\n"
"s_add_u32 m0, s60, 77824\n"
"v_add_f32 v222, v222, v201\n"
"v_mfma_f32_16x16x32_bf16 v[104:107], a[160:163], a[4:7], v[104:107]\n"
"ds_read_b128 a[156:159], v2 offset:33792\n"
"v_add_f32 v223, v223, v245\n"
"v_fmamk_f32 v203, v247, 0x3fb8aa3b, v220\n"
"v_mfma_f32_16x16x32_bf16 v[108:111], a[160:163], a[8:11], v[108:111]\n"
"v_fmamk_f32 v247, v247, 0xbfb8aa3b, v221\n"
"s_waitcnt lgkmcnt(4)\n"
"v_mfma_f32_16x16x32_bf16 v[40:43], a[164:167], a[12:15], v[40:43]\n"
"ds_read_b128 a[160:163], v2 offset:34816\n"
"buffer_load_dwordx4 v3, s[16:19], s61 offen offset:0 sc1 lds\n"
"v_mfma_f32_16x16x32_bf16 v[44:47], a[164:167], a[16:19], v[44:47]\n"
"v_exp_f32 v203, v203\n"
"v_mfma_f32_16x16x32_bf16 v[48:51], a[168:171], a[12:15], v[48:51]\n"
"ds_read_b128 a[164:167], v2 offset:35840\n"
"v_exp_f32 v247, v247\n"
"v_mfma_f32_16x16x32_bf16 v[52:55], a[168:171], a[16:19], v[52:55]\n"
"v_add_f32 v222, v222, v202\n"
"v_add_f32 v223, v223, v246\n"
"v_mfma_f32_16x16x32_bf16 v[56:59], a[172:175], a[12:15], v[56:59]\n"
"v_fmamk_f32 v204, v248, 0x3fb8aa3b, v220\n"
"v_fmamk_f32 v248, v248, 0xbfb8aa3b, v221\n"
"v_mfma_f32_16x16x32_bf16 v[60:63], a[172:175], a[16:19], v[60:63]\n"
"buffer_load_dwordx4 v3, s[16:19], s61 offen offset:1024 sc1 lds\n"
"s_waitcnt lgkmcnt(3)\n"
"v_mfma_f32_16x16x32_bf16 v[64:67], a[144:147], a[12:15], v[64:67]\n"
"v_exp_f32 v204, v204\n"
"v_mfma_f32_16x16x32_bf16 v[68:71], a[144:147], a[16:19], v[68:71]\n"
"s_waitcnt vmcnt(27)\n"
"s_waitcnt lgkmcnt(0)\n"
"s_barrier\n"
"v_mfma_f32_16x16x32_bf16 v[72:75], a[148:151], a[12:15], v[72:75]\n"
"ds_read_b128 a[168:171], v2 offset:36864\n"
"v_mfma_f32_16x16x32_bf16 v[76:79], a[148:151], a[16:19], v[76:79]\n"
"ds_read_b128 a[172:175], v2 offset:37888\n"
"buffer_load_dwordx4 v3, s[16:19], s61 offen offset:2048 sc1 lds\n"
"v_mfma_f32_16x16x32_bf16 v[80:83], a[152:155], a[12:15], v[80:83]\n"
"ds_read_b128 a[144:147], v2 offset:38912\n"
"v_exp_f32 v248, v248\n"
"v_mfma_f32_16x16x32_bf16 v[84:87], a[152:155], a[16:19], v[84:87]\n"
"ds_read_b128 a[148:151], v2 offset:39936\n"
"v_mfma_f32_16x16x32_bf16 v[88:91], a[156:159], a[12:15], v[88:91]\n"
"ds_read_b128 a[152:155], v2 offset:40960\n"
"v_add_f32 v222, v222, v203\n"
"v_mfma_f32_16x16x32_bf16 v[92:95], a[156:159], a[16:19], v[92:95]\n"
"buffer_load_dwordx4 v3, s[16:19], s61 offen offset:3072 sc1 lds\n"
"s_add_u32 m0, s60, 81920\n"
"v_mfma_f32_16x16x32_bf16 v[96:99], a[160:163], a[12:15], v[96:99]\n"
"ds_read_b128 a[156:159], v2 offset:41984\n"
"v_add_f32 v223, v223, v247\n"
"v_fmamk_f32 v205, v249, 0x3fb8aa3b, v220\n"
"v_mfma_f32_16x16x32_bf16 v[100:103], a[160:163], a[16:19], v[100:103]\n"
"v_fmamk_f32 v249, v249, 0xbfb8aa3b, v221\n"
"v_mfma_f32_16x16x32_bf16 v[104:107], a[164:167], a[12:15], v[104:107]\n"
"ds_read_b128 a[160:163], v2 offset:43008\n"
"v_mfma_f32_16x16x32_bf16 v[108:111], a[164:167], a[16:19], v[108:111]\n"
"s_waitcnt lgkmcnt(4)\n"
"v_mfma_f32_16x16x32_bf16 v[40:43], a[168:171], a[20:23], v[40:43]\n"
"ds_read_b128 a[164:167], v2 offset:44032\n"
"buffer_load_dwordx4 v4, s[16:19], s61 offen offset:0 sc1 lds\n"
"v_mfma_f32_16x16x32_bf16 v[44:47], a[168:171], a[24:27], v[44:47]\n"
"s_add_u32 s61, s61, 36864\n"
"s_add_u32 m0, s60, 0\n"
"v_mfma_f32_16x16x32_bf16 v[48:51], a[172:175], a[20:23], v[48:51]\n"
"ds_read_b128 a[168:171], v2 offset:45056\n"
"v_mfma_f32_16x16x32_bf16 v[52:55], a[172:175], a[24:27], v[52:55]\n"
"v_exp_f32 v205, v205\n"
"v_mfma_f32_16x16x32_bf16 v[56:59], a[144:147], a[20:23], v[56:59]\n"
"ds_read_b128 a[172:175], v2 offset:46080\n"
"buffer_load_dwordx4 v2, s[16:19], s61 offen offset:0 sc1 lds\n"
"v_mfma_f32_16x16x32_bf16 v[60:63], a[144:147], a[24:27], v[60:63]\n"
"s_waitcnt lgkmcnt(4)\n"
"v_mfma_f32_16x16x32_bf16 v[64:67], a[148:151], a[20:23], v[64:67]\n"
"ds_read_b128 a[144:147], v2 offset:47104\n"
"v_mfma_f32_16x16x32_bf16 v[68:71], a[148:151], a[24:27], v[68:71]\n"
"v_exp_f32 v249, v249\n"
"v_mfma_f32_16x16x32_bf16 v[72:75], a[152:155], a[20:23], v[72:75]\n"
"ds_read_b128 a[148:151], v2 offset:48128\n"
"buffer_load_dwordx4 v2, s[16:19], s61 offen offset:1024 sc1 lds\n"
"v_add_f32 v222, v222, v204\n"
"v_mfma_f32_16x16x32_bf16 v[76:79], a[152:155], a[24:27], v[76:79]\n"
"v_add_f32 v223, v223, v248\n"
"v_fmamk_f32 v206, v250, 0x3fb8aa3b, v220\n"
"v_mfma_f32_16x16x32_bf16 v[80:83], a[156:159], a[20:23], v[80:83]\n"
"ds_read_b128 a[152:155], v2 offset:49152\n"
"v_fmamk_f32 v250, v250, 0xbfb8aa3b, v221\n"
"v_mfma_f32_16x16x32_bf16 v[84:87], a[156:159], a[24:27], v[84:87]\n"
"s_waitcnt lgkmcnt(4)\n"
"v_mfma_f32_16x16x32_bf16 v[88:91], a[160:163], a[20:23], v[88:91]\n"
"ds_read_b128 a[156:159], v2 offset:50176\n"
"v_exp_f32 v206, v206\n"
"v_mfma_f32_16x16x32_bf16 v[92:95], a[160:163], a[24:27], v[92:95]\n"
"buffer_load_dwordx4 v2, s[16:19], s61 offen offset:2048 sc1 lds\n"
"v_mfma_f32_16x16x32_bf16 v[96:99], a[164:167], a[20:23], v[96:99]\n"
"ds_read_b128 a[160:163], v2 offset:51200\n"
"v_mfma_f32_16x16x32_bf16 v[100:103], a[164:167], a[24:27], v[100:103]\n"
"v_exp_f32 v250, v250\n"
"v_mfma_f32_16x16x32_bf16 v[104:107], a[168:171], a[20:23], v[104:107]\n"
"ds_read_b128 a[164:167], v2 offset:52224\n"
"v_add_f32 v222, v222, v205\n"
"v_add_f32 v223, v223, v249\n"
"v_mfma_f32_16x16x32_bf16 v[108:111], a[168:171], a[24:27], v[108:111]\n"
"buffer_load_dwordx4 v2, s[16:19], s61 offen offset:3072 sc1 lds\n"
"s_waitcnt lgkmcnt(4)\n"
"v_mfma_f32_16x16x32_bf16 v[40:43], a[172:175], a[28:31], v[40:43]\n"
"ds_read_b128 a[168:171], v2 offset:53248\n"
"s_add_u32 m0, s60, 4096\n"
"v_mfma_f32_16x16x32_bf16 v[44:47], a[172:175], a[32:35], v[44:47]\n"
"s_waitcnt vmcnt(19)\n"
"v_fmamk_f32 v207, v251, 0x3fb8aa3b, v220\n"
"v_mfma_f32_16x16x32_bf16 v[48:51], a[144:147], a[28:31], v[48:51]\n"
"ds_read_b128 a[172:175], v2 offset:54272\n"
"v_fmamk_f32 v251, v251, 0xbfb8aa3b, v221\n"
"v_mfma_f32_16x16x32_bf16 v[52:55], a[144:147], a[32:35], v[52:55]\n"
"buffer_load_dwordx4 v3, s[16:19], s61 offen offset:0 sc1 lds\n"
"v_mfma_f32_16x16x32_bf16 v[56:59], a[148:151], a[28:31], v[56:59]\n"
"ds_read_b128 a[144:147], v2 offset:55296\n"
"v_mfma_f32_16x16x32_bf16 v[60:63], a[148:151], a[32:35], v[60:63]\n"
"s_waitcnt lgkmcnt(4)\n"
"v_mfma_f32_16x16x32_bf16 v[64:67], a[152:155], a[28:31], v[64:67]\n"
"ds_read_b128 a[148:151], v2 offset:56320\n"
"s_mov_b64 exec, 1\n"
"buffer_store_dword v17, off, s[16:19], s68 sc1\n"
"s_mov_b64 exec, -1\n"
"v_mfma_f32_16x16x32_bf16 v[68:71], a[152:155], a[32:35], v[68:71]\n"
"v_lshrrev_b32 v192, 1, v1\n"
"v_min_u32 v192, 23, v192\n"
"v_mfma_f32_16x16x32_bf16 v[72:75], a[156:159], a[28:31], v[72:75]\n"
"ds_read_b128 a[152:155], v2 offset:57344\n"
"v_mul_u32_u24 v192, 36, v192\n"
"v_mfma_f32_16x16x32_bf16 v[76:79], a[156:159], a[32:35], v[76:79]\n"
"buffer_load_dwordx4 v3, s[16:19], s61 offen offset:1024 sc1 lds\n"
"s_lshl_b32 s69, s57, 3\n"
"v_mfma_f32_16x16x32_bf16 v[80:83], a[160:163], a[28:31], v[80:83]\n"
"ds_read_b128 a[156:159], v2 offset:58368\n"
"s_sub_u32 s70, 28, s69\n"
"v_and_b32 v193, 1, v1\n"
"v_mfma_f32_16x16x32_bf16 v[84:87], a[160:163], a[32:35], v[84:87]\n"
"v_mad_u32_u24 v192, v193, s70, v192\n"
"s_waitcnt lgkmcnt(4)\n"
"v_mfma_f32_16x16x32_bf16 v[88:91], a[164:167], a[28:31], v[88:91]\n"
"ds_read_b128 a[160:163], v2 offset:59392\n"
"s_mul_i32 s70, s56, 864\n"
"v_mfma_f32_16x16x32_bf16 v[92:95], a[164:167], a[32:35], v[92:95]\n"
"buffer_load_dwordx4 v3, s[16:19], s61 offen offset:2048 sc1 lds\n"
"s_add_u32 s69, s69, s70\n"
"v_mfma_f32_16x16x32_bf16 v[96:99], a[168:171], a[28:31], v[96:99]\n"
"ds_read_b128 a[164:167], v2 offset:60416\n"
"s_add_u32 s69, s69, 12288\n"
"buffer_load_dwordx2 v[194:195], v192, s[16:19], s69 offen sc1\n"
"v_mfma_f32_16x16x32_bf16 v[100:103], a[168:171], a[32:35], v[100:103]\n"
"v_exp_f32 v207, v207\n"
"v_mfma_f32_16x16x32_bf16 v[104:107], a[172:175], a[28:31], v[104:107]\n"
"ds_read_b128 a[168:171], v2 offset:61440\n"
"v_mfma_f32_16x16x32_bf16 v[108:111], a[172:175], a[32:35], v[108:111]\n"
"s_waitcnt lgkmcnt(4)\n"
"v_mfma_f32_16x16x32_bf16 v[40:43], a[144:147], a[36:39], v[40:43]\n"
"ds_read_b128 a[172:175], v2 offset:62464\n"
"v_exp_f32 v251, v251\n"
"v_mfma_f32_16x16x32_bf16 v[44:47], a[144:147], a[40:43], v[44:47]\n"
"buffer_load_dwordx4 v3, s[16:19], s61 offen offset:3072 sc1 lds\n"
"s_add_u32 m0, s60, 8192\n"
"v_mfma_f32_16x16x32_bf16 v[48:51], a[148:151], a[36:39], v[48:51]\n"
"ds_read_b128 a[144:147], v2 offset:63488\n"
"v_add_f32 v222, v222, v206\n"
"v_mfma_f32_16x16x32_bf16 v[52:55], a[148:151], a[40:43], v[52:55]\n"
"v_add_f32 v223, v223, v250\n"
"v_fmamk_f32 v216, v252, 0x3fb8aa3b, v220\n"
"v_mfma_f32_16x16x32_bf16 v[56:59], a[152:155], a[36:39], v[56:59]\n"
"ds_read_b128 a[148:151], v2 offset:64512\n"
"v_fmamk_f32 v252, v252, 0xbfb8aa3b, v221\n"
"v_mfma_f32_16x16x32_bf16 v[60:63], a[152:155], a[40:43], v[60:63]\n"
"buffer_load_dwordx4 v4, s[16:19], s61 offen offset:0 sc1 lds\n"
"s_waitcnt lgkmcnt(4)\n"
"v_mfma_f32_16x16x32_bf16 v[64:67], a[156:159], a[36:39], v[64:67]\n"
"ds_read_b128 a[152:155], v5 offset:0\n"
"v_mfma_f32_16x16x32_bf16 v[68:71], a[156:159], a[40:43], v[68:71]\n"
"v_exp_f32 v216, v216\n"
"v_mfma_f32_16x16x32_bf16 v[72:75], a[160:163], a[36:39], v[72:75]\n"
"ds_read_b128 a[156:159], v5 offset:1024\n"
"v_exp_f32 v252, v252\n"
"v_mfma_f32_16x16x32_bf16 v[76:79], a[160:163], a[40:43], v[76:79]\n"
"v_add_f32 v222, v222, v207\n"
"v_add_f32 v223, v223, v251\n"
"v_mfma_f32_16x16x32_bf16 v[80:83], a[164:167], a[36:39], v[80:83]\n"
"ds_read_b128 a[160:163], v5 offset:2048\n"
"buffer_load_dwordx4 a[128:131], v2, s[16:19], s62 offen offset:3072 sc1\n"
"v_mfma_f32_16x16x32_bf16 v[84:87], a[164:167], a[40:43], v[84:87]\n"
"v_fmamk_f32 v217, v253, 0x3fb8aa3b, v220\n"
"s_waitcnt lgkmcnt(4)\n"
"v_mfma_f32_16x16x32_bf16 v[88:91], a[168:171], a[36:39], v[88:91]\n"
"ds_read_b128 a[164:167], v5 offset:3072\n"
"v_fmamk_f32 v253, v253, 0xbfb8aa3b, v221\n"
"v_mfma_f32_16x16x32_bf16 v[92:95], a[168:171], a[40:43], v[92:95]\n"
"v_exp_f32 v217, v217\n"
"v_mfma_f32_16x16x32_bf16 v[96:99], a[172:175], a[36:39], v[96:99]\n"
"ds_read_b128 a[168:171], v5 offset:4096\n"
"buffer_load_dwordx4 a[132:135], v3, s[16:19], s62 offen offset:0 sc1\n"
"v_mfma_f32_16x16x32_bf16 v[100:103], a[172:175], a[40:43], v[100:103]\n"
"v_exp_f32 v253, v253\n"
"v_mfma_f32_16x16x32_bf16 v[104:107], a[144:147], a[36:39], v[104:107]\n"
"ds_read_b128 a[172:175], v5 offset:5120\n"
"v_add_f32 v222, v222, v216\n"
"v_add_f32 v223, v223, v252\n"
"v_mfma_f32_16x16x32_bf16 v[108:111], a[144:147], a[40:43], v[108:111]\n"
"v_add_f32 v222, v222, v217\n"
"s_waitcnt lgkmcnt(4)\n"
"v_mfma_f32_16x16x32_bf16 v[40:43], a[148:151], a[44:47], v[40:43]\n"
"ds_read_b128 a[144:147], v5 offset:6144\n"
"buffer_load_dwordx4 a[136:139], v3, s[16:19], s62 offen offset:1024 sc1\n"
"v_mfma_f32_16x16x32_bf16 v[44:47], a[148:151], a[48:51], v[44:47]\n"
"v_add_f32 v223, v223, v253\n"
"v_mfma_f32_16x16x32_bf16 v[48:51], a[152:155], a[44:47], v[48:51]\n"
"ds_read_b128 a[148:151], v5 offset:7168\n"
"v_rcp_f32 v222, v222\n"
"v_mfma_f32_16x16x32_bf16 v[52:55], a[152:155], a[48:51], v[52:55]\n"
"v_rcp_f32 v223, v223\n"
"v_mfma_f32_16x16x32_bf16 v[56:59], a[156:159], a[44:47], v[56:59]\n"
"v_mul_f32 v184, v184, v222\n"
"v_mul_f32 v185, v185, v222\n"
"v_mfma_f32_16x16x32_bf16 v[60:63], a[156:159], a[48:51], v[60:63]\n"
"buffer_load_dwordx4 a[140:143], v3, s[16:19], s62 offen offset:2048 sc1\n"
"s_waitcnt lgkmcnt(3)\n"
"v_mfma_f32_16x16x32_bf16 v[64:67], a[160:163], a[44:47], v[64:67]\n"
"v_mul_f32 v186, v186, v222\n"
"v_mul_f32 v187, v187, v222\n"
"v_cvt_pk_bf16_f32 v8, v184, v185\n"
"v_mfma_f32_16x16x32_bf16 v[68:71], a[160:163], a[48:51], v[68:71]\n"
"s_waitcnt vmcnt(24)\n"
"s_waitcnt lgkmcnt(0)\n"
"s_barrier\n"
"v_mfma_f32_16x16x32_bf16 v[72:75], a[164:167], a[44:47], v[72:75]\n"
"ds_read_b128 a[152:155], v5 offset:45056\n"
"s_add_u32 s61, s61, 36864\n"
"v_mfma_f32_16x16x32_bf16 v[76:79], a[164:167], a[48:51], v[76:79]\n"
"ds_read_b128 a[156:159], v5 offset:46080\n"
"s_add_u32 m0, s60, 36864\n"
"v_mfma_f32_16x16x32_bf16 v[80:83], a[168:171], a[44:47], v[80:83]\n"
"ds_read_b128 a[160:163], v5 offset:47104\n"
"buffer_load_dwordx4 a[176:179], v3, s[16:19], s62 offen offset:3072 sc1\n"
"v_mul_f32 v188, v188, v222\n"
"v_mfma_f32_16x16x32_bf16 v[84:87], a[168:171], a[48:51], v[84:87]\n"
"ds_read_b128 a[164:167], v5 offset:48128\n"
"v_mfma_f32_16x16x32_bf16 v[88:91], a[172:175], a[44:47], v[88:91]\n"
"ds_read_b128 a[168:171], v5 offset:49152\n"
"s_add_u32 s62, s62, 8192\n"
"v_mfma_f32_16x16x32_bf16 v[92:95], a[172:175], a[48:51], v[92:95]\n"
"buffer_load_dwordx4 a[180:183], v2, s[16:19], s62 offen offset:0 sc1\n"
"v_mul_f32 v189, v189, v222\n"
"v_mfma_f32_16x16x32_bf16 v[96:99], a[144:147], a[44:47], v[96:99]\n"
"ds_read_b128 a[172:175], v5 offset:50176\n"
"v_cvt_pk_bf16_f32 v9, v186, v187\n"
"v_mul_f32 v190, v190, v222\n"
"v_mfma_f32_16x16x32_bf16 v[100:103], a[144:147], a[48:51], v[100:103]\n"
"v_mul_f32 v191, v191, v222\n"
"v_cvt_pk_bf16_f32 v10, v188, v189\n"
"v_mfma_f32_16x16x32_bf16 v[104:107], a[148:151], a[44:47], v[104:107]\n"
"ds_read_b128 a[144:147], v5 offset:51200\n"
"v_mul_f32 v200, v200, v222\n"
"v_mfma_f32_16x16x32_bf16 v[108:111], a[148:151], a[48:51], v[108:111]\n"
"buffer_load_dwordx4 a[184:187], v2, s[16:19], s62 offen offset:1024 sc1\n"
"s_waitcnt lgkmcnt(4)\n"
"v_mfma_f32_16x16x32_bf16 v[40:43], a[152:155], a[52:55], v[40:43]\n"
"ds_read_b128 a[148:151], v5 offset:52224\n"
"v_mul_f32 v201, v201, v222\n"
"v_cvt_pk_bf16_f32 v11, v190, v191\n"
"v_mfma_f32_16x16x32_bf16 v[44:47], a[152:155], a[56:59], v[44:47]\n"
"v_mul_f32 v202, v202, v222\n"
"v_mul_f32 v203, v203, v222\n"
"v_mfma_f32_16x16x32_bf16 v[48:51], a[156:159], a[52:55], v[48:51]\n"
"ds_read_b128 a[152:155], v5 offset:53248\n"
"v_cvt_pk_bf16_f32 v12, v200, v201\n"
"v_mfma_f32_16x16x32_bf16 v[52:55], a[156:159], a[56:59], v[52:55]\n"
"buffer_load_dwordx4 v2, s[16:19], s61 offen offset:0 sc1 lds\n"
"v_mul_f32 v204, v204, v222\n"
"v_mfma_f32_16x16x32_bf16 v[56:59], a[160:163], a[52:55], v[56:59]\n"
"ds_read_b128 a[156:159], v5 offset:54272\n"
"v_mul_f32 v205, v205, v222\n"
"v_cvt_pk_bf16_f32 v13, v202, v203\n"
"v_mfma_f32_16x16x32_bf16 v[60:63], a[160:163], a[56:59], v[60:63]\n"
"v_mul_f32 v206, v206, v222\n"
"s_waitcnt lgkmcnt(4)\n"
"v_mfma_f32_16x16x32_bf16 v[64:67], a[164:167], a[52:55], v[64:67]\n"
"ds_read_b128 a[160:163], v5 offset:55296\n"
"v_mul_f32 v207, v207, v222\n"
"v_mfma_f32_16x16x32_bf16 v[68:71], a[164:167], a[56:59], v[68:71]\n"
"v_cvt_pk_bf16_f32 v14, v204, v205\n"
"v_mul_f32 v216, v216, v222\n"
"v_mfma_f32_16x16x32_bf16 v[72:75], a[168:171], a[52:55], v[72:75]\n"
"ds_read_b128 a[164:167], v5 offset:56320\n"
"buffer_load_dwordx4 v2, s[16:19], s61 offen offset:1024 sc1 lds\n"
"v_mul_f32 v217, v217, v222\n"
"v_mfma_f32_16x16x32_bf16 v[76:79], a[168:171], a[56:59], v[76:79]\n"
"v_cvt_pk_bf16_f32 v15, v206, v207\n"
"v_cvt_pk_bf16_f32 v22, v216, v217\n"
"v_mfma_f32_16x16x32_bf16 v[80:83], a[172:175], a[52:55], v[80:83]\n"
"ds_read_b128 a[168:171], v5 offset:57344\n"
"v_mul_f32 v208, v208, v223\n"
"v_mfma_f32_16x16x32_bf16 v[84:87], a[172:175], a[56:59], v[84:87]\n"
"v_mul_f32 v209, v209, v223\n"
"s_waitcnt lgkmcnt(4)\n"
"v_mfma_f32_16x16x32_bf16 v[88:91], a[144:147], a[52:55], v[88:91]\n"
"ds_read_b128 a[172:175], v5 offset:58368\n"
"buffer_load_dwordx4 v2, s[16:19], s61 offen offset:2048 sc1 lds\n"
"v_mul_f32 v210, v210, v223\n"
"v_mfma_f32_16x16x32_bf16 v[92:95], a[144:147], a[56:59], v[92:95]\n"
"v_mul_f32 v211, v211, v223\n"
"v_cvt_pk_bf16_f32 v23, v208, v209\n"
"v_mfma_f32_16x16x32_bf16 v[96:99], a[148:151], a[52:55], v[96:99]\n"
"ds_read_b128 a[144:147], v5 offset:59392\n"
"v_mul_f32 v240, v240, v223\n"
"v_mfma_f32_16x16x32_bf16 v[100:103], a[148:151], a[56:59], v[100:103]\n"
"v_mul_f32 v241, v241, v223\n"
"v_cvt_pk_bf16_f32 v24, v210, v211\n"
"v_mfma_f32_16x16x32_bf16 v[104:107], a[152:155], a[52:55], v[104:107]\n"
"ds_read_b128 a[148:151], v5 offset:60416\n"
"buffer_load_dwordx4 v2, s[16:19], s61 offen offset:3072 sc1 lds\n"
"v_mul_f32 v242, v242, v223\n"
"v_mfma_f32_16x16x32_bf16 v[108:111], a[152:155], a[56:59], v[108:111]\n"
"s_add_u32 m0, s60, 40960\n"
"s_waitcnt lgkmcnt(4)\n"
"v_mfma_f32_16x16x32_bf16 v[40:43], a[156:159], a[60:63], v[40:43]\n"
"ds_read_b128 a[152:155], v5 offset:61440\n"
"v_mul_f32 v243, v243, v223\n"
"v_mfma_f32_16x16x32_bf16 v[44:47], a[156:159], a[64:67], v[44:47]\n"
"v_cvt_pk_bf16_f32 v25, v240, v241\n"
"v_mul_f32 v244, v244, v223\n"
"v_mfma_f32_16x16x32_bf16 v[48:51], a[160:163], a[60:63], v[48:51]\n"
"ds_read_b128 a[156:159], v5 offset:62464\n"
"buffer_load_dwordx4 v3, s[16:19], s61 offen offset:0 sc1 lds\n"
"v_mul_f32 v245, v245, v223\n"
"v_mfma_f32_16x16x32_bf16 v[52:55], a[160:163], a[64:67], v[52:55]\n"
"v_cvt_pk_bf16_f32 v26, v242, v243\n"
"v_mul_f32 v246, v246, v223\n"
"v_mfma_f32_16x16x32_bf16 v[56:59], a[164:167], a[60:63], v[56:59]\n"
"ds_read_b128 a[160:163], v5 offset:63488\n"
"v_mul_f32 v247, v247, v223\n"
"v_mfma_f32_16x16x32_bf16 v[60:63], a[164:167], a[64:67], v[60:63]\n"
"v_cvt_pk_bf16_f32 v27, v244, v245\n"
"s_waitcnt lgkmcnt(4)\n"
"v_mfma_f32_16x16x32_bf16 v[64:67], a[168:171], a[60:63], v[64:67]\n"
"ds_read_b128 a[164:167], v5 offset:64512\n"
"buffer_load_dwordx4 v3, s[16:19], s61 offen offset:1024 sc1 lds\n"
"v_mul_f32 v248, v248, v223\n"
"v_mfma_f32_16x16x32_bf16 v[68:71], a[168:171], a[64:67], v[68:71]\n"
"v_mul_f32 v249, v249, v223\n"
"v_cvt_pk_bf16_f32 v28, v246, v247\n"
"v_mfma_f32_16x16x32_bf16 v[72:75], a[172:175], a[60:63], v[72:75]\n"
"ds_read_b128 a[168:171], v0 offset:0\n"
"v_mul_f32 v250, v250, v223\n"
"v_mfma_f32_16x16x32_bf16 v[76:79], a[172:175], a[64:67], v[76:79]\n"
"v_mul_f32 v251, v251, v223\n"
"v_cvt_pk_bf16_f32 v29, v248, v249\n"
"v_mfma_f32_16x16x32_bf16 v[80:83], a[144:147], a[60:63], v[80:83]\n"
"ds_read_b128 a[172:175], v0 offset:1024\n"
"v_mul_f32 v252, v252, v223\n"
"v_mul_f32 v253, v253, v223\n"
"v_mfma_f32_16x16x32_bf16 v[84:87], a[144:147], a[64:67], v[84:87]\n"
"buffer_load_dwordx4 v3, s[16:19], s61 offen offset:2048 sc1 lds\n"
"s_waitcnt lgkmcnt(4)\n"
"v_mfma_f32_16x16x32_bf16 v[88:91], a[148:151], a[60:63], v[88:91]\n"
"ds_read_b128 a[144:147], v0 offset:2048\n"
"v_cvt_pk_bf16_f32 v30, v250, v251\n"
"v_mfma_f32_16x16x32_bf16 v[92:95], a[148:151], a[64:67], v[92:95]\n"
"v_cvt_pk_bf16_f32 v31, v252, v253\n"
"v_mov_b32 v32, 0\n"
"v_mfma_f32_16x16x32_bf16 v[96:99], a[152:155], a[60:63], v[96:99]\n"
"ds_read_b128 a[148:151], v0 offset:3072\n"
"v_mov_b32 v33, 0\n"
"v_mfma_f32_16x16x32_bf16 v[100:103], a[152:155], a[64:67], v[100:103]\n"
"buffer_load_dwordx4 v3, s[16:19], s61 offen offset:3072 sc1 lds\n"
"s_add_u32 m0, s60, 45056\n"
"v_mfma_f32_16x16x32_bf16 v[104:107], a[156:159], a[60:63], v[104:107]\n"
"ds_read_b128 a[152:155], v0 offset:4096\n"
"v_mfma_f32_16x16x32_bf16 v[108:111], a[156:159], a[64:67], v[108:111]\n"
"s_waitcnt lgkmcnt(4)\n"
"v_mfma_f32_16x16x32_bf16 v[40:43], a[160:163], a[68:71], v[40:43]\n"
"ds_read_b128 a[156:159], v0 offset:5120\n"
"v_exp_f32 v7, v7\n"
"v_mfma_f32_16x16x32_bf16 v[44:47], a[160:163], a[72:75], v[44:47]\n"
"buffer_load_dwordx4 v4, s[16:19], s61 offen offset:0 sc1 lds\n"
"v_mfma_f32_16x16x32_bf16 v[48:51], a[164:167], a[68:71], v[48:51]\n"
".Lmk_PCHERE_5:\n"
"s_add_u32 s0, s0, .Lmk_PCHERE_5-.Lmk_PCHERE_4\n"
"s_addc_u32 s1, s1, 0\n"
"s_sub_u32 s2, s2, .Lmk_PCHERE_5-.Lmk_PCHERE_4\n"
"s_max_i32 s2, s2, 0\n"
"buffer_load_dword a255, off, s[0:3], 0\n"
"ds_read_b128 a[160:163], v0 offset:6144\n"
"v_mfma_f32_16x16x32_bf16 v[52:55], a[164:167], a[72:75], v[52:55]\n"
"v_exp_f32 v16, v16\n"
"v_mfma_f32_16x16x32_bf16 v[56:59], a[168:171], a[68:71], v[56:59]\n"
"ds_read_b128 a[164:167], v0 offset:7168\n"
"s_waitcnt vmcnt(19)\n"
"v_xor_b32 v194, s55, v194\n"
"v_xor_b32 v195, s55, v195\n"
"v_mfma_f32_16x16x32_bf16 v[60:63], a[168:171], a[72:75], v[60:63]\n"
"v_or_b32 v194, v194, v195\n"
"s_waitcnt lgkmcnt(4)\n"
"v_mfma_f32_16x16x32_bf16 v[64:67], a[172:175], a[68:71], v[64:67]\n"
"ds_read_b128 a[168:171], v0 offset:8192\n"
"buffer_load_dwordx4 a[188:191], v2, s[16:19], s62 offen offset:2048 sc1\n"
"v_mfma_f32_16x16x32_bf16 v[68:71], a[172:175], a[72:75], v[68:71]\n"
"v_cmp_ne_u32 vcc, 0, v194\n"
"s_cbranch_vccnz .Lmk_SLOW_3\n"
".Lmk_BACK_3:\n"
"v_mfma_f32_16x16x32_bf16 v[72:75], a[144:147], a[68:71], v[72:75]\n"
"ds_read_b128 a[172:175], v0 offset:9216\n"
"v_lshlrev_b32 v192, 2, v1\n"
"s_add_u32 s71, s71, 4096\n"
"v_mfma_f32_16x16x32_bf16 v[76:79], a[144:147], a[72:75], v[76:79]\n"
"v_exp_f32 v37, v37\n"
"v_mfma_f32_16x16x32_bf16 v[80:83], a[148:151], a[68:71], v[80:83]\n"
"ds_read_b128 a[144:147], v0 offset:10240\n"
"buffer_load_dwordx4 a[192:195], v2, s[16:19], s62 offen offset:3072 sc1\n"
"v_mfma_f32_16x16x32_bf16 v[84:87], a[148:151], a[72:75], v[84:87]\n"
"v_add_f32 v7, 1.0, v7\n"
"s_waitcnt lgkmcnt(4)\n"
"v_mfma_f32_16x16x32_bf16 v[88:91], a[152:155], a[68:71], v[88:91]\n"
"ds_read_b128 a[148:151], v0 offset:11264\n"
"v_add_f32 v16, 1.0, v16\n"
"v_add_f32 v37, 1.0, v37\n"
"v_mfma_f32_16x16x32_bf16 v[92:95], a[152:155], a[72:75], v[92:95]\n"
"buffer_load_dword v193, v192, s[16:19], s71 offen sc1\n"
"v_mfma_f32_16x16x32_bf16 v[96:99], a[156:159], a[68:71], v[96:99]\n"
"ds_read_b128 a[152:155], v0 offset:12288\n"
"v_mfma_f32_16x16x32_bf16 v[100:103], a[156:159], a[72:75], v[100:103]\n"
"v_rcp_f32 v7, v7\n"
"v_mfma_f32_16x16x32_bf16 v[104:107], a[160:163], a[68:71], v[104:107]\n"
"ds_read_b128 a[156:159], v0 offset:13312\n"
"buffer_load_dwordx4 a[196:199], v3, s[16:19], s62 offen offset:0 sc1\n"
"v_mfma_f32_16x16x32_bf16 v[108:111], a[160:163], a[72:75], v[108:111]\n"
"s_waitcnt lgkmcnt(4)\n"
"v_mfma_f32_16x16x32_bf16 v[40:43], a[164:167], a[76:79], v[40:43]\n"
"ds_read_b128 a[160:163], v0 offset:14336\n"
"v_mfma_f32_16x16x32_bf16 v[44:47], a[164:167], a[80:83], v[44:47]\n"
"v_rcp_f32 v16, v16\n"
"v_mfma_f32_16x16x32_bf16 v[48:51], a[168:171], a[76:79], v[48:51]\n"
"ds_read_b128 a[164:167], v0 offset:15360\n"
"buffer_load_dwordx4 a[200:203], v3, s[16:19], s62 offen offset:1024 sc1\n"
"v_mfma_f32_16x16x32_bf16 v[52:55], a[168:171], a[80:83], v[52:55]\n"
"v_rcp_f32 v37, v37\n"
"v_mfma_f32_16x16x32_bf16 v[56:59], a[172:175], a[76:79], v[56:59]\n"
"v_mul_f32 v121, v121, v7\n"
"v_mul_f32 v122, v122, v16\n"
"v_mfma_f32_16x16x32_bf16 v[60:63], a[172:175], a[80:83], v[60:63]\n"
"v_mul_f32 v123, v123, v37\n"
"s_waitcnt lgkmcnt(3)\n"
"v_mfma_f32_16x16x32_bf16 v[64:67], a[144:147], a[76:79], v[64:67]\n"
"v_mul_f32 v7, v124, v124\n"
"v_mul_f32 v16, v125, v125\n"
"v_mul_f32 v37, v126, v126\n"
"v_mfma_f32_16x16x32_bf16 v[68:71], a[144:147], a[80:83], v[68:71]\n"
"s_waitcnt lgkmcnt(0)\n"
"s_barrier\n"
"v_mfma_f32_16x16x32_bf16 v[72:75], a[148:151], a[76:79], v[72:75]\n"
"ds_read_b128 a[168:171], v5 offset:8192\n"
"s_add_u32 s61, s61, 36864\n"
"v_mfma_f32_16x16x32_bf16 v[76:79], a[148:151], a[80:83], v[76:79]\n"
"ds_read_b128 a[172:175], v5 offset:9216\n"
"s_add_u32 m0, s60, 110592\n"
"v_mfma_f32_16x16x32_bf16 v[80:83], a[152:155], a[76:79], v[80:83]\n"
"ds_read_b128 a[144:147], v5 offset:10240\n"
"buffer_load_dwordx4 a[204:207], v3, s[16:19], s62 offen offset:2048 sc1\n"
"v_fma_f32 v7, v7, s80, v38\n"
"v_mfma_f32_16x16x32_bf16 v[84:87], a[152:155], a[80:83], v[84:87]\n"
"ds_read_b128 a[148:151], v5 offset:11264\n"
"v_mfma_f32_16x16x32_bf16 v[88:91], a[156:159], a[76:79], v[88:91]\n"
"ds_read_b128 a[152:155], v5 offset:12288\n"
"buffer_load_dwordx4 a[208:211], v3, s[16:19], s62 offen offset:3072 sc1\n"
"v_mfma_f32_16x16x32_bf16 v[92:95], a[156:159], a[80:83], v[92:95]\n"
"s_add_u32 s62, s62, 8192\n"
"v_fma_f32 v16, v16, s80, v38\n"
"v_mfma_f32_16x16x32_bf16 v[96:99], a[160:163], a[76:79], v[96:99]\n"
"ds_read_b128 a[156:159], v5 offset:13312\n"
"v_fma_f32 v37, v37, s80, v38\n"
"v_mul_f32 v7, v7, v124\n"
"v_mfma_f32_16x16x32_bf16 v[100:103], a[160:163], a[80:83], v[100:103]\n"
"buffer_load_dwordx4 a[212:215], v2, s[16:19], s62 offen offset:0 sc1\n"
"v_mul_f32 v16, v16, v125\n"
"v_mfma_f32_16x16x32_bf16 v[104:107], a[164:167], a[76:79], v[104:107]\n"
"ds_read_b128 a[160:163], v5 offset:14336\n"
"v_mul_f32 v37, v37, v126\n"
"v_mfma_f32_16x16x32_bf16 v[108:111], a[164:167], a[80:83], v[108:111]\n"
"s_waitcnt lgkmcnt(4)\n"
"v_mfma_f32_16x16x32_bf16 v[40:43], a[168:171], a[84:87], v[40:43]\n"
"ds_read_b128 a[164:167], v5 offset:15360\n"
"v_exp_f32 v7, v7\n"
"v_mfma_f32_16x16x32_bf16 v[44:47], a[168:171], a[88:91], v[44:47]\n"
"buffer_load_dwordx4 a[216:219], v2, s[16:19], s62 offen offset:1024 sc1\n"
"v_mfma_f32_16x16x32_bf16 v[48:51], a[172:175], a[84:87], v[48:51]\n"
"ds_read_b128 a[168:171], v5 offset:16384\n"
"v_mfma_f32_16x16x32_bf16 v[52:55], a[172:175], a[88:91], v[52:55]\n"
"v_exp_f32 v16, v16\n"
"v_mfma_f32_16x16x32_bf16 v[56:59], a[144:147], a[84:87], v[56:59]\n"
"ds_read_b128 a[172:175], v5 offset:17408\n"
"v_exp_f32 v37, v37\n"
"v_mfma_f32_16x16x32_bf16 v[60:63], a[144:147], a[88:91], v[60:63]\n"
"buffer_load_dwordx4 v2, s[16:19], s61 offen offset:0 sc1 lds\n"
"s_waitcnt lgkmcnt(4)\n"
"v_mfma_f32_16x16x32_bf16 v[64:67], a[148:151], a[84:87], v[64:67]\n"
"ds_read_b128 a[144:147], v5 offset:18432\n"
"v_add_f32 v7, 1.0, v7\n"
"v_mfma_f32_16x16x32_bf16 v[68:71], a[148:151], a[88:91], v[68:71]\n"
"v_add_f32 v16, 1.0, v16\n"
"v_add_f32 v37, 1.0, v37\n"
"v_mfma_f32_16x16x32_bf16 v[72:75], a[152:155], a[84:87], v[72:75]\n"
"ds_read_b128 a[148:151], v5 offset:19456\n"
"v_rcp_f32 v7, v7\n"
"v_mfma_f32_16x16x32_bf16 v[76:79], a[152:155], a[88:91], v[76:79]\n"
"v_rcp_f32 v16, v16\n"
"v_mfma_f32_16x16x32_bf16 v[80:83], a[156:159], a[84:87], v[80:83]\n"
"ds_read_b128 a[152:155], v5 offset:20480\n"
"buffer_load_dwordx4 v2, s[16:19], s61 offen offset:1024 sc1 lds\n"
"v_mfma_f32_16x16x32_bf16 v[84:87], a[156:159], a[88:91], v[84:87]\n"
"s_waitcnt lgkmcnt(4)\n"
"v_mfma_f32_16x16x32_bf16 v[88:91], a[160:163], a[84:87], v[88:91]\n"
"ds_read_b128 a[156:159], v5 offset:21504\n"
"v_rcp_f32 v37, v37\n"
"v_mfma_f32_16x16x32_bf16 v[92:95], a[160:163], a[88:91], v[92:95]\n"
"v_mul_f32 v124, v124, v7\n"
"v_mul_f32 v125, v125, v16\n"
"v_mfma_f32_16x16x32_bf16 v[96:99], a[164:167], a[84:87], v[96:99]\n"
"ds_read_b128 a[160:163], v5 offset:22528\n"
"buffer_load_dwordx4 v2, s[16:19], s61 offen offset:2048 sc1 lds\n"
"v_mfma_f32_16x16x32_bf16 v[100:103], a[164:167], a[88:91], v[100:103]\n"
"v_mul_f32 v126, v126, v37\n"
"v_mul_f32 v7, v127, v127\n"
"v_mfma_f32_16x16x32_bf16 v[104:107], a[168:171], a[84:87], v[104:107]\n"
"ds_read_b128 a[164:167], v5 offset:23552\n"
"v_mul_f32 v16, v128, v128\n"
"v_mul_f32 v37, v129, v129\n"
"v_mfma_f32_16x16x32_bf16 v[108:111], a[168:171], a[88:91], v[108:111]\n"
"v_fma_f32 v7, v7, s80, v38\n"
"s_waitcnt lgkmcnt(4)\n"
"v_mfma_f32_16x16x32_bf16 v[40:43], a[172:175], a[92:95], v[40:43]\n"
"ds_read_b128 a[168:171], v5 offset:24576\n"
"buffer_load_dwordx4 v2, s[16:19], s61 offen offset:3072 sc1 lds\n"
"v_mfma_f32_16x16x32_bf16 v[44:47], a[172:175], a[96:99], v[44:47]\n"
"s_add_u32 m0, s60, 114688\n"
"v_fma_f32 v16, v16, s80, v38\n"
"v_mfma_f32_16x16x32_bf16 v[48:51], a[144:147], a[92:95], v[48:51]\n"
"ds_read_b128 a[172:175], v5 offset:25600\n"
"v_fma_f32 v37, v37, s80, v38\n"
"v_mul_f32 v7, v7, v127\n"
"v_mfma_f32_16x16x32_bf16 v[52:55], a[144:147], a[96:99], v[52:55]\n"
"v_mul_f32 v16, v16, v128\n"
"v_mul_f32 v37, v37, v129\n"
"v_mfma_f32_16x16x32_bf16 v[56:59], a[148:151], a[92:95], v[56:59]\n"
"ds_read_b128 a[144:147], v5 offset:26624\n"
"buffer_load_dwordx4 v3, s[16:19], s61 offen offset:0 sc1 lds\n"
"v_mfma_f32_16x16x32_bf16 v[60:63], a[148:151], a[96:99], v[60:63]\n"
"s_waitcnt lgkmcnt(4)\n"
"v_mfma_f32_16x16x32_bf16 v[64:67], a[152:155], a[92:95], v[64:67]\n"
"ds_read_b128 a[148:151], v5 offset:27648\n"
"v_exp_f32 v7, v7\n"
"v_mfma_f32_16x16x32_bf16 v[68:71], a[152:155], a[96:99], v[68:71]\n"
"v_exp_f32 v16, v16\n"
"v_mfma_f32_16x16x32_bf16 v[72:75], a[156:159], a[92:95], v[72:75]\n"
"ds_read_b128 a[152:155], v5 offset:28672\n"
"buffer_load_dwordx4 v3, s[16:19], s61 offen offset:1024 sc1 lds\n"
"v_mfma_f32_16x16x32_bf16 v[76:79], a[156:159], a[96:99], v[76:79]\n"
"v_exp_f32 v37, v37\n"
"v_mfma_f32_16x16x32_bf16 v[80:83], a[160:163], a[92:95], v[80:83]\n"
"ds_read_b128 a[156:159], v5 offset:29696\n"
"v_add_f32 v7, 1.0, v7\n"
"v_add_f32 v16, 1.0, v16\n"
"v_mfma_f32_16x16x32_bf16 v[84:87], a[160:163], a[96:99], v[84:87]\n"
"v_add_f32 v37, 1.0, v37\n"
"s_waitcnt lgkmcnt(4)\n"
"v_mfma_f32_16x16x32_bf16 v[88:91], a[164:167], a[92:95], v[88:91]\n"
"ds_read_b128 a[160:163], v5 offset:30720\n"
"v_mfma_f32_16x16x32_bf16 v[92:95], a[164:167], a[96:99], v[92:95]\n"
"buffer_load_dwordx4 v3, s[16:19], s61 offen offset:2048 sc1 lds\n"
"v_mfma_f32_16x16x32_bf16 v[96:99], a[168:171], a[92:95], v[96:99]\n"
"ds_read_b128 a[164:167], v5 offset:31744\n"
"v_rcp_f32 v7, v7\n"
"v_mfma_f32_16x16x32_bf16 v[100:103], a[168:171], a[96:99], v[100:103]\n"
"v_rcp_f32 v16, v16\n"
"v_mfma_f32_16x16x32_bf16 v[104:107], a[172:175], a[92:95], v[104:107]\n"
"ds_read_b128 a[168:171], v5 offset:32768\n"
"v_mfma_f32_16x16x32_bf16 v[108:111], a[172:175], a[96:99], v[108:111]\n"
"buffer_load_dwordx4 v3, s[16:19], s61 offen offset:3072 sc1 lds\n"
"s_waitcnt lgkmcnt(4)\n"
"v_mfma_f32_16x16x32_bf16 v[40:43], a[144:147], a[100:103], v[40:43]\n"
"ds_read_b128 a[172:175], v5 offset:33792\n"
"s_add_u32 m0, s60, 118784\n"
"v_mfma_f32_16x16x32_bf16 v[44:47], a[144:147], a[104:107], v[44:47]\n"
"s_waitcnt vmcnt(14)\n"
"v_cmp_ne_u32 vcc, s55, v193\n"
"s_cbranch_vccnz .Lmk_SLOW_4\n"
".Lmk_BACK_4:\n"
"v_mfma_f32_16x16x32_bf16 v[48:51], a[148:151], a[100:103], v[48:51]\n"
"ds_read_b128 a[144:147], v5 offset:34816\n"
"v_and_b32 v192, 15, v1\n"
"v_mfma_f32_16x16x32_bf16 v[52:55], a[148:151], a[104:107], v[52:55]\n"
"v_lshrrev_b32 v193, 4, v1\n"
"v_cmp_gt_u32 s[84:85], 11, v192\n"
"v_mfma_f32_16x16x32_bf16 v[56:59], a[152:155], a[100:103], v[56:59]\n"
"ds_read_b128 a[148:151], v5 offset:35840\n"
"buffer_load_dwordx4 v4, s[16:19], s61 offen offset:0 sc1 lds\n"
"v_lshlrev_b32 v192, 3, v192\n"
"v_mfma_f32_16x16x32_bf16 v[60:63], a[152:155], a[104:107], v[60:63]\n"
"v_mov_b32 v194, 1073741824\n"
"s_waitcnt lgkmcnt(4)\n"
"v_mfma_f32_16x16x32_bf16 v[64:67], a[156:159], a[100:103], v[64:67]\n"
"ds_read_b128 a[152:155], v5 offset:36864\n"
"v_cndmask_b32 v192, v194, v192, s[84:85]\n"
"v_mfma_f32_16x16x32_bf16 v[68:71], a[156:159], a[104:107], v[68:71]\n"
"v_mul_u32_u24 v194, 5632, v193\n"
"v_add_u32 v194, v194, v192\n"
"v_mfma_f32_16x16x32_bf16 v[72:75], a[160:163], a[100:103], v[72:75]\n"
"ds_read_b128 a[156:159], v5 offset:37888\n"
"buffer_load_dwordx4 a[220:223], v2, s[16:19], s62 offen offset:2048 sc1\n"
"v_lshl_add_u32 v195, v193, 27, v192\n"
"v_mfma_f32_16x16x32_bf16 v[76:79], a[160:163], a[104:107], v[76:79]\n"
"s_mul_i32 s69, s53, 90112\n"
"s_mul_i32 s70, s63, 88\n"
"v_mfma_f32_16x16x32_bf16 v[80:83], a[164:167], a[100:103], v[80:83]\n"
"ds_read_b128 a[160:163], v5 offset:38912\n"
"s_add_u32 s69, s69, s70\n"
"v_mfma_f32_16x16x32_bf16 v[84:87], a[164:167], a[104:107], v[84:87]\n"
"s_add_u32 s69, s69, 1048576\n"
"s_waitcnt lgkmcnt(4)\n"
"v_mfma_f32_16x16x32_bf16 v[88:91], a[168:171], a[100:103], v[88:91]\n"
"ds_read_b128 a[164:167], v5 offset:39936\n"
"buffer_load_dwordx4 a[224:227], v2, s[16:19], s62 offen offset:3072 sc1\n"
"buffer_load_dwordx2 v[196:197], v194, s[16:19], s69 offen sc1\n"
"v_mfma_f32_16x16x32_bf16 v[92:95], a[168:171], a[104:107], v[92:95]\n"
"s_add_u32 s69, s69, 22528\n"
"buffer_load_dwordx2 v[198:199], v194, s[16:19], s69 offen sc1\n"
"v_mfma_f32_16x16x32_bf16 v[96:99], a[172:175], a[100:103], v[96:99]\n"
"ds_read_b128 a[168:171], v5 offset:40960\n"
"s_add_u32 s69, s69, 22528\n"
"v_mfma_f32_16x16x32_bf16 v[100:103], a[172:175], a[104:107], v[100:103]\n"
"buffer_load_dwordx2 v[212:213], v194, s[16:19], s69 offen sc1\n"
"s_add_u32 s69, s69, 22528\n"
"v_mfma_f32_16x16x32_bf16 v[104:107], a[144:147], a[100:103], v[104:107]\n"
"ds_read_b128 a[172:175], v5 offset:41984\n"
"buffer_load_dwordx2 v[214:215], v194, s[16:19], s69 offen sc1\n"
"s_mul_i32 s69, s53, 6144\n"
"v_mfma_f32_16x16x32_bf16 v[108:111], a[144:147], a[104:107], v[108:111]\n"
"s_add_u32 s69, s69, 589824\n"
"s_waitcnt lgkmcnt(4)\n"
"v_mfma_f32_16x16x32_bf16 v[40:43], a[148:151], a[108:111], v[40:43]\n"
"ds_read_b128 a[144:147], v5 offset:43008\n"
"s_add_u32 s70, s70, s69\n"
"v_mfma_f32_16x16x32_bf16 v[44:47], a[148:151], a[112:115], v[44:47]\n"
"v_rcp_f32 v37, v37\n"
"v_mfma_f32_16x16x32_bf16 v[48:51], a[152:155], a[108:111], v[48:51]\n"
"ds_read_b128 a[148:151], v5 offset:44032\n"
"v_mul_f32 v127, v127, v7\n"
"v_mul_f32 v128, v128, v16\n"
"v_mfma_f32_16x16x32_bf16 v[52:55], a[152:155], a[112:115], v[52:55]\n"
"v_mul_f32 v129, v129, v37\n"
"v_mul_f32 v7, v130, v130\n"
"v_mfma_f32_16x16x32_bf16 v[56:59], a[156:159], a[108:111], v[56:59]\n"
"v_mul_f32 v16, v131, v131\n"
"v_mul_f32 v37, v132, v132\n"
"v_mfma_f32_16x16x32_bf16 v[60:63], a[156:159], a[112:115], v[60:63]\n"
"buffer_load_dwordx4 a[228:231], v3, s[16:19], s62 offen offset:0 sc1\n"
"s_waitcnt lgkmcnt(3)\n"
"v_mfma_f32_16x16x32_bf16 v[64:67], a[160:163], a[108:111], v[64:67]\n"
"v_fma_f32 v7, v7, s80, v38\n"
"v_fma_f32 v16, v16, s80, v38\n"
"v_fma_f32 v37, v37, s80, v38\n"
"v_mfma_f32_16x16x32_bf16 v[68:71], a[160:163], a[112:115], v[68:71]\n"
"s_waitcnt lgkmcnt(0)\n"
"s_barrier\n"
"v_mfma_f32_16x16x32_bf16 v[72:75], a[164:167], a[108:111], v[72:75]\n"
"ds_read_b128 a[152:155], v2 offset:0\n"
"s_add_u32 s61, s61, 36864\n"
"v_mfma_f32_16x16x32_bf16 v[76:79], a[164:167], a[112:115], v[76:79]\n"
"ds_read_b128 a[156:159], v2 offset:1024\n"
"s_add_u32 m0, s60, 73728\n"
"v_mfma_f32_16x16x32_bf16 v[80:83], a[168:171], a[108:111], v[80:83]\n"
"ds_read_b128 a[160:163], v2 offset:2048\n"
"buffer_load_dwordx4 a[232:235], v3, s[16:19], s62 offen offset:1024 sc1\n"
"v_mul_f32 v7, v7, v130\n"
"v_mfma_f32_16x16x32_bf16 v[84:87], a[168:171], a[112:115], v[84:87]\n"
"ds_read_b128 a[164:167], v2 offset:3072\n"
"v_mfma_f32_16x16x32_bf16 v[88:91], a[172:175], a[108:111], v[88:91]\n"
"ds_read_b128 a[168:171], v2 offset:4096\n"
"v_mul_f32 v16, v16, v131\n"
"v_mfma_f32_16x16x32_bf16 v[92:95], a[172:175], a[112:115], v[92:95]\n"
"buffer_load_dwordx4 a[236:239], v3, s[16:19], s62 offen offset:2048 sc1\n"
"v_mul_f32 v37, v37, v132\n"
"v_mfma_f32_16x16x32_bf16 v[96:99], a[144:147], a[108:111], v[96:99]\n"
"ds_read_b128 a[172:175], v2 offset:5120\n"
"v_exp_f32 v7, v7\n"
"v_mfma_f32_16x16x32_bf16 v[100:103], a[144:147], a[112:115], v[100:103]\n"
"v_exp_f32 v16, v16\n"
"v_mfma_f32_16x16x32_bf16 v[104:107], a[148:151], a[108:111], v[104:107]\n"
"ds_read_b128 a[144:147], v2 offset:6144\n"
"v_mfma_f32_16x16x32_bf16 v[108:111], a[148:151], a[112:115], v[108:111]\n"
"buffer_load_dwordx4 a[240:243], v3, s[16:19], s62 offen offset:3072 sc1\n"
"s_waitcnt lgkmcnt(4)\n"
"v_mfma_f32_16x16x32_bf16 v[40:43], a[152:155], a[116:119], v[40:43]\n"
"ds_read_b128 a[148:151], v2 offset:7168\n"
"s_add_u32 s62, s62, 8192\n"
"v_mfma_f32_16x16x32_bf16 v[44:47], a[152:155], a[120:123], v[44:47]\n"
"v_exp_f32 v37, v37\n"
"v_mfma_f32_16x16x32_bf16 v[48:51], a[156:159], a[116:119], v[48:51]\n"
"ds_read_b128 a[152:155], v2 offset:8192\n"
"v_add_f32 v7, 1.0, v7\n"
"v_mfma_f32_16x16x32_bf16 v[52:55], a[156:159], a[120:123], v[52:55]\n"
"v_add_f32 v16, 1.0, v16\n"
"v_add_f32 v37, 1.0, v37\n"
"v_mfma_f32_16x16x32_bf16 v[56:59], a[160:163], a[116:119], v[56:59]\n"
"ds_read_b128 a[156:159], v2 offset:9216\n"
"buffer_load_dwordx4 a[244:247], v2, s[16:19], s62 offen offset:0 sc1\n"
"v_mfma_f32_16x16x32_bf16 v[60:63], a[160:163], a[120:123], v[60:63]\n"
"s_waitcnt lgkmcnt(4)\n"
"v_mfma_f32_16x16x32_bf16 v[64:67], a[164:167], a[116:119], v[64:67]\n"
"ds_read_b128 a[160:163], v2 offset:10240\n"
"v_mfma_f32_16x16x32_bf16 v[68:71], a[164:167], a[120:123], v[68:71]\n"
"v_rcp_f32 v7, v7\n"
"v_mfma_f32_16x16x32_bf16 v[72:75], a[168:171], a[116:119], v[72:75]\n"
"ds_read_b128 a[164:167], v2 offset:11264\n"
"buffer_load_dwordx4 a[248:251], v2, s[16:19], s62 offen offset:1024 sc1\n"
"v_mfma_f32_16x16x32_bf16 v[76:79], a[168:171], a[120:123], v[76:79]\n"
"v_rcp_f32 v16, v16\n"
"v_mfma_f32_16x16x32_bf16 v[80:83], a[172:175], a[116:119], v[80:83]\n"
"ds_read_b128 a[168:171], v2 offset:12288\n"
"v_mfma_f32_16x16x32_bf16 v[84:87], a[172:175], a[120:123], v[84:87]\n"
"s_waitcnt lgkmcnt(4)\n"
"v_mfma_f32_16x16x32_bf16 v[88:91], a[144:147], a[116:119], v[88:91]\n"
"ds_read_b128 a[172:175], v2 offset:13312\n"
"buffer_load_dwordx4 v2, s[16:19], s61 offen offset:0 sc1 lds\n"
"v_mfma_f32_16x16x32_bf16 v[92:95], a[144:147], a[120:123], v[92:95]\n"
"v_rcp_f32 v37, v37\n"
"v_mfma_f32_16x16x32_bf16 v[96:99], a[148:151], a[116:119], v[96:99]\n"
"ds_read_b128 a[144:147], v2 offset:14336\n"
"v_mul_f32 v130, v130, v7\n"
"v_mfma_f32_16x16x32_bf16 v[100:103], a[148:151], a[120:123], v[100:103]\n"
"v_mul_f32 v131, v131, v16\n"
"v_mul_f32 v132, v132, v37\n"
"v_mfma_f32_16x16x32_bf16 v[104:107], a[152:155], a[116:119], v[104:107]\n"
"ds_read_b128 a[148:151], v2 offset:15360\n"
"v_mul_f32 v7, v133, v133\n"
"v_mul_f32 v16, v134, v134\n"
"v_mfma_f32_16x16x32_bf16 v[108:111], a[152:155], a[120:123], v[108:111]\n"
"buffer_load_dwordx4 v2, s[16:19], s61 offen offset:1024 sc1 lds\n"
"s_waitcnt lgkmcnt(4)\n"
"v_mfma_f32_16x16x32_bf16 v[40:43], a[156:159], a[124:127], v[40:43]\n"
"ds_read_b128 a[152:155], v2 offset:16384\n"
"v_mul_f32 v37, v135, v135\n"
"v_mfma_f32_16x16x32_bf16 v[44:47], a[156:159], a[128:131], v[44:47]\n"
"v_fma_f32 v7, v7, s80, v38\n"
"v_fma_f32 v16, v16, s80, v38\n"
"v_mfma_f32_16x16x32_bf16 v[48:51], a[160:163], a[124:127], v[48:51]\n"
"ds_read_b128 a[156:159], v2 offset:17408\n"
"v_fma_f32 v37, v37, s80, v38\n"
"v_mul_f32 v7, v7, v133\n"
"v_mfma_f32_16x16x32_bf16 v[52:55], a[160:163], a[128:131], v[52:55]\n"
"buffer_load_dwordx4 v2, s[16:19], s61 offen offset:2048 sc1 lds\n"
"v_mul_f32 v16, v16, v134\n"
"v_mfma_f32_16x16x32_bf16 v[56:59], a[164:167], a[124:127], v[56:59]\n"
"ds_read_b128 a[160:163], v2 offset:18432\n"
"v_mul_f32 v37, v37, v135\n"
"v_mfma_f32_16x16x32_bf16 v[60:63], a[164:167], a[128:131], v[60:63]\n"
"s_waitcnt lgkmcnt(4)\n"
"v_mfma_f32_16x16x32_bf16 v[64:67], a[168:171], a[124:127], v[64:67]\n"
"ds_read_b128 a[164:167], v2 offset:19456\n"
"v_exp_f32 v7, v7\n"
"v_mfma_f32_16x16x32_bf16 v[68:71], a[168:171], a[128:131], v[68:71]\n"
"buffer_load_dwordx4 v2, s[16:19], s61 offen offset:3072 sc1 lds\n"
"s_add_u32 m0, s60, 77824\n"
"v_mfma_f32_16x16x32_bf16 v[72:75], a[172:175], a[124:127], v[72:75]\n"
"ds_read_b128 a[168:171], v2 offset:20480\n"
"v_mfma_f32_16x16x32_bf16 v[76:79], a[172:175], a[128:131], v[76:79]\n"
"v_exp_f32 v16, v16\n"
"v_mfma_f32_16x16x32_bf16 v[80:83], a[144:147], a[124:127], v[80:83]\n"
"ds_read_b128 a[172:175], v2 offset:21504\n"
"v_exp_f32 v37, v37\n"
"v_mfma_f32_16x16x32_bf16 v[84:87], a[144:147], a[128:131], v[84:87]\n"
"v_add_f32 v7, 1.0, v7\n"
"s_waitcnt lgkmcnt(4)\n"
"v_mfma_f32_16x16x32_bf16 v[88:91], a[148:151], a[124:127], v[88:91]\n"
"ds_read_b128 a[144:147], v2 offset:22528\n"
"buffer_load_dwordx4 v3, s[16:19], s61 offen offset:0 sc1 lds\n"
"v_mfma_f32_16x16x32_bf16 v[92:95], a[148:151], a[128:131], v[92:95]\n"
"v_add_f32 v16, 1.0, v16\n"
"v_add_f32 v37, 1.0, v37\n"
"v_mfma_f32_16x16x32_bf16 v[96:99], a[152:155], a[124:127], v[96:99]\n"
"ds_read_b128 a[148:151], v2 offset:23552\n"
"v_rcp_f32 v7, v7\n"
"v_mfma_f32_16x16x32_bf16 v[100:103], a[152:155], a[128:131], v[100:103]\n"
"v_rcp_f32 v16, v16\n"
"v_mfma_f32_16x16x32_bf16 v[104:107], a[156:159], a[124:127], v[104:107]\n"
"ds_read_b128 a[152:155], v2 offset:24576\n"
"buffer_load_dwordx4 v3, s[16:19], s61 offen offset:1024 sc1 lds\n"
"v_mfma_f32_16x16x32_bf16 v[108:111], a[156:159], a[128:131], v[108:111]\n"
"s_waitcnt lgkmcnt(4)\n"
"v_mfma_f32_16x16x32_bf16 v[40:43], a[160:163], a[132:135], v[40:43]\n"
"ds_read_b128 a[156:159], v2 offset:25600\n"
"v_rcp_f32 v37, v37\n"
"v_mfma_f32_16x16x32_bf16 v[44:47], a[160:163], a[136:139], v[44:47]\n"
"v_mul_f32 v133, v133, v7\n"
"v_mul_f32 v134, v134, v16\n"
"v_mfma_f32_16x16x32_bf16 v[48:51], a[164:167], a[132:135], v[48:51]\n"
"ds_read_b128 a[160:163], v2 offset:26624\n"
"buffer_load_dwordx4 v3, s[16:19], s61 offen offset:2048 sc1 lds\n"
"v_mfma_f32_16x16x32_bf16 v[52:55], a[164:167], a[136:139], v[52:55]\n"
"v_mul_f32 v135, v135, v37\n"
"v_mul_f32 v7, v136, v136\n"
"v_mfma_f32_16x16x32_bf16 v[56:59], a[168:171], a[132:135], v[56:59]\n"
"ds_read_b128 a[164:167], v2 offset:27648\n"
"v_mul_f32 v16, v137, v137\n"
"v_mul_f32 v37, v138, v138\n"
"v_mfma_f32_16x16x32_bf16 v[60:63], a[168:171], a[136:139], v[60:63]\n"
"v_fma_f32 v7, v7, s80, v38\n"
"s_waitcnt lgkmcnt(4)\n"
"v_mfma_f32_16x16x32_bf16 v[64:67], a[172:175], a[132:135], v[64:67]\n"
"ds_read_b128 a[168:171], v2 offset:28672\n"
"buffer_load_dwordx4 v3, s[16:19], s61 offen offset:3072 sc1 lds\n"
"v_mfma_f32_16x16x32_bf16 v[68:71], a[172:175], a[136:139], v[68:71]\n"
"s_add_u32 m0, s60, 81920\n"
"s_waitcnt vmcnt(16)\n"
"v_add_f32 v196, v196, v198\n"
"v_mfma_f32_16x16x32_bf16 v[72:75], a[144:147], a[132:135], v[72:75]\n"
"ds_read_b128 a[172:175], v2 offset:29696\n"
"v_add_f32 v197, v197, v199\n"
"s_waitcnt vmcnt(15)\n"
"v_add_f32 v196, v196, v212\n"
"v_mfma_f32_16x16x32_bf16 v[76:79], a[144:147], a[136:139], v[76:79]\n"
"v_add_f32 v197, v197, v213\n"
"s_waitcnt vmcnt(14)\n"
"v_add_f32 v196, v196, v214\n"
"v_mfma_f32_16x16x32_bf16 v[80:83], a[148:151], a[132:135], v[80:83]\n"
"ds_read_b128 a[144:147], v2 offset:30720\n"
"buffer_load_dwordx4 v4, s[16:19], s61 offen offset:0 sc1 lds\n"
"v_mfma_f32_16x16x32_bf16 v[84:87], a[148:151], a[136:139], v[84:87]\n"
"v_add_f32 v197, v197, v215\n"
"s_waitcnt lgkmcnt(4)\n"
"v_mfma_f32_16x16x32_bf16 v[88:91], a[152:155], a[132:135], v[88:91]\n"
"ds_read_b128 a[148:151], v2 offset:31744\n"
"v_mov_b32 v198, v196\n"
"v_mov_b32 v199, v197\n"
"v_mfma_f32_16x16x32_bf16 v[92:95], a[152:155], a[136:139], v[92:95]\n"
"v_permlane32_swap_b32 v196, v198\n"
"v_mfma_f32_16x16x32_bf16 v[96:99], a[156:159], a[132:135], v[96:99]\n"
"ds_read_b128 a[152:155], v2 offset:32768\n"
"v_fma_f32 v16, v16, s80, v38\n"
"v_mfma_f32_16x16x32_bf16 v[100:103], a[156:159], a[136:139], v[100:103]\n"
"buffer_load_dwordx4 a[0:3], v2, s[16:19], s62 offen offset:2048 sc1\n"
"v_fma_f32 v37, v37, s80, v38\n"
"v_mfma_f32_16x16x32_bf16 v[104:107], a[160:163], a[132:135], v[104:107]\n"
"ds_read_b128 a[156:159], v2 offset:33792\n"
"v_mul_f32 v7, v7, v136\n"
"v_mul_f32 v16, v16, v137\n"
"v_mfma_f32_16x16x32_bf16 v[108:111], a[160:163], a[136:139], v[108:111]\n"
"v_mul_f32 v37, v37, v138\n"
"s_waitcnt lgkmcnt(4)\n"
"v_mfma_f32_16x16x32_bf16 v[40:43], a[164:167], a[140:143], v[40:43]\n"
"ds_read_b128 a[160:163], v2 offset:34816\n"
"v_mfma_f32_16x16x32_bf16 v[44:47], a[164:167], a[176:179], v[44:47]\n"
"buffer_load_dwordx4 a[4:7], v2, s[16:19], s62 offen offset:3072 sc1\n"
"v_mfma_f32_16x16x32_bf16 v[48:51], a[168:171], a[140:143], v[48:51]\n"
"ds_read_b128 a[164:167], v2 offset:35840\n"
"v_permlane32_swap_b32 v197, v199\n"
"v_mfma_f32_16x16x32_bf16 v[52:55], a[168:171], a[176:179], v[52:55]\n"
"v_add_f32 v196, v196, v198\n"
"v_add_f32 v197, v197, v199\n"
"v_mfma_f32_16x16x32_bf16 v[56:59], a[172:175], a[140:143], v[56:59]\n"
"v_mov_b32 v198, v196\n"
"v_mov_b32 v199, v197\n"
"v_mfma_f32_16x16x32_bf16 v[60:63], a[172:175], a[176:179], v[60:63]\n"
"buffer_load_dwordx4 a[8:11], v3, s[16:19], s62 offen offset:0 sc1\n"
"s_waitcnt lgkmcnt(3)\n"
"v_mfma_f32_16x16x32_bf16 v[64:67], a[144:147], a[140:143], v[64:67]\n"
"v_permlane16_swap_b32 v196, v198\n"
"v_mfma_f32_16x16x32_bf16 v[68:71], a[144:147], a[176:179], v[68:71]\n"
"s_waitcnt lgkmcnt(0)\n"
"s_barrier\n"
"v_mfma_f32_16x16x32_bf16 v[72:75], a[148:151], a[140:143], v[72:75]\n"
"ds_read_b128 a[168:171], v2 offset:36864\n"
"s_add_u32 s61, s61, 36864\n"
"v_mfma_f32_16x16x32_bf16 v[76:79], a[148:151], a[176:179], v[76:79]\n"
"ds_read_b128 a[172:175], v2 offset:37888\n"
"s_add_u32 m0, s60, 0\n"
"v_mfma_f32_16x16x32_bf16 v[80:83], a[152:155], a[140:143], v[80:83]\n"
"ds_read_b128 a[144:147], v2 offset:38912\n"
"buffer_load_dwordx4 a[12:15], v3, s[16:19], s62 offen offset:1024 sc1\n"
"v_mfma_f32_16x16x32_bf16 v[84:87], a[152:155], a[176:179], v[84:87]\n"
"ds_read_b128 a[148:151], v2 offset:39936\n"
"v_mfma_f32_16x16x32_bf16 v[88:91], a[156:159], a[140:143], v[88:91]\n"
"ds_read_b128 a[152:155], v2 offset:40960\n"
"v_mfma_f32_16x16x32_bf16 v[92:95], a[156:159], a[176:179], v[92:95]\n"
"v_permlane16_swap_b32 v197, v199\n"
"v_mfma_f32_16x16x32_bf16 v[96:99], a[160:163], a[140:143], v[96:99]\n"
"ds_read_b128 a[156:159], v2 offset:41984\n"
"buffer_load_dwordx4 a[16:19], v3, s[16:19], s62 offen offset:2048 sc1\n"
"v_add_f32 v196, v196, v198\n"
"v_mfma_f32_16x16x32_bf16 v[100:103], a[160:163], a[176:179], v[100:103]\n"
"v_add_f32 v197, v197, v199\n"
"v_cvt_pk_bf16_f32 v196, v196, v197\n"
"v_mfma_f32_16x16x32_bf16 v[104:107], a[164:167], a[140:143], v[104:107]\n"
"ds_read_b128 a[160:163], v2 offset:43008\n"
"v_mov_b32 v197, v17\n"
"v_mfma_f32_16x16x32_bf16 v[108:111], a[164:167], a[176:179], v[108:111]\n"
"buffer_store_dwordx2 v[196:197], v195, s[16:19], s70 offen sc1\n"
"s_waitcnt lgkmcnt(4)\n"
"v_mfma_f32_16x16x32_bf16 v[40:43], a[168:171], a[180:183], v[40:43]\n"
"ds_read_b128 a[164:167], v2 offset:44032\n"
"s_lshl_b32 s70, s54, 6\n"
"v_add_u32 v192, s70, v1\n"
"v_mfma_f32_16x16x32_bf16 v[44:47], a[168:171], a[184:187], v[44:47]\n"
"v_lshlrev_b32 v224, 3, v192\n"
"v_mul_u32_u24 v193, 911, v192\n"
"v_mfma_f32_16x16x32_bf16 v[48:51], a[172:175], a[180:183], v[48:51]\n"
"ds_read_b128 a[168:171], v2 offset:45056\n"
"buffer_load_dwordx4 a[20:23], v3, s[16:19], s62 offen offset:3072 sc1\n"
"v_mfma_f32_16x16x32_bf16 v[52:55], a[172:175], a[184:187], v[52:55]\n"
"s_add_u32 s62, s62, 8192\n"
"v_lshrrev_b32 v193, 14, v193\n"
"v_mfma_f32_16x16x32_bf16 v[56:59], a[144:147], a[180:183], v[56:59]\n"
"ds_read_b128 a[172:175], v2 offset:46080\n"
"v_lshlrev_b32 v192, 2, v192\n"
"v_lshl_add_u32 v192, v193, 3, v192\n"
"v_mfma_f32_16x16x32_bf16 v[60:63], a[144:147], a[184:187], v[60:63]\n"
"v_mul_u32_u24 v193, 27, v193\n"
"s_waitcnt lgkmcnt(4)\n"
"v_mfma_f32_16x16x32_bf16 v[64:67], a[148:151], a[180:183], v[64:67]\n"
"ds_read_b128 a[144:147], v2 offset:47104\n"
"buffer_load_dwordx4 a[24:27], v2, s[16:19], s62 offen offset:0 sc1\n"
"v_mfma_f32_16x16x32_bf16 v[68:71], a[148:151], a[184:187], v[68:71]\n"
"v_lshrrev_b32 v193, 9, v193\n"
"v_mul_u32_u24 v193, 80, v193\n"
"v_mfma_f32_16x16x32_bf16 v[72:75], a[152:155], a[180:183], v[72:75]\n"
"ds_read_b128 a[148:151], v2 offset:48128\n"
"v_add_u32 v192, v192, v193\n"
"v_add_u32 v228, 159104, v192\n"
"v_mfma_f32_16x16x32_bf16 v[76:79], a[152:155], a[184:187], v[76:79]\n"
"v_add_u32 v192, s70, v1\n"
"v_add_u32 v192, 256, v192\n"
"v_mfma_f32_16x16x32_bf16 v[80:83], a[156:159], a[180:183], v[80:83]\n"
"ds_read_b128 a[152:155], v2 offset:49152\n"
"buffer_load_dwordx4 a[28:31], v2, s[16:19], s62 offen offset:1024 sc1\n"
"v_mfma_f32_16x16x32_bf16 v[84:87], a[156:159], a[184:187], v[84:87]\n"
"v_lshlrev_b32 v225, 3, v192\n"
"s_waitcnt lgkmcnt(4)\n"
"v_mfma_f32_16x16x32_bf16 v[88:91], a[160:163], a[180:183], v[88:91]\n"
"ds_read_b128 a[156:159], v2 offset:50176\n"
"v_mul_u32_u24 v193, 911, v192\n"
"v_lshrrev_b32 v193, 14, v193\n"
"v_mfma_f32_16x16x32_bf16 v[92:95], a[160:163], a[184:187], v[92:95]\n"
"v_lshlrev_b32 v192, 2, v192\n"
"v_lshl_add_u32 v192, v193, 3, v192\n"
"v_mfma_f32_16x16x32_bf16 v[96:99], a[164:167], a[180:183], v[96:99]\n"
"ds_read_b128 a[160:163], v2 offset:51200\n"
"buffer_load_dwordx4 v2, s[16:19], s61 offen offset:0 sc1 lds\n"
"v_mfma_f32_16x16x32_bf16 v[100:103], a[164:167], a[184:187], v[100:103]\n"
"v_mul_u32_u24 v193, 27, v193\n"
"v_lshrrev_b32 v193, 9, v193\n"
"v_mfma_f32_16x16x32_bf16 v[104:107], a[168:171], a[180:183], v[104:107]\n"
"ds_read_b128 a[164:167], v2 offset:52224\n"
"v_mul_u32_u24 v193, 80, v193\n"
"v_add_u32 v192, v192, v193\n"
"v_mfma_f32_16x16x32_bf16 v[108:111], a[168:171], a[184:187], v[108:111]\n"
"v_add_u32 v229, 159104, v192\n"
"s_waitcnt lgkmcnt(4)\n"
"v_mfma_f32_16x16x32_bf16 v[40:43], a[172:175], a[188:191], v[40:43]\n"
"ds_read_b128 a[168:171], v2 offset:53248\n"
"v_add_u32 v192, s70, v1\n"
"v_mfma_f32_16x16x32_bf16 v[44:47], a[172:175], a[192:195], v[44:47]\n"
"buffer_load_dwordx4 v2, s[16:19], s61 offen offset:1024 sc1 lds\n"
"v_add_u32 v192, 512, v192\n"
"v_mfma_f32_16x16x32_bf16 v[48:51], a[144:147], a[188:191], v[48:51]\n"
"ds_read_b128 a[172:175], v2 offset:54272\n"
"v_min_u32 v192, 683, v192\n"
"v_lshlrev_b32 v226, 3, v192\n"
"v_mfma_f32_16x16x32_bf16 v[52:55], a[144:147], a[192:195], v[52:55]\n"
"v_mul_u32_u24 v193, 911, v192\n"
"v_lshrrev_b32 v193, 14, v193\n"
"v_mfma_f32_16x16x32_bf16 v[56:59], a[148:151], a[188:191], v[56:59]\n"
"ds_read_b128 a[144:147], v2 offset:55296\n"
"v_lshlrev_b32 v192, 2, v192\n"
"v_mfma_f32_16x16x32_bf16 v[60:63], a[148:151], a[192:195], v[60:63]\n"
"buffer_load_dwordx4 v2, s[16:19], s61 offen offset:2048 sc1 lds\n"
"s_waitcnt lgkmcnt(4)\n"
"v_mfma_f32_16x16x32_bf16 v[64:67], a[152:155], a[188:191], v[64:67]\n"
"ds_read_b128 a[148:151], v2 offset:56320\n"
"v_lshl_add_u32 v192, v193, 3, v192\n"
"v_mul_u32_u24 v193, 27, v193\n"
"v_mfma_f32_16x16x32_bf16 v[68:71], a[152:155], a[192:195], v[68:71]\n"
"v_lshrrev_b32 v193, 9, v193\n"
"v_mul_u32_u24 v193, 80, v193\n"
"v_mfma_f32_16x16x32_bf16 v[72:75], a[156:159], a[188:191], v[72:75]\n"
"ds_read_b128 a[152:155], v2 offset:57344\n"
"v_add_u32 v192, v192, v193\n"
"v_mfma_f32_16x16x32_bf16 v[76:79], a[156:159], a[192:195], v[76:79]\n"
"buffer_load_dwordx4 v2, s[16:19], s61 offen offset:3072 sc1 lds\n"
"s_add_u32 m0, s60, 4096\n"
"v_mfma_f32_16x16x32_bf16 v[80:83], a[160:163], a[188:191], v[80:83]\n"
"ds_read_b128 a[156:159], v2 offset:58368\n"
"v_add_u32 v230, 159104, v192\n"
"v_mfma_f32_16x16x32_bf16 v[84:87], a[160:163], a[192:195], v[84:87]\n"
"s_waitcnt lgkmcnt(4)\n"
"v_mfma_f32_16x16x32_bf16 v[88:91], a[164:167], a[188:191], v[88:91]\n"
"ds_read_b128 a[160:163], v2 offset:59392\n"
"v_mfma_f32_16x16x32_bf16 v[92:95], a[164:167], a[192:195], v[92:95]\n"
"v_exp_f32 v7, v7\n"
"v_mfma_f32_16x16x32_bf16 v[96:99], a[168:171], a[188:191], v[96:99]\n"
"ds_read_b128 a[164:167], v2 offset:60416\n"
"buffer_load_dwordx4 v3, s[16:19], s61 offen offset:0 sc1 lds\n"
"v_mfma_f32_16x16x32_bf16 v[100:103], a[168:171], a[192:195], v[100:103]\n"
"v_exp_f32 v16, v16\n"
"v_mfma_f32_16x16x32_bf16 v[104:107], a[172:175], a[188:191], v[104:107]\n"
"ds_read_b128 a[168:171], v2 offset:61440\n"
"v_mfma_f32_16x16x32_bf16 v[108:111], a[172:175], a[192:195], v[108:111]\n"
"s_waitcnt lgkmcnt(4)\n"
"v_mfma_f32_16x16x32_bf16 v[40:43], a[144:147], a[196:199], v[40:43]\n"
"ds_read_b128 a[172:175], v2 offset:62464\n"
"buffer_load_dwordx4 v3, s[16:19], s61 offen offset:1024 sc1 lds\n"
"v_mfma_f32_16x16x32_bf16 v[44:47], a[144:147], a[200:203], v[44:47]\n"
"v_exp_f32 v37, v37\n"
"v_mfma_f32_16x16x32_bf16 v[48:51], a[148:151], a[196:199], v[48:51]\n"
"ds_read_b128 a[144:147], v2 offset:63488\n"
"v_add_f32 v7, 1.0, v7\n"
"v_mfma_f32_16x16x32_bf16 v[52:55], a[148:151], a[200:203], v[52:55]\n"
"v_add_f32 v16, 1.0, v16\n"
"v_add_f32 v37, 1.0, v37\n"
"v_mfma_f32_16x16x32_bf16 v[56:59], a[152:155], a[196:199], v[56:59]\n"
"ds_read_b128 a[148:151], v2 offset:64512\n"
"v_rcp_f32 v7, v7\n"
"v_mfma_f32_16x16x32_bf16 v[60:63], a[152:155], a[200:203], v[60:63]\n"
"buffer_load_dwordx4 v3, s[16:19], s61 offen offset:2048 sc1 lds\n"
"s_waitcnt lgkmcnt(4)\n"
"v_mfma_f32_16x16x32_bf16 v[64:67], a[156:159], a[196:199], v[64:67]\n"
"ds_read_b128 a[152:155], v5 offset:0\n"
"v_mfma_f32_16x16x32_bf16 v[68:71], a[156:159], a[200:203], v[68:71]\n"
"v_rcp_f32 v16, v16\n"
"v_mfma_f32_16x16x32_bf16 v[72:75], a[160:163], a[196:199], v[72:75]\n"
"ds_read_b128 a[156:159], v5 offset:1024\n"
"v_rcp_f32 v37, v37\n"
"v_mfma_f32_16x16x32_bf16 v[76:79], a[160:163], a[200:203], v[76:79]\n"
"buffer_load_dwordx4 v3, s[16:19], s61 offen offset:3072 sc1 lds\n"
"s_add_u32 m0, s60, 8192\n"
"v_mfma_f32_16x16x32_bf16 v[80:83], a[164:167], a[196:199], v[80:83]\n"
"ds_read_b128 a[160:163], v5 offset:2048\n"
"v_mul_f32 v136, v136, v7\n"
"v_mfma_f32_16x16x32_bf16 v[84:87], a[164:167], a[200:203], v[84:87]\n"
"v_mul_f32 v137, v137, v16\n"
"s_waitcnt lgkmcnt(4)\n"
"v_mfma_f32_16x16x32_bf16 v[88:91], a[168:171], a[196:199], v[88:91]\n"
"ds_read_b128 a[164:167], v5 offset:3072\n"
"v_mul_f32 v138, v138, v37\n"
"v_mul_f32 v7, v139, v139\n"
"v_mfma_f32_16x16x32_bf16 v[92:95], a[168:171], a[200:203], v[92:95]\n"
"buffer_load_dwordx4 v4, s[16:19], s61 offen offset:0 sc1 lds\n"
"v_mul_f32 v16, v140, v140\n"
"v_mfma_f32_16x16x32_bf16 v[96:99], a[172:175], a[196:199], v[96:99]\n"
"ds_read_b128 a[168:171], v5 offset:4096\n"
"v_mul_f32 v37, v141, v141\n"
"v_mfma_f32_16x16x32_bf16 v[100:103], a[172:175], a[200:203], v[100:103]\n"
"v_fma_f32 v7, v7, s80, v38\n"
"v_fma_f32 v16, v16, s80, v38\n"
"v_mfma_f32_16x16x32_bf16 v[104:107], a[144:147], a[196:199], v[104:107]\n"
"ds_read_b128 a[172:175], v5 offset:5120\n"
"v_fma_f32 v37, v37, s80, v38\n"
"v_mul_f32 v7, v7, v139\n"
"v_mfma_f32_16x16x32_bf16 v[108:111], a[144:147], a[200:203], v[108:111]\n"
"buffer_load_dwordx4 a[32:35], v2, s[16:19], s62 offen offset:2048 sc1\n"
"s_waitcnt lgkmcnt(4)\n"
"v_mfma_f32_16x16x32_bf16 v[40:43], a[148:151], a[204:207], v[40:43]\n"
"ds_read_b128 a[144:147], v5 offset:6144\n"
"v_mul_f32 v16, v16, v140\n"
"v_mfma_f32_16x16x32_bf16 v[44:47], a[148:151], a[208:211], v[44:47]\n"
"v_mul_f32 v37, v37, v141\n"
"v_mfma_f32_16x16x32_bf16 v[48:51], a[152:155], a[204:207], v[48:51]\n"
"ds_read_b128 a[148:151], v5 offset:7168\n"
"v_exp_f32 v7, v7\n"
"v_mfma_f32_16x16x32_bf16 v[52:55], a[152:155], a[208:211], v[52:55]\n"
"v_exp_f32 v16, v16\n"
"v_mfma_f32_16x16x32_bf16 v[56:59], a[156:159], a[204:207], v[56:59]\n"
"buffer_load_dwordx4 a[36:39], v2, s[16:19], s62 offen offset:3072 sc1\n"
"v_mfma_f32_16x16x32_bf16 v[60:63], a[156:159], a[208:211], v[60:63]\n"
"s_waitcnt lgkmcnt(3)\n"
"v_mfma_f32_16x16x32_bf16 v[64:67], a[160:163], a[204:207], v[64:67]\n"
"v_exp_f32 v37, v37\n"
"v_add_f32 v7, 1.0, v7\n"
"v_mfma_f32_16x16x32_bf16 v[68:71], a[160:163], a[208:211], v[68:71]\n"
"s_waitcnt lgkmcnt(0)\n"
"s_barrier\n"
"v_mfma_f32_16x16x32_bf16 v[72:75], a[164:167], a[204:207], v[72:75]\n"
"ds_read_b128 a[152:155], v5 offset:45056\n"
"s_add_u32 s61, s61, 36864\n"
"v_mfma_f32_16x16x32_bf16 v[76:79], a[164:167], a[208:211], v[76:79]\n"
"ds_read_b128 a[156:159], v5 offset:46080\n"
"s_add_u32 m0, s60, 36864\n"
"v_mfma_f32_16x16x32_bf16 v[80:83], a[168:171], a[204:207], v[80:83]\n"
"ds_read_b128 a[160:163], v5 offset:47104\n"
"buffer_load_dwordx4 a[40:43], v3, s[16:19], s62 offen offset:0 sc1\n"
"v_add_f32 v16, 1.0, v16\n"
"v_mfma_f32_16x16x32_bf16 v[84:87], a[168:171], a[208:211], v[84:87]\n"
"ds_read_b128 a[164:167], v5 offset:48128\n"
"v_mfma_f32_16x16x32_bf16 v[88:91], a[172:175], a[204:207], v[88:91]\n"
"ds_read_b128 a[168:171], v5 offset:49152\n"
"buffer_load_dwordx4 a[44:47], v3, s[16:19], s62 offen offset:1024 sc1\n"
"v_mfma_f32_16x16x32_bf16 v[92:95], a[172:175], a[208:211], v[92:95]\n"
"v_add_f32 v37, 1.0, v37\n"
"v_mfma_f32_16x16x32_bf16 v[96:99], a[144:147], a[204:207], v[96:99]\n"
"ds_read_b128 a[172:175], v5 offset:50176\n"
"v_rcp_f32 v7, v7\n"
"v_mfma_f32_16x16x32_bf16 v[100:103], a[144:147], a[208:211], v[100:103]\n"
"v_rcp_f32 v16, v16\n"
"v_mfma_f32_16x16x32_bf16 v[104:107], a[148:151], a[204:207], v[104:107]\n"
"ds_read_b128 a[144:147], v5 offset:51200\n"
"buffer_load_dwordx4 a[48:51], v3, s[16:19], s62 offen offset:2048 sc1\n"
"v_mfma_f32_16x16x32_bf16 v[108:111], a[148:151], a[208:211], v[108:111]\n"
"s_waitcnt lgkmcnt(4)\n"
"v_mfma_f32_16x16x32_bf16 v[40:43], a[152:155], a[212:215], v[40:43]\n"
"ds_read_b128 a[148:151], v5 offset:52224\n"
"v_rcp_f32 v37, v37\n"
"v_mfma_f32_16x16x32_bf16 v[44:47], a[152:155], a[216:219], v[44:47]\n"
"v_mul_f32 v139, v139, v7\n"
"v_mul_f32 v140, v140, v16\n"
"v_mfma_f32_16x16x32_bf16 v[48:51], a[156:159], a[212:215], v[48:51]\n"
"ds_read_b128 a[152:155], v5 offset:53248\n"
"buffer_load_dwordx4 a[52:55], v3, s[16:19], s62 offen offset:3072 sc1\n"
"v_mfma_f32_16x16x32_bf16 v[52:55], a[156:159], a[216:219], v[52:55]\n"
"s_add_u32 s62, s62, 8192\n"
"v_mul_f32 v141, v141, v37\n"
"v_mfma_f32_16x16x32_bf16 v[56:59], a[160:163], a[212:215], v[56:59]\n"
"ds_read_b128 a[156:159], v5 offset:54272\n"
"v_mul_f32 v7, v142, v142\n"
"v_mul_f32 v16, v143, v143\n"
"v_mfma_f32_16x16x32_bf16 v[60:63], a[160:163], a[216:219], v[60:63]\n"
"v_mul_f32 v37, v144, v144\n"
"s_waitcnt lgkmcnt(4)\n"
"v_mfma_f32_16x16x32_bf16 v[64:67], a[164:167], a[212:215], v[64:67]\n"
"ds_read_b128 a[160:163], v5 offset:55296\n"
"buffer_load_dwordx4 a[56:59], v2, s[16:19], s62 offen offset:0 sc1\n"
"v_mfma_f32_16x16x32_bf16 v[68:71], a[164:167], a[216:219], v[68:71]\n"
"v_fma_f32 v7, v7, s80, v38\n"
"v_fma_f32 v16, v16, s80, v38\n"
"v_mfma_f32_16x16x32_bf16 v[72:75], a[168:171], a[212:215], v[72:75]\n"
"ds_read_b128 a[164:167], v5 offset:56320\n"
"v_fma_f32 v37, v37, s80, v38\n"
"v_mul_f32 v7, v7, v142\n"
"v_mfma_f32_16x16x32_bf16 v[76:79], a[168:171], a[216:219], v[76:79]\n"
"v_mul_f32 v16, v16, v143\n"
"v_mul_f32 v37, v37, v144\n"
"v_mfma_f32_16x16x32_bf16 v[80:83], a[172:175], a[212:215], v[80:83]\n"
"ds_read_b128 a[168:171], v5 offset:57344\n"
"v_mfma_f32_16x16x32_bf16 v[84:87], a[172:175], a[216:219], v[84:87]\n"
"buffer_load_dwordx4 a[60:63], v2, s[16:19], s62 offen offset:1024 sc1\n"
"s_waitcnt lgkmcnt(4)\n"
"v_mfma_f32_16x16x32_bf16 v[88:91], a[144:147], a[212:215], v[88:91]\n"
"ds_read_b128 a[172:175], v5 offset:58368\n"
"v_exp_f32 v7, v7\n"
"v_mfma_f32_16x16x32_bf16 v[92:95], a[144:147], a[216:219], v[92:95]\n"
"v_exp_f32 v16, v16\n"
"v_mfma_f32_16x16x32_bf16 v[96:99], a[148:151], a[212:215], v[96:99]\n"
"ds_read_b128 a[144:147], v5 offset:59392\n"
"v_mfma_f32_16x16x32_bf16 v[100:103], a[148:151], a[216:219], v[100:103]\n"
"buffer_load_dwordx4 v2, s[16:19], s61 offen offset:0 sc1 lds\n"
"v_mfma_f32_16x16x32_bf16 v[104:107], a[152:155], a[212:215], v[104:107]\n"
"ds_read_b128 a[148:151], v5 offset:60416\n"
"v_exp_f32 v37, v37\n"
"v_mfma_f32_16x16x32_bf16 v[108:111], a[152:155], a[216:219], v[108:111]\n"
"v_add_f32 v7, 1.0, v7\n"
"s_waitcnt lgkmcnt(4)\n"
"v_mfma_f32_16x16x32_bf16 v[40:43], a[156:159], a[220:223], v[40:43]\n"
"ds_read_b128 a[152:155], v5 offset:61440\n"
"v_add_f32 v16, 1.0, v16\n"
"v_mfma_f32_16x16x32_bf16 v[44:47], a[156:159], a[224:227], v[44:47]\n"
"buffer_load_dwordx4 v2, s[16:19], s61 offen offset:1024 sc1 lds\n"
"v_add_f32 v37, 1.0, v37\n"
"v_mfma_f32_16x16x32_bf16 v[48:51], a[160:163], a[220:223], v[48:51]\n"
"ds_read_b128 a[156:159], v5 offset:62464\n"
"v_rcp_f32 v7, v7\n"
"v_mfma_f32_16x16x32_bf16 v[52:55], a[160:163], a[224:227], v[52:55]\n"
"v_rcp_f32 v16, v16\n"
"v_mfma_f32_16x16x32_bf16 v[56:59], a[164:167], a[220:223], v[56:59]\n"
"ds_read_b128 a[160:163], v5 offset:63488\n"
"v_mfma_f32_16x16x32_bf16 v[60:63], a[164:167], a[224:227], v[60:63]\n"
"s_waitcnt lgkmcnt(4)\n"
"v_mfma_f32_16x16x32_bf16 v[64:67], a[168:171], a[220:223], v[64:67]\n"
"ds_read_b128 a[164:167], v5 offset:64512\n"
"buffer_load_dwordx4 v2, s[16:19], s61 offen offset:2048 sc1 lds\n"
"v_mfma_f32_16x16x32_bf16 v[68:71], a[168:171], a[224:227], v[68:71]\n"
"v_rcp_f32 v37, v37\n"
"v_mfma_f32_16x16x32_bf16 v[72:75], a[172:175], a[220:223], v[72:75]\n"
"ds_read_b128 a[168:171], v0 offset:0\n"
"buffer_load_dwordx2 v[196:197], v224, s[16:19], s69 offen sc1\n"
"v_mfma_f32_16x16x32_bf16 v[76:79], a[172:175], a[224:227], v[76:79]\n"
"buffer_load_dwordx2 v[198:199], v225, s[16:19], s69 offen sc1\n"
"buffer_load_dwordx2 v[212:213], v226, s[16:19], s69 offen sc1\n"
"v_mfma_f32_16x16x32_bf16 v[80:83], a[144:147], a[220:223], v[80:83]\n"
"ds_read_b128 a[172:175], v0 offset:1024\n"
"v_mul_f32 v142, v142, v7\n"
"v_mul_f32 v143, v143, v16\n"
"v_mfma_f32_16x16x32_bf16 v[84:87], a[144:147], a[224:227], v[84:87]\n"
"v_mul_f32 v144, v144, v37\n"
"s_waitcnt lgkmcnt(4)\n"
"v_mfma_f32_16x16x32_bf16 v[88:91], a[148:151], a[220:223], v[88:91]\n"
"ds_read_b128 a[144:147], v0 offset:2048\n"
"v_mul_f32 v7, v145, v145\n"
"v_mfma_f32_16x16x32_bf16 v[92:95], a[148:151], a[224:227], v[92:95]\n"
"v_mul_f32 v16, v146, v146\n"
"v_mul_f32 v37, v147, v147\n"
"v_mfma_f32_16x16x32_bf16 v[96:99], a[152:155], a[220:223], v[96:99]\n"
"ds_read_b128 a[148:151], v0 offset:3072\n"
"v_fma_f32 v7, v7, s80, v38\n"
"v_fma_f32 v16, v16, s80, v38\n"
"v_mfma_f32_16x16x32_bf16 v[100:103], a[152:155], a[224:227], v[100:103]\n"
"buffer_load_dwordx4 v2, s[16:19], s61 offen offset:3072 sc1 lds\n"
"s_add_u32 m0, s60, 40960\n"
"v_mfma_f32_16x16x32_bf16 v[104:107], a[156:159], a[220:223], v[104:107]\n"
"ds_read_b128 a[152:155], v0 offset:4096\n"
"v_fma_f32 v37, v37, s80, v38\n"
"v_mfma_f32_16x16x32_bf16 v[108:111], a[156:159], a[224:227], v[108:111]\n"
"v_mul_f32 v7, v7, v145\n"
"s_waitcnt vmcnt(46) lgkmcnt(4)\n"
"v_mfma_f32_16x16x32_bf16 v[40:43], a[160:163], a[228:231], v[40:43]\n"
"ds_read_b128 a[156:159], v0 offset:5120\n"
"v_mul_f32 v16, v16, v146\n"
"v_mul_f32 v37, v37, v147\n"
"v_mfma_f32_16x16x32_bf16 v[44:47], a[160:163], a[232:235], v[44:47]\n"
"buffer_load_dwordx4 v3, s[16:19], s61 offen offset:0 sc1 lds\n"
"v_mfma_f32_16x16x32_bf16 v[48:51], a[164:167], a[228:231], v[48:51]\n"
"ds_read_b128 a[160:163], v0 offset:6144\n"
"v_mfma_f32_16x16x32_bf16 v[52:55], a[164:167], a[232:235], v[52:55]\n"
"v_exp_f32 v7, v7\n"
"v_mfma_f32_16x16x32_bf16 v[56:59], a[168:171], a[228:231], v[56:59]\n"
"ds_read_b128 a[164:167], v0 offset:7168\n"
"v_exp_f32 v16, v16\n"
"v_mfma_f32_16x16x32_bf16 v[60:63], a[168:171], a[232:235], v[60:63]\n"
"buffer_load_dwordx4 v3, s[16:19], s61 offen offset:1024 sc1 lds\n"
"s_waitcnt lgkmcnt(4)\n"
"v_mfma_f32_16x16x32_bf16 v[64:67], a[172:175], a[228:231], v[64:67]\n"
"ds_read_b128 a[168:171], v0 offset:8192\n"
"v_mfma_f32_16x16x32_bf16 v[68:71], a[172:175], a[232:235], v[68:71]\n"
"v_exp_f32 v37, v37\n"
"v_mfma_f32_16x16x32_bf16 v[72:75], a[144:147], a[228:231], v[72:75]\n"
"ds_read_b128 a[172:175], v0 offset:9216\n"
"v_add_f32 v7, 1.0, v7\n"
"v_add_f32 v16, 1.0, v16\n"
"v_mfma_f32_16x16x32_bf16 v[76:79], a[144:147], a[232:235], v[76:79]\n"
"buffer_load_dwordx4 v3, s[16:19], s61 offen offset:2048 sc1 lds\n"
"v_add_f32 v37, 1.0, v37\n"
"v_mfma_f32_16x16x32_bf16 v[80:83], a[148:151], a[228:231], v[80:83]\n"
"ds_read_b128 a[144:147], v0 offset:10240\n"
"v_mfma_f32_16x16x32_bf16 v[84:87], a[148:151], a[232:235], v[84:87]\n"
"s_waitcnt lgkmcnt(4)\n"
"v_mfma_f32_16x16x32_bf16 v[88:91], a[152:155], a[228:231], v[88:91]\n"
"ds_read_b128 a[148:151], v0 offset:11264\n"
"v_rcp_f32 v7, v7\n"
"v_mfma_f32_16x16x32_bf16 v[92:95], a[152:155], a[232:235], v[92:95]\n"
"v_rcp_f32 v16, v16\n"
"v_mfma_f32_16x16x32_bf16 v[96:99], a[156:159], a[228:231], v[96:99]\n"
"ds_read_b128 a[152:155], v0 offset:12288\n"
"buffer_load_dwordx4 v3, s[16:19], s61 offen offset:3072 sc1 lds\n"
"v_mfma_f32_16x16x32_bf16 v[100:103], a[156:159], a[232:235], v[100:103]\n"
"s_add_u32 m0, s60, 45056\n"
"v_mfma_f32_16x16x32_bf16 v[104:107], a[160:163], a[228:231], v[104:107]\n"
"ds_read_b128 a[156:159], v0 offset:13312\n"
"v_rcp_f32 v37, v37\n"
"v_mfma_f32_16x16x32_bf16 v[108:111], a[160:163], a[232:235], v[108:111]\n"
"v_mul_f32 v145, v145, v7\n"
"s_waitcnt vmcnt(48) lgkmcnt(4)\n"
"v_mfma_f32_16x16x32_bf16 v[40:43], a[164:167], a[236:239], v[40:43]\n"
"ds_read_b128 a[160:163], v0 offset:14336\n"
"buffer_load_dwordx4 v4, s[16:19], s61 offen offset:0 sc1 lds\n"
"v_mfma_f32_16x16x32_bf16 v[44:47], a[164:167], a[240:243], v[44:47]\n"
"v_mul_f32 v146, v146, v16\n"
"v_mul_f32 v147, v147, v37\n"
"v_mfma_f32_16x16x32_bf16 v[48:51], a[168:171], a[236:239], v[48:51]\n"
"ds_read_b128 a[164:167], v0 offset:15360\n"
"v_mul_f32 v7, v148, v148\n"
"v_mul_f32 v16, v149, v149\n"
"v_mfma_f32_16x16x32_bf16 v[52:55], a[168:171], a[240:243], v[52:55]\n"
"v_mul_f32 v37, v150, v150\n"
"v_fma_f32 v7, v7, s80, v38\n"
"v_mfma_f32_16x16x32_bf16 v[56:59], a[172:175], a[236:239], v[56:59]\n"
"buffer_load_dwordx4 a[64:67], v2, s[16:19], s62 offen offset:2048 sc1\n"
"v_fma_f32 v16, v16, s80, v38\n"
"v_mfma_f32_16x16x32_bf16 v[60:63], a[172:175], a[240:243], v[60:63]\n"
"v_fma_f32 v37, v37, s80, v38\n"
"s_waitcnt lgkmcnt(3)\n"
"v_mfma_f32_16x16x32_bf16 v[64:67], a[144:147], a[236:239], v[64:67]\n"
"v_mul_f32 v7, v7, v148\n"
"v_mul_f32 v16, v16, v149\n"
"v_mul_f32 v37, v37, v150\n"
"v_mfma_f32_16x16x32_bf16 v[68:71], a[144:147], a[240:243], v[68:71]\n"
"s_waitcnt vmcnt(39)\n"
"s_waitcnt lgkmcnt(0)\n"
"s_barrier\n"
"v_mfma_f32_16x16x32_bf16 v[72:75], a[148:151], a[236:239], v[72:75]\n"
"ds_read_b128 a[168:171], v5 offset:8192\n"
"s_add_u32 s61, s61, 36864\n"
"v_mfma_f32_16x16x32_bf16 v[76:79], a[148:151], a[240:243], v[76:79]\n"
"ds_read_b128 a[172:175], v5 offset:9216\n"
"s_add_u32 m0, s60, 110592\n"
"v_mfma_f32_16x16x32_bf16 v[80:83], a[152:155], a[236:239], v[80:83]\n"
"ds_read_b128 a[144:147], v5 offset:10240\n"
"buffer_load_dwordx4 a[68:71], v2, s[16:19], s62 offen offset:3072 sc1\n"
"v_mfma_f32_16x16x32_bf16 v[84:87], a[152:155], a[240:243], v[84:87]\n"
"ds_read_b128 a[148:151], v5 offset:11264\n"
"v_mfma_f32_16x16x32_bf16 v[88:91], a[156:159], a[236:239], v[88:91]\n"
"ds_read_b128 a[152:155], v5 offset:12288\n"
"buffer_load_dwordx4 a[72:75], v3, s[16:19], s62 offen offset:0 sc1\n"
"v_mfma_f32_16x16x32_bf16 v[92:95], a[156:159], a[240:243], v[92:95]\n"
"v_exp_f32 v7, v7\n"
"v_mfma_f32_16x16x32_bf16 v[96:99], a[160:163], a[236:239], v[96:99]\n"
"ds_read_b128 a[156:159], v5 offset:13312\n"
"v_exp_f32 v16, v16\n"
"v_mfma_f32_16x16x32_bf16 v[100:103], a[160:163], a[240:243], v[100:103]\n"
"v_exp_f32 v37, v37\n"
"v_mfma_f32_16x16x32_bf16 v[104:107], a[164:167], a[236:239], v[104:107]\n"
"ds_read_b128 a[160:163], v5 offset:14336\n"
"v_add_f32 v7, 1.0, v7\n"
"v_mfma_f32_16x16x32_bf16 v[108:111], a[164:167], a[240:243], v[108:111]\n"
"buffer_load_dwordx4 a[76:79], v3, s[16:19], s62 offen offset:1024 sc1\n"
"s_waitcnt lgkmcnt(4)\n"
"v_mfma_f32_16x16x32_bf16 v[40:43], a[168:171], a[244:247], v[40:43]\n"
"ds_read_b128 a[164:167], v5 offset:15360\n"
"v_add_f32 v16, 1.0, v16\n"
"v_add_f32 v37, 1.0, v37\n"
"v_mfma_f32_16x16x32_bf16 v[44:47], a[168:171], a[248:251], v[44:47]\n"
"v_rcp_f32 v7, v7\n"
"v_mfma_f32_16x16x32_bf16 v[48:51], a[172:175], a[244:247], v[48:51]\n"
"ds_read_b128 a[168:171], v5 offset:16384\n"
"v_mfma_f32_16x16x32_bf16 v[52:55], a[172:175], a[248:251], v[52:55]\n"
"buffer_load_dwordx4 a[80:83], v3, s[16:19], s62 offen offset:2048 sc1\n"
"v_mfma_f32_16x16x32_bf16 v[56:59], a[144:147], a[244:247], v[56:59]\n"
"ds_read_b128 a[172:175], v5 offset:17408\n"
"v_rcp_f32 v16, v16\n"
"v_mfma_f32_16x16x32_bf16 v[60:63], a[144:147], a[248:251], v[60:63]\n"
"s_waitcnt lgkmcnt(4)\n"
"v_mfma_f32_16x16x32_bf16 v[64:67], a[148:151], a[244:247], v[64:67]\n"
"ds_read_b128 a[144:147], v5 offset:18432\n"
"v_mfma_f32_16x16x32_bf16 v[68:71], a[148:151], a[248:251], v[68:71]\n"
"buffer_load_dwordx4 a[84:87], v3, s[16:19], s62 offen offset:3072 sc1\n"
"s_add_u32 s62, s62, 8192\n"
"v_mfma_f32_16x16x32_bf16 v[72:75], a[152:155], a[244:247], v[72:75]\n"
"ds_read_b128 a[148:151], v5 offset:19456\n"
"v_rcp_f32 v37, v37\n"
"v_mfma_f32_16x16x32_bf16 v[76:79], a[152:155], a[248:251], v[76:79]\n"
"v_mul_f32 v148, v148, v7\n"
"v_mul_f32 v149, v149, v16\n"
"v_mfma_f32_16x16x32_bf16 v[80:83], a[156:159], a[244:247], v[80:83]\n"
"ds_read_b128 a[152:155], v5 offset:20480\n"
"v_mul_f32 v150, v150, v37\n"
"v_mfma_f32_16x16x32_bf16 v[84:87], a[156:159], a[248:251], v[84:87]\n"
"v_mul_f32 v7, v151, v151\n"
"s_waitcnt lgkmcnt(4)\n"
"v_mfma_f32_16x16x32_bf16 v[88:91], a[160:163], a[244:247], v[88:91]\n"
"ds_read_b128 a[156:159], v5 offset:21504\n"
"buffer_load_dwordx4 a[88:91], v2, s[16:19], s62 offen offset:0 sc1\n"
"v_mul_f32 v16, v152, v152\n"
"v_mfma_f32_16x16x32_bf16 v[92:95], a[160:163], a[248:251], v[92:95]\n"
"v_mul_f32 v37, v153, v153\n"
"v_fma_f32 v7, v7, s80, v38\n"
"v_mfma_f32_16x16x32_bf16 v[96:99], a[164:167], a[244:247], v[96:99]\n"
"ds_read_b128 a[160:163], v5 offset:22528\n"
"v_fma_f32 v16, v16, s80, v38\n"
"v_mfma_f32_16x16x32_bf16 v[100:103], a[164:167], a[248:251], v[100:103]\n"
"s_waitcnt vmcnt(13)\n"
"v_bitop3_b32 v192, v197, v199, v213 bitop3:0x80\n"
"v_or3_b32 v193, v197, v199, v213\n"
"v_mfma_f32_16x16x32_bf16 v[104:107], a[168:171], a[244:247], v[104:107]\n"
"ds_read_b128 a[164:167], v5 offset:23552\n"
"buffer_load_dwordx4 a[92:95], v2, s[16:19], s62 offen offset:1024 sc1\n"
"v_and_b32 v192, v192, v17\n"
"v_mfma_f32_16x16x32_bf16 v[108:111], a[168:171], a[248:251], v[108:111]\n"
"v_or_b32 v193, v193, v17\n"
"s_waitcnt lgkmcnt(4)\n"
"v_mfma_f32_16x16x32_bf16 v[40:43], a[172:175], a[0:3], v[40:43]\n"
"ds_read_b128 a[168:171], v5 offset:24576\n"
"v_fma_f32 v37, v37, s80, v38\n"
"v_mfma_f32_16x16x32_bf16 v[44:47], a[172:175], a[4:7], v[44:47]\n"
"v_cmp_ne_u32 vcc, v192, v193\n"
"s_cbranch_vccnz .Lmk_SLOW_5\n"
".Lmk_BACK_5:\n"
"v_mfma_f32_16x16x32_bf16 v[48:51], a[144:147], a[0:3], v[48:51]\n"
"ds_read_b128 a[172:175], v5 offset:25600\n"
"buffer_load_dwordx4 v2, s[16:19], s61 offen offset:0 sc1 lds\n"
"ds_write_b32 v228, v196\n"
"v_mfma_f32_16x16x32_bf16 v[52:55], a[144:147], a[4:7], v[52:55]\n"
"ds_write_b32 v229, v198\n"
"ds_write_b32 v230, v212\n"
"v_mfma_f32_16x16x32_bf16 v[56:59], a[148:151], a[0:3], v[56:59]\n"
".Lmk_PCHERE_6:\n"
"s_add_u32 s0, s0, .Lmk_PCHERE_6-.Lmk_PCHERE_5\n"
"s_addc_u32 s1, s1, 0\n"
"s_sub_u32 s2, s2, .Lmk_PCHERE_6-.Lmk_PCHERE_5\n"
"s_max_i32 s2, s2, 0\n"
"buffer_load_dword a255, off, s[0:3], 0\n"
"ds_read_b128 a[144:147], v5 offset:26624\n"
"v_and_b32 v192, 15, v1\n"
"v_mfma_f32_16x16x32_bf16 v[60:63], a[148:151], a[4:7], v[60:63]\n"
"v_lshrrev_b32 v193, 4, v1\n"
"s_waitcnt lgkmcnt(7)\n"
"v_mfma_f32_16x16x32_bf16 v[64:67], a[152:155], a[0:3], v[64:67]\n"
"ds_read_b128 a[148:151], v5 offset:27648\n"
"buffer_load_dwordx4 v2, s[16:19], s61 offen offset:1024 sc1 lds\n"
"v_lshrrev_b32 v194, 2, v192\n"
"v_mfma_f32_16x16x32_bf16 v[68:71], a[152:155], a[4:7], v[68:71]\n"
"v_cmp_eq_u32 s[84:85], v194, v193\n"
"v_and_b32 v194, 1, v193\n"
"v_mfma_f32_16x16x32_bf16 v[72:75], a[156:159], a[0:3], v[72:75]\n"
"ds_read_b128 a[152:155], v5 offset:28672\n"
"v_mul_u32_u24 v195, 1600, v194\n"
"v_mfma_f32_16x16x32_bf16 v[76:79], a[156:159], a[4:7], v[76:79]\n"
"v_and_b32 v196, 3, v192\n"
"v_mul_u32_u24 v196, 80, v196\n"
"v_mfma_f32_16x16x32_bf16 v[80:83], a[160:163], a[0:3], v[80:83]\n"
"ds_read_b128 a[156:159], v5 offset:29696\n"
"buffer_load_dwordx4 v2, s[16:19], s61 offen offset:2048 sc1 lds\n"
"v_add_u32 v196, v196, v195\n"
"v_mfma_f32_16x16x32_bf16 v[84:87], a[160:163], a[4:7], v[84:87]\n"
"v_add_u32 v196, 159104, v196\n"
"s_waitcnt lgkmcnt(7)\n"
"v_mfma_f32_16x16x32_bf16 v[88:91], a[164:167], a[0:3], v[88:91]\n"
"ds_read_b128 a[160:163], v5 offset:30720\n"
"v_mov_b32 v197, 157696\n"
"v_mfma_f32_16x16x32_bf16 v[92:95], a[164:167], a[4:7], v[92:95]\n"
"v_cndmask_b32 v18, v197, v196, s[84:85]\n"
"v_lshrrev_b32 v193, 1, v193\n"
"v_mfma_f32_16x16x32_bf16 v[96:99], a[168:171], a[0:3], v[96:99]\n"
"ds_read_b128 a[164:167], v5 offset:31744\n"
"v_lshlrev_b32 v193, 10, v193\n"
"v_lshl_add_u32 v192, v192, 4, v193\n"
"v_mfma_f32_16x16x32_bf16 v[100:103], a[168:171], a[4:7], v[100:103]\n"
"buffer_load_dwordx4 v2, s[16:19], s61 offen offset:3072 sc1 lds\n"
"s_add_u32 m0, s60, 114688\n"
"v_mfma_f32_16x16x32_bf16 v[104:107], a[172:175], a[0:3], v[104:107]\n"
"ds_read_b128 a[168:171], v5 offset:32768\n"
"v_lshl_add_u32 v19, v194, 9, v192\n"
"v_mfma_f32_16x16x32_bf16 v[108:111], a[172:175], a[4:7], v[108:111]\n"
"v_lshl_add_u32 v20, v194, 8, v192\n"
"s_waitcnt lgkmcnt(4)\n"
"v_mfma_f32_16x16x32_bf16 v[40:43], a[144:147], a[8:11], v[40:43]\n"
"ds_read_b128 a[172:175], v5 offset:33792\n"
"s_lshl_b32 s66, s63, 4\n"
"s_add_u32 s67, s66, s53\n"
"v_mfma_f32_16x16x32_bf16 v[44:47], a[144:147], a[12:15], v[44:47]\n"
"buffer_load_dwordx4 v3, s[16:19], s61 offen offset:0 sc1 lds\n"
"s_andn2_b32 s66, s67, 3\n"
"v_mfma_f32_16x16x32_bf16 v[48:51], a[148:151], a[8:11], v[48:51]\n"
"ds_read_b128 a[144:147], v5 offset:34816\n"
"s_add_u32 s89, s57, 0\n"
"v_mfma_f32_16x16x32_bf16 v[52:55], a[148:151], a[12:15], v[52:55]\n"
"s_and_b32 s89, s89, 3\n"
"s_add_u32 s89, s89, s66\n"
"v_mfma_f32_16x16x32_bf16 v[56:59], a[152:155], a[8:11], v[56:59]\n"
"ds_read_b128 a[148:151], v5 offset:35840\n"
"s_mul_i32 s89, s89, 12288\n"
"s_add_u32 s89, s89, 27262976\n"
"v_mfma_f32_16x16x32_bf16 v[60:63], a[152:155], a[12:15], v[60:63]\n"
"buffer_load_dwordx4 v3, s[16:19], s61 offen offset:1024 sc1 lds\n"
"s_waitcnt lgkmcnt(4)\n"
"v_mfma_f32_16x16x32_bf16 v[64:67], a[156:159], a[8:11], v[64:67]\n"
"ds_read_b128 a[152:155], v5 offset:36864\n"
"s_add_u32 s90, s57, 1\n"
"v_mfma_f32_16x16x32_bf16 v[68:71], a[156:159], a[12:15], v[68:71]\n"
"s_and_b32 s90, s90, 3\n"
"s_add_u32 s90, s90, s66\n"
"v_mfma_f32_16x16x32_bf16 v[72:75], a[160:163], a[8:11], v[72:75]\n"
"ds_read_b128 a[156:159], v5 offset:37888\n"
"s_mul_i32 s90, s90, 12288\n"
"s_add_u32 s90, s90, 27262976\n"
"v_mfma_f32_16x16x32_bf16 v[76:79], a[160:163], a[12:15], v[76:79]\n"
"buffer_load_dwordx4 v3, s[16:19], s61 offen offset:2048 sc1 lds\n"
"s_add_u32 s91, s57, 2\n"
"v_mfma_f32_16x16x32_bf16 v[80:83], a[164:167], a[8:11], v[80:83]\n"
"ds_read_b128 a[160:163], v5 offset:38912\n"
"s_and_b32 s91, s91, 3\n"
"v_mfma_f32_16x16x32_bf16 v[84:87], a[164:167], a[12:15], v[84:87]\n"
"s_add_u32 s91, s91, s66\n"
"s_waitcnt lgkmcnt(4)\n"
"v_mfma_f32_16x16x32_bf16 v[88:91], a[168:171], a[8:11], v[88:91]\n"
"ds_read_b128 a[164:167], v5 offset:39936\n"
"s_mul_i32 s91, s91, 12288\n"
"s_add_u32 s91, s91, 27262976\n"
"v_mfma_f32_16x16x32_bf16 v[92:95], a[168:171], a[12:15], v[92:95]\n"
"s_add_u32 s92, s57, 3\n"
"buffer_load_dwordx4 v3, s[16:19], s61 offen offset:3072 sc1 lds\n"
"v_mfma_f32_16x16x32_bf16 v[96:99], a[172:175], a[8:11], v[96:99]\n"
"ds_read_b128 a[168:171], v5 offset:40960\n"
"s_add_u32 m0, s60, 118784\n"
"v_mfma_f32_16x16x32_bf16 v[100:103], a[172:175], a[12:15], v[100:103]\n"
"s_and_b32 s92, s92, 3\n"
"s_add_u32 s92, s92, s66\n"
"v_mfma_f32_16x16x32_bf16 v[104:107], a[144:147], a[8:11], v[104:107]\n"
"ds_read_b128 a[172:175], v5 offset:41984\n"
"s_mul_i32 s92, s92, 12288\n"
"s_add_u32 s92, s92, 27262976\n"
"v_mfma_f32_16x16x32_bf16 v[108:111], a[144:147], a[12:15], v[108:111]\n"
"s_lshl_b32 s66, s66, 7\n"
"s_waitcnt lgkmcnt(4)\n"
"v_mfma_f32_16x16x32_bf16 v[40:43], a[148:151], a[16:19], v[40:43]\n"
"ds_read_b128 a[144:147], v5 offset:43008\n"
"buffer_load_dwordx4 v4, s[16:19], s61 offen offset:0 sc1 lds\n"
"v_mfma_f32_16x16x32_bf16 v[44:47], a[148:151], a[20:23], v[44:47]\n"
"s_lshl_b32 s67, s67, 7\n"
"s_add_u32 s69, s89, 8192\n"
"v_mfma_f32_16x16x32_bf16 v[48:51], a[152:155], a[16:19], v[48:51]\n"
"ds_read_b128 a[148:151], v5 offset:44032\n"
"v_mul_f32 v7, v7, v151\n"
"v_mul_f32 v16, v16, v152\n"
"v_mfma_f32_16x16x32_bf16 v[52:55], a[152:155], a[20:23], v[52:55]\n"
"v_mul_f32 v37, v37, v153\n"
"v_mfma_f32_16x16x32_bf16 v[56:59], a[156:159], a[16:19], v[56:59]\n"
"buffer_load_dwordx4 a[96:99], v2, s[16:19], s62 offen offset:2048 sc1\n"
"v_mfma_f32_16x16x32_bf16 v[60:63], a[156:159], a[20:23], v[60:63]\n"
"s_waitcnt lgkmcnt(3)\n"
"v_mfma_f32_16x16x32_bf16 v[64:67], a[160:163], a[16:19], v[64:67]\n"
"v_exp_f32 v7, v7\n"
"v_mfma_f32_16x16x32_bf16 v[68:71], a[160:163], a[20:23], v[68:71]\n"
"s_waitcnt lgkmcnt(0)\n"
"s_barrier\n"
"v_mfma_f32_16x16x32_bf16 v[72:75], a[164:167], a[16:19], v[72:75]\n"
"ds_read_b128 a[152:155], v2 offset:0\n"
"s_add_u32 s61, s61, 36864\n"
"v_mfma_f32_16x16x32_bf16 v[76:79], a[164:167], a[20:23], v[76:79]\n"
"ds_read_b128 a[156:159], v2 offset:1024\n"
"s_add_u32 m0, s60, 73728\n"
"v_mfma_f32_16x16x32_bf16 v[80:83], a[168:171], a[16:19], v[80:83]\n"
"ds_read_b128 a[160:163], v2 offset:2048\n"
"buffer_load_dwordx4 a[100:103], v2, s[16:19], s62 offen offset:3072 sc1\n"
"ds_read_b128 v[184:187], v18 offset:0\n"
"v_mfma_f32_16x16x32_bf16 v[84:87], a[168:171], a[20:23], v[84:87]\n"
"ds_read_b128 a[164:167], v2 offset:3072\n"
"v_mfma_f32_16x16x32_bf16 v[88:91], a[172:175], a[16:19], v[88:91]\n"
"ds_read_b128 a[168:171], v2 offset:4096\n"
"buffer_load_dwordx4 a[104:107], v3, s[16:19], s62 offen offset:0 sc1\n"
"v_mfma_f32_16x16x32_bf16 v[92:95], a[172:175], a[20:23], v[92:95]\n"
"ds_read_b128 v[188:191], v18 offset:16\n"
"ds_read_b128 v[200:203], v18 offset:32\n"
"v_mfma_f32_16x16x32_bf16 v[96:99], a[144:147], a[16:19], v[96:99]\n"
"ds_read_b128 a[172:175], v2 offset:5120\n"
"ds_read_b128 v[204:207], v18 offset:48\n"
"ds_read_b128 v[216:219], v18 offset:64\n"
"v_mfma_f32_16x16x32_bf16 v[100:103], a[144:147], a[20:23], v[100:103]\n"
"v_exp_f32 v16, v16\n"
"v_mfma_f32_16x16x32_bf16 v[104:107], a[148:151], a[16:19], v[104:107]\n"
"ds_read_b128 a[144:147], v2 offset:6144\n"
"v_mfma_f32_16x16x32_bf16 v[108:111], a[148:151], a[20:23], v[108:111]\n"
"buffer_load_dwordx4 a[108:111], v3, s[16:19], s62 offen offset:1024 sc1\n"
"s_waitcnt lgkmcnt(9)\n"
"v_mfma_f32_16x16x32_bf16 v[40:43], a[152:155], a[24:27], v[40:43]\n"
"ds_read_b128 a[148:151], v2 offset:7168\n"
"v_exp_f32 v37, v37\n"
"s_waitcnt lgkmcnt(9)\n"
"v_mfma_f32_16x16x32_bf16 v[208:211], v[184:187], v[8:11], 0\n"
"v_add_f32 v7, 1.0, v7\n"
"v_add_f32 v16, 1.0, v16\n"
"s_waitcnt lgkmcnt(6)\n"
"v_mfma_f32_16x16x32_bf16 v[208:211], v[188:191], v[12:15], v[208:211]\n"
"v_add_f32 v37, 1.0, v37\n"
"s_waitcnt lgkmcnt(5)\n"
"v_mfma_f32_16x16x32_bf16 v[208:211], v[200:203], v[22:25], v[208:211]\n"
"buffer_load_dwordx4 a[112:115], v3, s[16:19], s62 offen offset:2048 sc1\n"
"s_waitcnt lgkmcnt(3)\n"
"v_mfma_f32_16x16x32_bf16 v[208:211], v[204:207], v[26:29], v[208:211]\n"
"v_rcp_f32 v7, v7\n"
"s_waitcnt lgkmcnt(2)\n"
"v_mfma_f32_16x16x32_bf16 v[208:211], v[216:219], v[30:33], v[208:211]\n"
"ds_read_b128 v[184:187], v18 offset:320\n"
"ds_read_b128 v[188:191], v18 offset:336\n"
"v_mfma_f32_16x16x32_bf16 v[44:47], a[152:155], a[28:31], v[44:47]\n"
"ds_read_b128 v[200:203], v18 offset:352\n"
"ds_read_b128 v[204:207], v18 offset:368\n"
"v_mfma_f32_16x16x32_bf16 v[48:51], a[156:159], a[24:27], v[48:51]\n"
"ds_read_b128 a[152:155], v2 offset:8192\n"
"buffer_load_dwordx4 a[116:119], v3, s[16:19], s62 offen offset:3072 sc1\n"
"v_mfma_f32_16x16x32_bf16 v[52:55], a[156:159], a[28:31], v[52:55]\n"
"s_add_u32 s62, s62, 8192\n"
"ds_read_b128 v[216:219], v18 offset:384\n"
"v_mfma_f32_16x16x32_bf16 v[56:59], a[160:163], a[24:27], v[56:59]\n"
"ds_read_b128 a[156:159], v2 offset:9216\n"
"v_mfma_f32_16x16x32_bf16 v[60:63], a[160:163], a[28:31], v[60:63]\n"
"v_mfma_f32_16x16x32_bf16 v[64:67], a[164:167], a[24:27], v[64:67]\n"
"ds_read_b128 a[160:163], v2 offset:10240\n"
"v_mfma_f32_16x16x32_bf16 v[68:71], a[164:167], a[28:31], v[68:71]\n"
"buffer_load_dwordx4 a[120:123], v2, s[16:19], s62 offen offset:0 sc1\n"
"v_rcp_f32 v16, v16\n"
"s_waitcnt lgkmcnt(7)\n"
"v_mfma_f32_16x16x32_bf16 v[240:243], v[184:187], v[8:11], 0\n"
"v_rcp_f32 v37, v37\n"
"s_waitcnt lgkmcnt(6)\n"
"v_mfma_f32_16x16x32_bf16 v[240:243], v[188:191], v[12:15], v[240:243]\n"
"v_mul_f32 v151, v151, v7\n"
"v_mul_f32 v152, v152, v16\n"
"s_waitcnt lgkmcnt(5)\n"
"v_mfma_f32_16x16x32_bf16 v[240:243], v[200:203], v[22:25], v[240:243]\n"
"v_mul_f32 v153, v153, v37\n"
"v_mul_f32 v7, v154, v154\n"
"s_waitcnt lgkmcnt(4)\n"
"v_mfma_f32_16x16x32_bf16 v[240:243], v[204:207], v[26:29], v[240:243]\n"
"buffer_load_dwordx4 a[124:127], v2, s[16:19], s62 offen offset:1024 sc1\n"
"v_mul_f32 v16, v155, v155\n"
"s_waitcnt lgkmcnt(2)\n"
"v_mfma_f32_16x16x32_bf16 v[240:243], v[216:219], v[30:33], v[240:243]\n"
"ds_read_b128 v[184:187], v18 offset:640\n"
"ds_read_b128 v[188:191], v18 offset:656\n"
"v_mfma_f32_16x16x32_bf16 v[72:75], a[168:171], a[24:27], v[72:75]\n"
"ds_read_b128 a[164:167], v2 offset:11264\n"
"ds_read_b128 v[200:203], v18 offset:672\n"
"v_mfma_f32_16x16x32_bf16 v[76:79], a[168:171], a[28:31], v[76:79]\n"
"ds_read_b128 v[204:207], v18 offset:688\n"
"ds_read_b128 v[216:219], v18 offset:704\n"
"v_mfma_f32_16x16x32_bf16 v[80:83], a[172:175], a[24:27], v[80:83]\n"
"ds_read_b128 a[168:171], v2 offset:12288\n"
"v_mul_f32 v37, v156, v156\n"
"v_fma_f32 v7, v7, s80, v38\n"
"v_mfma_f32_16x16x32_bf16 v[84:87], a[172:175], a[28:31], v[84:87]\n"
"buffer_load_dwordx4 v2, s[16:19], s61 offen offset:0 sc1 lds\n"
"v_mfma_f32_16x16x32_bf16 v[88:91], a[144:147], a[24:27], v[88:91]\n"
"ds_read_b128 a[172:175], v2 offset:13312\n"
"v_fma_f32 v16, v16, s80, v38\n"
"v_mfma_f32_16x16x32_bf16 v[92:95], a[144:147], a[28:31], v[92:95]\n"
"v_fma_f32 v37, v37, s80, v38\n"
"v_mul_f32 v7, v7, v154\n"
"s_waitcnt lgkmcnt(7)\n"
"v_mfma_f32_16x16x32_bf16 v[244:247], v[184:187], v[8:11], 0\n"
"v_mul_f32 v16, v16, v155\n"
"v_mul_f32 v37, v37, v156\n"
"s_waitcnt lgkmcnt(6)\n"
"v_mfma_f32_16x16x32_bf16 v[244:247], v[188:191], v[12:15], v[244:247]\n"
"buffer_load_dwordx4 v2, s[16:19], s61 offen offset:1024 sc1 lds\n"
"s_waitcnt lgkmcnt(4)\n"
"v_mfma_f32_16x16x32_bf16 v[244:247], v[200:203], v[22:25], v[244:247]\n"
"v_exp_f32 v7, v7\n"
"s_waitcnt lgkmcnt(3)\n"
"v_mfma_f32_16x16x32_bf16 v[244:247], v[204:207], v[26:29], v[244:247]\n"
"v_exp_f32 v16, v16\n"
"s_waitcnt lgkmcnt(2)\n"
"v_mfma_f32_16x16x32_bf16 v[244:247], v[216:219], v[30:33], v[244:247]\n"
"ds_read_b128 v[184:187], v18 offset:960\n"
"ds_read_b128 v[188:191], v18 offset:976\n"
"v_mfma_f32_16x16x32_bf16 v[96:99], a[148:151], a[24:27], v[96:99]\n"
"ds_read_b128 a[144:147], v2 offset:14336\n"
"buffer_load_dwordx4 v2, s[16:19], s61 offen offset:2048 sc1 lds\n"
"v_mfma_f32_16x16x32_bf16 v[100:103], a[148:151], a[28:31], v[100:103]\n"
"ds_read_b128 v[200:203], v18 offset:992\n"
"ds_read_b128 v[204:207], v18 offset:1008\n"
"v_mfma_f32_16x16x32_bf16 v[104:107], a[152:155], a[24:27], v[104:107]\n"
"ds_read_b128 a[148:151], v2 offset:15360\n"
"ds_read_b128 v[216:219], v18 offset:1024\n"
"v_mfma_f32_16x16x32_bf16 v[108:111], a[152:155], a[28:31], v[108:111]\n"
"v_exp_f32 v37, v37\n"
"v_mfma_f32_16x16x32_bf16 v[40:43], a[156:159], a[32:35], v[40:43]\n"
"ds_read_b128 a[152:155], v2 offset:16384\n"
"v_add_f32 v7, 1.0, v7\n"
"v_mfma_f32_16x16x32_bf16 v[44:47], a[156:159], a[36:39], v[44:47]\n"
"buffer_load_dwordx4 v2, s[16:19], s61 offen offset:3072 sc1 lds\n"
"s_add_u32 m0, s60, 77824\n"
"v_mfma_f32_16x16x32_bf16 v[48:51], a[160:163], a[32:35], v[48:51]\n"
"ds_read_b128 a[156:159], v2 offset:17408\n"
"v_add_f32 v16, 1.0, v16\n"
"s_waitcnt lgkmcnt(8)\n"
"v_mfma_f32_16x16x32_bf16 v[248:251], v[184:187], v[8:11], 0\n"
"v_add_f32 v37, 1.0, v37\n"
"s_waitcnt lgkmcnt(7)\n"
"v_mfma_f32_16x16x32_bf16 v[248:251], v[188:191], v[12:15], v[248:251]\n"
"v_rcp_f32 v7, v7\n"
"s_waitcnt lgkmcnt(5)\n"
"v_mfma_f32_16x16x32_bf16 v[248:251], v[200:203], v[22:25], v[248:251]\n"
"buffer_load_dwordx4 v3, s[16:19], s61 offen offset:0 sc1 lds\n"
"s_waitcnt lgkmcnt(4)\n"
"v_mfma_f32_16x16x32_bf16 v[248:251], v[204:207], v[26:29], v[248:251]\n"
"v_rcp_f32 v16, v16\n"
"s_waitcnt lgkmcnt(2)\n"
"v_mfma_f32_16x16x32_bf16 v[248:251], v[216:219], v[30:33], v[248:251]\n"
"ds_read_b128 v[184:187], v18 offset:1280\n"
"ds_read_b128 v[188:191], v18 offset:1296\n"
"v_mfma_f32_16x16x32_bf16 v[52:55], a[160:163], a[36:39], v[52:55]\n"
"ds_read_b128 v[200:203], v18 offset:1312\n"
"ds_read_b128 v[204:207], v18 offset:1328\n"
"v_mfma_f32_16x16x32_bf16 v[56:59], a[164:167], a[32:35], v[56:59]\n"
"ds_read_b128 a[160:163], v2 offset:18432\n"
"ds_read_b128 v[216:219], v18 offset:1344\n"
"v_mfma_f32_16x16x32_bf16 v[60:63], a[164:167], a[36:39], v[60:63]\n"
"buffer_load_dwordx4 v3, s[16:19], s61 offen offset:1024 sc1 lds\n"
"v_mfma_f32_16x16x32_bf16 v[64:67], a[168:171], a[32:35], v[64:67]\n"
"ds_read_b128 a[164:167], v2 offset:19456\n"
"v_rcp_f32 v37, v37\n"
"v_mfma_f32_16x16x32_bf16 v[68:71], a[168:171], a[36:39], v[68:71]\n"
"v_mul_f32 v154, v154, v7\n"
"v_mul_f32 v155, v155, v16\n"
"v_mfma_f32_16x16x32_bf16 v[72:75], a[172:175], a[32:35], v[72:75]\n"
"ds_read_b128 a[168:171], v2 offset:20480\n"
"v_mul_f32 v156, v156, v37\n"
"s_waitcnt lgkmcnt(7)\n"
"v_mfma_f32_16x16x32_bf16 v[252:255], v[184:187], v[8:11], 0\n"
"buffer_load_dwordx4 v3, s[16:19], s61 offen offset:2048 sc1 lds\n"
"v_mul_f32 v7, v157, v157\n"
"s_waitcnt lgkmcnt(6)\n"
"v_mfma_f32_16x16x32_bf16 v[252:255], v[188:191], v[12:15], v[252:255]\n"
"v_mul_f32 v16, v158, v158\n"
"v_mul_f32 v37, v159, v159\n"
"s_waitcnt lgkmcnt(5)\n"
"v_mfma_f32_16x16x32_bf16 v[252:255], v[200:203], v[22:25], v[252:255]\n"
"v_fma_f32 v7, v7, s80, v38\n"
"v_fma_f32 v16, v16, s80, v38\n"
"s_waitcnt lgkmcnt(4)\n"
"v_mfma_f32_16x16x32_bf16 v[252:255], v[204:207], v[26:29], v[252:255]\n"
"v_fma_f32 v37, v37, s80, v38\n"
"v_mul_f32 v7, v7, v157\n"
"s_waitcnt lgkmcnt(2)\n"
"v_mfma_f32_16x16x32_bf16 v[252:255], v[216:219], v[30:33], v[252:255]\n"
"v_mul_f32 v16, v16, v158\n"
"v_mul_f32 v37, v37, v159\n"
"v_mfma_f32_16x16x32_bf16 v[76:79], a[172:175], a[36:39], v[76:79]\n"
"buffer_load_dwordx4 v3, s[16:19], s61 offen offset:3072 sc1 lds\n"
"s_add_u32 m0, s60, 81920\n"
"v_mfma_f32_16x16x32_bf16 v[80:83], a[144:147], a[32:35], v[80:83]\n"
"ds_read_b128 a[172:175], v2 offset:21504\n"
"v_mfma_f32_16x16x32_bf16 v[84:87], a[144:147], a[36:39], v[84:87]\n"
"v_mfma_f32_16x16x32_bf16 v[88:91], a[148:151], a[32:35], v[88:91]\n"
"ds_read_b128 a[144:147], v2 offset:22528\n"
"v_add_f32 v254, 0x358637bd, v254\n"
"v_mfma_f32_16x16x32_bf16 v[92:95], a[148:151], a[36:39], v[92:95]\n"
"buffer_load_dwordx4 v4, s[16:19], s61 offen offset:0 sc1 lds\n"
"v_rcp_f32 v254, v254\n"
"v_mfma_f32_16x16x32_bf16 v[96:99], a[152:155], a[32:35], v[96:99]\n"
"ds_read_b128 a[148:151], v2 offset:23552\n"
"v_mov_b32 v213, 0\n"
"v_mfma_f32_16x16x32_bf16 v[100:103], a[152:155], a[36:39], v[100:103]\n"
"v_mov_b32 v214, 0\n"
"v_mov_b32 v215, 0\n"
"v_mfma_f32_16x16x32_bf16 v[104:107], a[156:159], a[32:35], v[104:107]\n"
"ds_read_b128 a[152:155], v2 offset:24576\n"
"v_mul_f32 v208, v208, v254\n"
"v_mfma_f32_16x16x32_bf16 v[108:111], a[156:159], a[36:39], v[108:111]\n"
"buffer_load_dwordx4 a[128:131], v2, s[16:19], s62 offen offset:2048 sc1\n"
"v_mul_f32 v209, v209, v254\n"
"s_waitcnt lgkmcnt(4)\n"
"v_mfma_f32_16x16x32_bf16 v[40:43], a[160:163], a[40:43], v[40:43]\n"
"ds_read_b128 a[156:159], v2 offset:25600\n"
"v_mul_f32 v210, v210, v254\n"
"v_mfma_f32_16x16x32_bf16 v[44:47], a[160:163], a[44:47], v[44:47]\n"
"v_mul_f32 v211, v211, v254\n"
"v_mul_f32 v240, v240, v254\n"
"v_mfma_f32_16x16x32_bf16 v[48:51], a[164:167], a[40:43], v[48:51]\n"
"ds_read_b128 a[160:163], v2 offset:26624\n"
"v_mul_f32 v241, v241, v254\n"
"v_mfma_f32_16x16x32_bf16 v[52:55], a[164:167], a[44:47], v[52:55]\n"
"v_mul_f32 v242, v242, v254\n"
"v_mul_f32 v243, v243, v254\n"
"v_mul_f32 v244, v244, v254\n"
"v_mfma_f32_16x16x32_bf16 v[56:59], a[168:171], a[40:43], v[56:59]\n"
"ds_read_b128 a[164:167], v2 offset:27648\n"
"buffer_load_dwordx4 a[132:135], v2, s[16:19], s62 offen offset:3072 sc1\n"
"v_mfma_f32_16x16x32_bf16 v[60:63], a[168:171], a[44:47], v[60:63]\n"
"v_mul_f32 v245, v245, v254\n"
"s_waitcnt lgkmcnt(4)\n"
"v_mfma_f32_16x16x32_bf16 v[64:67], a[172:175], a[40:43], v[64:67]\n"
"ds_read_b128 a[168:171], v2 offset:28672\n"
"v_mul_f32 v246, v246, v254\n"
"v_mfma_f32_16x16x32_bf16 v[68:71], a[172:175], a[44:47], v[68:71]\n"
"v_mul_f32 v247, v247, v254\n"
"v_mul_f32 v248, v248, v254\n"
"v_mul_f32 v249, v249, v254\n"
"v_mfma_f32_16x16x32_bf16 v[72:75], a[144:147], a[40:43], v[72:75]\n"
"ds_read_b128 a[172:175], v2 offset:29696\n"
"buffer_load_dwordx4 a[136:139], v3, s[16:19], s62 offen offset:0 sc1\n"
"v_mfma_f32_16x16x32_bf16 v[76:79], a[144:147], a[44:47], v[76:79]\n"
"v_mul_f32 v250, v250, v254\n"
"v_mul_f32 v251, v251, v254\n"
"v_mfma_f32_16x16x32_bf16 v[80:83], a[148:151], a[40:43], v[80:83]\n"
"ds_read_b128 a[144:147], v2 offset:30720\n"
"v_mul_f32 v252, v252, v254\n"
"v_mfma_f32_16x16x32_bf16 v[84:87], a[148:151], a[44:47], v[84:87]\n"
"v_mul_f32 v253, v253, v254\n"
"v_cvt_pk_bf16_f32 v192, v208, v209\n"
"s_waitcnt lgkmcnt(4)\n"
"v_mfma_f32_16x16x32_bf16 v[88:91], a[152:155], a[40:43], v[88:91]\n"
"ds_read_b128 a[148:151], v2 offset:31744\n"
"buffer_load_dwordx4 a[140:143], v3, s[16:19], s62 offen offset:1024 sc1\n"
"v_mfma_f32_16x16x32_bf16 v[92:95], a[152:155], a[44:47], v[92:95]\n"
"v_cvt_pk_bf16_f32 v193, v210, v211\n"
"v_cvt_pk_bf16_f32 v194, v240, v241\n"
"v_mfma_f32_16x16x32_bf16 v[96:99], a[156:159], a[40:43], v[96:99]\n"
"ds_read_b128 a[152:155], v2 offset:32768\n"
"v_cvt_pk_bf16_f32 v195, v242, v243\n"
"v_mfma_f32_16x16x32_bf16 v[100:103], a[156:159], a[44:47], v[100:103]\n"
"v_cvt_pk_bf16_f32 v196, v244, v245\n"
"v_cvt_pk_bf16_f32 v197, v246, v247\n"
"v_cvt_pk_bf16_f32 v198, v248, v249\n"
"v_mfma_f32_16x16x32_bf16 v[104:107], a[160:163], a[40:43], v[104:107]\n"
"ds_read_b128 a[156:159], v2 offset:33792\n"
"buffer_load_dwordx4 a[176:179], v3, s[16:19], s62 offen offset:2048 sc1\n"
"v_mfma_f32_16x16x32_bf16 v[108:111], a[160:163], a[44:47], v[108:111]\n"
"v_cvt_pk_bf16_f32 v199, v250, v251\n"
"s_waitcnt lgkmcnt(4)\n"
"v_mfma_f32_16x16x32_bf16 v[40:43], a[164:167], a[48:51], v[40:43]\n"
"ds_read_b128 a[160:163], v2 offset:34816\n"
"v_cvt_pk_bf16_f32 v212, v252, v253\n"
"v_mfma_f32_16x16x32_bf16 v[44:47], a[164:167], a[52:55], v[44:47]\n"
"buffer_store_dwordx4 v[192:195], v19, s[16:19], s69 offen offset:2048 sc1\n"
"buffer_store_dwordx4 v[196:199], v19, s[16:19], s69 offen offset:2304 sc1\n"
"buffer_store_dwordx4 v[212:215], v20, s[16:19], s69 offen offset:512 sc1\n"
"v_mfma_f32_16x16x32_bf16 v[48:51], a[168:171], a[48:51], v[48:51]\n"
"ds_read_b128 a[164:167], v2 offset:35840\n"
"v_and_b32 v33, 3, v1\n"
"v_mfma_f32_16x16x32_bf16 v[52:55], a[168:171], a[52:55], v[52:55]\n"
"v_lshlrev_b32 v33, 7, v33\n"
"v_add_u32 v33, 4, v33\n"
"v_mfma_f32_16x16x32_bf16 v[56:59], a[172:175], a[48:51], v[56:59]\n"
"s_add_u32 s68, s67, 2490368\n"
"s_add_u32 s70, s66, 2490368\n"
"v_mfma_f32_16x16x32_bf16 v[60:63], a[172:175], a[52:55], v[60:63]\n"
"v_exp_f32 v7, v7\n"
"s_waitcnt lgkmcnt(3)\n"
"v_mfma_f32_16x16x32_bf16 v[64:67], a[144:147], a[48:51], v[64:67]\n"
"v_exp_f32 v16, v16\n"
"v_mfma_f32_16x16x32_bf16 v[68:71], a[144:147], a[52:55], v[68:71]\n"
"s_waitcnt vmcnt(43)\n"
"s_waitcnt lgkmcnt(0)\n"
"s_barrier\n"
"v_mfma_f32_16x16x32_bf16 v[72:75], a[148:151], a[48:51], v[72:75]\n"
"ds_read_b128 a[168:171], v2 offset:36864\n"
"s_add_u32 s61, s61, 36864\n"
"v_mfma_f32_16x16x32_bf16 v[76:79], a[148:151], a[52:55], v[76:79]\n"
"ds_read_b128 a[172:175], v2 offset:37888\n"
"s_add_u32 m0, s60, 0\n"
"v_mfma_f32_16x16x32_bf16 v[80:83], a[152:155], a[48:51], v[80:83]\n"
"ds_read_b128 a[144:147], v2 offset:38912\n"
"v_mfma_f32_16x16x32_bf16 v[84:87], a[152:155], a[52:55], v[84:87]\n"
"ds_read_b128 a[148:151], v2 offset:39936\n"
"v_mfma_f32_16x16x32_bf16 v[88:91], a[156:159], a[48:51], v[88:91]\n"
"ds_read_b128 a[152:155], v2 offset:40960\n"
"v_mfma_f32_16x16x32_bf16 v[92:95], a[156:159], a[52:55], v[92:95]\n"
"v_exp_f32 v37, v37\n"
"v_add_f32 v7, 1.0, v7\n"
"v_mfma_f32_16x16x32_bf16 v[96:99], a[160:163], a[48:51], v[96:99]\n"
"ds_read_b128 a[156:159], v2 offset:41984\n"
"v_add_f32 v16, 1.0, v16\n"
"v_mfma_f32_16x16x32_bf16 v[100:103], a[160:163], a[52:55], v[100:103]\n"
"v_add_f32 v37, 1.0, v37\n"
"v_mfma_f32_16x16x32_bf16 v[104:107], a[164:167], a[48:51], v[104:107]\n"
"ds_read_b128 a[160:163], v2 offset:43008\n"
"v_mfma_f32_16x16x32_bf16 v[108:111], a[164:167], a[52:55], v[108:111]\n"
"v_rcp_f32 v7, v7\n"
"s_waitcnt lgkmcnt(4)\n"
"v_mfma_f32_16x16x32_bf16 v[40:43], a[168:171], a[56:59], v[40:43]\n"
"ds_read_b128 a[164:167], v2 offset:44032\n"
"v_mfma_f32_16x16x32_bf16 v[44:47], a[168:171], a[60:63], v[44:47]\n"
"v_rcp_f32 v16, v16\n"
"v_mfma_f32_16x16x32_bf16 v[48:51], a[172:175], a[56:59], v[48:51]\n"
"ds_read_b128 a[168:171], v2 offset:45056\n"
"v_mfma_f32_16x16x32_bf16 v[52:55], a[172:175], a[60:63], v[52:55]\n"
"v_rcp_f32 v37, v37\n"
"v_mul_f32 v157, v157, v7\n"
"v_mfma_f32_16x16x32_bf16 v[56:59], a[144:147], a[56:59], v[56:59]\n"
"ds_read_b128 a[172:175], v2 offset:46080\n"
"buffer_load_dwordx4 a[180:183], v3, s[16:19], s62 offen offset:3072 sc1\n"
"v_mfma_f32_16x16x32_bf16 v[60:63], a[144:147], a[60:63], v[60:63]\n"
"s_add_u32 s62, s62, 8192\n"
"s_waitcnt lgkmcnt(4)\n"
"v_mfma_f32_16x16x32_bf16 v[64:67], a[148:151], a[56:59], v[64:67]\n"
"ds_read_b128 a[144:147], v2 offset:47104\n"
"v_mul_f32 v158, v158, v16\n"
"v_mfma_f32_16x16x32_bf16 v[68:71], a[148:151], a[60:63], v[68:71]\n"
"v_mul_f32 v159, v159, v37\n"
"v_mul_f32 v7, v160, v160\n"
"v_mul_f32 v16, v161, v161\n"
"v_mfma_f32_16x16x32_bf16 v[72:75], a[152:155], a[56:59], v[72:75]\n"
"ds_read_b128 a[148:151], v2 offset:48128\n"
"buffer_load_dwordx4 a[184:187], v2, s[16:19], s62 offen offset:0 sc1\n"
"v_mfma_f32_16x16x32_bf16 v[76:79], a[152:155], a[60:63], v[76:79]\n"
"v_mul_f32 v37, v162, v162\n"
"v_fma_f32 v7, v7, s80, v38\n"
"v_mfma_f32_16x16x32_bf16 v[80:83], a[156:159], a[56:59], v[80:83]\n"
"ds_read_b128 a[152:155], v2 offset:49152\n"
"v_fma_f32 v16, v16, s80, v38\n"
"v_mfma_f32_16x16x32_bf16 v[84:87], a[156:159], a[60:63], v[84:87]\n"
"v_fma_f32 v37, v37, s80, v38\n"
"v_mul_f32 v7, v7, v160\n"
"s_waitcnt lgkmcnt(4)\n"
"v_mfma_f32_16x16x32_bf16 v[88:91], a[160:163], a[56:59], v[88:91]\n"
"ds_read_b128 a[156:159], v2 offset:50176\n"
"buffer_load_dwordx4 a[188:191], v2, s[16:19], s62 offen offset:1024 sc1\n"
"v_mfma_f32_16x16x32_bf16 v[92:95], a[160:163], a[60:63], v[92:95]\n"
"v_mul_f32 v16, v16, v161\n"
"v_mul_f32 v37, v37, v162\n"
"v_mfma_f32_16x16x32_bf16 v[96:99], a[164:167], a[56:59], v[96:99]\n"
"ds_read_b128 a[160:163], v2 offset:51200\n"
"v_mfma_f32_16x16x32_bf16 v[100:103], a[164:167], a[60:63], v[100:103]\n"
"v_exp_f32 v7, v7\n"
"v_mfma_f32_16x16x32_bf16 v[104:107], a[168:171], a[56:59], v[104:107]\n"
"ds_read_b128 a[164:167], v2 offset:52224\n"
"buffer_load_dwordx4 a[192:195], v2, s[16:19], s62 offen offset:2048 sc1\n"
"v_mfma_f32_16x16x32_bf16 v[108:111], a[168:171], a[60:63], v[108:111]\n"
"s_waitcnt vmcnt(45) lgkmcnt(4)\n"
"v_mfma_f32_16x16x32_bf16 v[40:43], a[172:175], a[64:67], v[40:43]\n"
"ds_read_b128 a[168:171], v2 offset:53248\n"
"v_mfma_f32_16x16x32_bf16 v[44:47], a[172:175], a[68:71], v[44:47]\n"
"v_exp_f32 v16, v16\n"
"v_mfma_f32_16x16x32_bf16 v[48:51], a[144:147], a[64:67], v[48:51]\n"
"ds_read_b128 a[172:175], v2 offset:54272\n"
"v_mfma_f32_16x16x32_bf16 v[52:55], a[144:147], a[68:71], v[52:55]\n"
"buffer_load_dwordx4 a[196:199], v2, s[16:19], s62 offen offset:3072 sc1\n"
"v_mfma_f32_16x16x32_bf16 v[56:59], a[148:151], a[64:67], v[56:59]\n"
"ds_read_b128 a[144:147], v2 offset:55296\n"
"v_mfma_f32_16x16x32_bf16 v[60:63], a[148:151], a[68:71], v[60:63]\n"
"v_exp_f32 v37, v37\n"
"s_waitcnt lgkmcnt(4)\n"
"v_mfma_f32_16x16x32_bf16 v[64:67], a[152:155], a[64:67], v[64:67]\n"
"ds_read_b128 a[148:151], v2 offset:56320\n"
"v_add_f32 v7, 1.0, v7\n"
"v_mfma_f32_16x16x32_bf16 v[68:71], a[152:155], a[68:71], v[68:71]\n"
"buffer_load_dwordx4 a[200:203], v3, s[16:19], s62 offen offset:0 sc1\n"
"v_add_f32 v16, 1.0, v16\n"
"v_mfma_f32_16x16x32_bf16 v[72:75], a[156:159], a[64:67], v[72:75]\n"
"ds_read_b128 a[152:155], v2 offset:57344\n"
"v_add_f32 v37, 1.0, v37\n"
"v_mfma_f32_16x16x32_bf16 v[76:79], a[156:159], a[68:71], v[76:79]\n"
"v_rcp_f32 v7, v7\n"
"v_mfma_f32_16x16x32_bf16 v[80:83], a[160:163], a[64:67], v[80:83]\n"
"ds_read_b128 a[156:159], v2 offset:58368\n"
"v_mfma_f32_16x16x32_bf16 v[84:87], a[160:163], a[68:71], v[84:87]\n"
"s_waitcnt lgkmcnt(4)\n"
"v_mfma_f32_16x16x32_bf16 v[88:91], a[164:167], a[64:67], v[88:91]\n"
"ds_read_b128 a[160:163], v2 offset:59392\n"
"buffer_load_dwordx4 a[204:207], v3, s[16:19], s62 offen offset:1024 sc1\n"
"v_mfma_f32_16x16x32_bf16 v[92:95], a[164:167], a[68:71], v[92:95]\n"
"v_rcp_f32 v16, v16\n"
"v_mfma_f32_16x16x32_bf16 v[96:99], a[168:171], a[64:67], v[96:99]\n"
"ds_read_b128 a[164:167], v2 offset:60416\n"
"v_mfma_f32_16x16x32_bf16 v[100:103], a[168:171], a[68:71], v[100:103]\n"
"v_rcp_f32 v37, v37\n"
"v_mfma_f32_16x16x32_bf16 v[104:107], a[172:175], a[64:67], v[104:107]\n"
"ds_read_b128 a[168:171], v2 offset:61440\n"
"buffer_load_dwordx4 a[208:211], v3, s[16:19], s62 offen offset:2048 sc1\n"
"v_mfma_f32_16x16x32_bf16 v[108:111], a[172:175], a[68:71], v[108:111]\n"
"v_mul_f32 v160, v160, v7\n"
"v_mul_f32 v161, v161, v16\n"
"s_waitcnt vmcnt(47) lgkmcnt(4)\n"
"v_mfma_f32_16x16x32_bf16 v[40:43], a[144:147], a[72:75], v[40:43]\n"
"ds_read_b128 a[172:175], v2 offset:62464\n"
"v_mul_f32 v162, v162, v37\n"
"v_mfma_f32_16x16x32_bf16 v[44:47], a[144:147], a[76:79], v[44:47]\n"
"v_mul_f32 v7, v163, v163\n"
"v_mul_f32 v16, v164, v164\n"
"v_mfma_f32_16x16x32_bf16 v[48:51], a[148:151], a[72:75], v[48:51]\n"
"ds_read_b128 a[144:147], v2 offset:63488\n"
"buffer_load_dwordx4 a[212:215], v3, s[16:19], s62 offen offset:3072 sc1\n"
"v_mfma_f32_16x16x32_bf16 v[52:55], a[148:151], a[76:79], v[52:55]\n"
"s_add_u32 s62, s62, 8192\n"
"v_mul_f32 v37, v165, v165\n"
"v_fma_f32 v7, v7, s80, v38\n"
"v_mfma_f32_16x16x32_bf16 v[56:59], a[152:155], a[72:75], v[56:59]\n"
"ds_read_b128 a[148:151], v2 offset:64512\n"
"v_fma_f32 v16, v16, s80, v38\n"
"v_mfma_f32_16x16x32_bf16 v[60:63], a[152:155], a[76:79], v[60:63]\n"
"v_fma_f32 v37, v37, s80, v38\n"
"s_waitcnt lgkmcnt(4)\n"
"v_mfma_f32_16x16x32_bf16 v[64:67], a[156:159], a[72:75], v[64:67]\n"
"ds_read_b128 a[152:155], v5 offset:0\n"
"buffer_load_dwordx4 a[216:219], v2, s[16:19], s62 offen offset:0 sc1\n"
"v_mfma_f32_16x16x32_bf16 v[68:71], a[156:159], a[76:79], v[68:71]\n"
"v_mul_f32 v7, v7, v163\n"
"v_mul_f32 v16, v16, v164\n"
"v_mul_f32 v37, v37, v165\n"
"v_mfma_f32_16x16x32_bf16 v[72:75], a[160:163], a[72:75], v[72:75]\n"
"ds_read_b128 a[156:159], v5 offset:1024\n"
"v_mfma_f32_16x16x32_bf16 v[76:79], a[160:163], a[76:79], v[76:79]\n"
"v_exp_f32 v7, v7\n"
"v_mfma_f32_16x16x32_bf16 v[80:83], a[164:167], a[72:75], v[80:83]\n"
"ds_read_b128 a[160:163], v5 offset:2048\n"
"v_mfma_f32_16x16x32_bf16 v[84:87], a[164:167], a[76:79], v[84:87]\n"
"buffer_load_dwordx4 a[220:223], v2, s[16:19], s62 offen offset:1024 sc1\n"
"s_waitcnt lgkmcnt(4)\n"
"v_mfma_f32_16x16x32_bf16 v[88:91], a[168:171], a[72:75], v[88:91]\n"
"ds_read_b128 a[164:167], v5 offset:3072\n"
"v_mfma_f32_16x16x32_bf16 v[92:95], a[168:171], a[76:79], v[92:95]\n"
"v_exp_f32 v16, v16\n"
"v_mfma_f32_16x16x32_bf16 v[96:99], a[172:175], a[72:75], v[96:99]\n"
"ds_read_b128 a[168:171], v5 offset:4096\n"
"v_mfma_f32_16x16x32_bf16 v[100:103], a[172:175], a[76:79], v[100:103]\n"
"buffer_load_dwordx4 v2, s[16:19], s61 offen offset:0 sc1 lds\n"
"v_exp_f32 v37, v37\n"
"v_mfma_f32_16x16x32_bf16 v[104:107], a[144:147], a[72:75], v[104:107]\n"
"ds_read_b128 a[172:175], v5 offset:5120\n"
"v_add_f32 v7, 1.0, v7\n"
"v_mfma_f32_16x16x32_bf16 v[108:111], a[144:147], a[76:79], v[108:111]\n"
"v_add_f32 v16, 1.0, v16\n"
"s_waitcnt vmcnt(49) lgkmcnt(4)\n"
"v_mfma_f32_16x16x32_bf16 v[40:43], a[148:151], a[80:83], v[40:43]\n"
"ds_read_b128 a[144:147], v5 offset:6144\n"
"v_add_f32 v37, 1.0, v37\n"
"v_mfma_f32_16x16x32_bf16 v[44:47], a[148:151], a[84:87], v[44:47]\n"
"v_rcp_f32 v7, v7\n"
"v_mfma_f32_16x16x32_bf16 v[48:51], a[152:155], a[80:83], v[48:51]\n"
"ds_read_b128 a[148:151], v5 offset:7168\n"
"buffer_load_dwordx4 v2, s[16:19], s61 offen offset:1024 sc1 lds\n"
"v_mfma_f32_16x16x32_bf16 v[52:55], a[152:155], a[84:87], v[52:55]\n"
"v_rcp_f32 v16, v16\n"
"v_mfma_f32_16x16x32_bf16 v[56:59], a[156:159], a[80:83], v[56:59]\n"
"v_rcp_f32 v37, v37\n"
"v_mfma_f32_16x16x32_bf16 v[60:63], a[156:159], a[84:87], v[60:63]\n"
"v_mul_f32 v163, v163, v7\n"
"v_mul_f32 v164, v164, v16\n"
"s_waitcnt lgkmcnt(3)\n"
"v_mfma_f32_16x16x32_bf16 v[64:67], a[160:163], a[80:83], v[64:67]\n"
"buffer_load_dwordx4 v2, s[16:19], s61 offen offset:2048 sc1 lds\n"
"v_mul_f32 v165, v165, v37\n"
"v_mfma_f32_16x16x32_bf16 v[68:71], a[160:163], a[84:87], v[68:71]\n"
"s_waitcnt vmcnt(39)\n"
"s_waitcnt lgkmcnt(0)\n"
"s_barrier\n"
"v_mfma_f32_16x16x32_bf16 v[72:75], a[164:167], a[80:83], v[72:75]\n"
"ds_read_b128 a[152:155], v5 offset:45056\n"
"v_mul_f32 v7, v166, v166\n"
"v_mfma_f32_16x16x32_bf16 v[76:79], a[164:167], a[84:87], v[76:79]\n"
"ds_read_b128 a[156:159], v5 offset:46080\n"
"v_mul_f32 v16, v167, v167\n"
"v_mul_f32 v37, v168, v168\n"
"v_mfma_f32_16x16x32_bf16 v[80:83], a[168:171], a[80:83], v[80:83]\n"
"ds_read_b128 a[160:163], v5 offset:47104\n"
"buffer_load_dwordx4 v2, s[16:19], s61 offen offset:3072 sc1 lds\n"
"v_mfma_f32_16x16x32_bf16 v[84:87], a[168:171], a[84:87], v[84:87]\n"
"ds_read_b128 a[164:167], v5 offset:48128\n"
"v_mfma_f32_16x16x32_bf16 v[88:91], a[172:175], a[80:83], v[88:91]\n"
"ds_read_b128 a[168:171], v5 offset:49152\n"
"s_add_u32 m0, s60, 4096\n"
"v_mfma_f32_16x16x32_bf16 v[92:95], a[172:175], a[84:87], v[92:95]\n"
"s_waitcnt vmcnt(15)\n"
"v_fma_f32 v7, v7, s80, v38\n"
"v_fma_f32 v16, v16, s80, v38\n"
"v_mfma_f32_16x16x32_bf16 v[96:99], a[144:147], a[80:83], v[96:99]\n"
"ds_read_b128 a[172:175], v5 offset:50176\n"
"buffer_load_dwordx4 v3, s[16:19], s61 offen offset:0 sc1 lds\n"
"v_mfma_f32_16x16x32_bf16 v[100:103], a[144:147], a[84:87], v[100:103]\n"
"s_mov_b64 exec, 0xff\n"
"buffer_store_dwordx4 v[16:19], v2, s[16:19], s68 offen sc1\n"
"s_mov_b64 exec, -1\n"
"v_mfma_f32_16x16x32_bf16 v[104:107], a[148:151], a[80:83], v[104:107]\n"
"ds_read_b128 a[144:147], v5 offset:51200\n"
"v_fma_f32 v37, v37, s80, v38\n"
"v_mfma_f32_16x16x32_bf16 v[108:111], a[148:151], a[84:87], v[108:111]\n"
"v_mul_f32 v7, v7, v166\n"
"v_mul_f32 v16, v16, v167\n"
"s_waitcnt lgkmcnt(4)\n"
"v_mfma_f32_16x16x32_bf16 v[40:43], a[152:155], a[88:91], v[40:43]\n"
"ds_read_b128 a[148:151], v5 offset:52224\n"
"v_mul_f32 v37, v37, v168\n"
"v_mfma_f32_16x16x32_bf16 v[44:47], a[152:155], a[92:95], v[44:47]\n"
"v_exp_f32 v7, v7\n"
"v_mfma_f32_16x16x32_bf16 v[48:51], a[156:159], a[88:91], v[48:51]\n"
"ds_read_b128 a[152:155], v5 offset:53248\n"
"v_mfma_f32_16x16x32_bf16 v[52:55], a[156:159], a[92:95], v[52:55]\n"
"v_exp_f32 v16, v16\n"
"v_mfma_f32_16x16x32_bf16 v[56:59], a[160:163], a[88:91], v[56:59]\n"
"ds_read_b128 a[156:159], v5 offset:54272\n"
"buffer_load_dwordx4 v3, s[16:19], s61 offen offset:1024 sc1 lds\n"
"v_mfma_f32_16x16x32_bf16 v[60:63], a[160:163], a[92:95], v[60:63]\n"
"s_waitcnt lgkmcnt(4)\n"
"v_mfma_f32_16x16x32_bf16 v[64:67], a[164:167], a[88:91], v[64:67]\n"
"ds_read_b128 a[160:163], v5 offset:55296\n"
"v_mfma_f32_16x16x32_bf16 v[68:71], a[164:167], a[92:95], v[68:71]\n"
"v_exp_f32 v37, v37\n"
"v_add_f32 v7, 1.0, v7\n"
"v_mfma_f32_16x16x32_bf16 v[72:75], a[168:171], a[88:91], v[72:75]\n"
"ds_read_b128 a[164:167], v5 offset:56320\n"
"v_add_f32 v16, 1.0, v16\n"
"v_mfma_f32_16x16x32_bf16 v[76:79], a[168:171], a[92:95], v[76:79]\n"
"buffer_load_dwordx4 v3, s[16:19], s61 offen offset:2048 sc1 lds\n"
"v_add_f32 v37, 1.0, v37\n"
"v_mfma_f32_16x16x32_bf16 v[80:83], a[172:175], a[88:91], v[80:83]\n"
"ds_read_b128 a[168:171], v5 offset:57344\n"
"v_mfma_f32_16x16x32_bf16 v[84:87], a[172:175], a[92:95], v[84:87]\n"
"v_rcp_f32 v7, v7\n"
"s_waitcnt lgkmcnt(4)\n"
"v_mfma_f32_16x16x32_bf16 v[88:91], a[144:147], a[88:91], v[88:91]\n"
"ds_read_b128 a[172:175], v5 offset:58368\n"
"v_mfma_f32_16x16x32_bf16 v[92:95], a[144:147], a[92:95], v[92:95]\n"
"buffer_load_dwordx4 v3, s[16:19], s61 offen offset:3072 sc1 lds\n"
"s_add_u32 m0, s60, 8192\n"
"v_mfma_f32_16x16x32_bf16 v[96:99], a[148:151], a[88:91], v[96:99]\n"
"ds_read_b128 a[144:147], v5 offset:59392\n"
"v_mfma_f32_16x16x32_bf16 v[100:103], a[148:151], a[92:95], v[100:103]\n"
"v_rcp_f32 v16, v16\n"
"v_mfma_f32_16x16x32_bf16 v[104:107], a[152:155], a[88:91], v[104:107]\n"
"ds_read_b128 a[148:151], v5 offset:60416\n"
"v_mfma_f32_16x16x32_bf16 v[108:111], a[152:155], a[92:95], v[108:111]\n"
"s_waitcnt lgkmcnt(4)\n"
"v_mfma_f32_16x16x32_bf16 v[40:43], a[156:159], a[96:99], v[40:43]\n"
"ds_read_b128 a[152:155], v5 offset:61440\n"
"buffer_load_dwordx4 v4, s[16:19], s61 offen offset:0 sc1 lds\n"
"v_mfma_f32_16x16x32_bf16 v[44:47], a[156:159], a[100:103], v[44:47]\n"
"s_add_u32 s61, s61, 36864\n"
"s_add_u32 m0, s60, 36864\n"
"v_mfma_f32_16x16x32_bf16 v[48:51], a[160:163], a[96:99], v[48:51]\n"
"ds_read_b128 a[156:159], v5 offset:62464\n"
"v_mfma_f32_16x16x32_bf16 v[52:55], a[160:163], a[100:103], v[52:55]\n"
"v_rcp_f32 v37, v37\n"
"v_mfma_f32_16x16x32_bf16 v[56:59], a[164:167], a[96:99], v[56:59]\n"
"ds_read_b128 a[160:163], v5 offset:63488\n"
"buffer_load_dwordx4 v2, s[16:19], s61 offen offset:0 sc1 lds\n"
"v_mfma_f32_16x16x32_bf16 v[60:63], a[164:167], a[100:103], v[60:63]\n"
"v_mul_f32 v166, v166, v7\n"
"v_mul_f32 v167, v167, v16\n"
"s_waitcnt lgkmcnt(4)\n"
"v_mfma_f32_16x16x32_bf16 v[64:67], a[168:171], a[96:99], v[64:67]\n"
"ds_read_b128 a[164:167], v5 offset:64512\n"
"v_mul_f32 v168, v168, v37\n"
"v_mfma_f32_16x16x32_bf16 v[68:71], a[168:171], a[100:103], v[68:71]\n"
"v_mul_f32 v7, v169, v169\n"
"v_mul_f32 v16, v170, v170\n"
"v_mfma_f32_16x16x32_bf16 v[72:75], a[172:175], a[96:99], v[72:75]\n"
"ds_read_b128 a[168:171], v0 offset:0\n"
"buffer_load_dwordx4 v2, s[16:19], s61 offen offset:1024 sc1 lds\n"
"v_mfma_f32_16x16x32_bf16 v[76:79], a[172:175], a[100:103], v[76:79]\n"
"v_mul_f32 v37, v171, v171\n"
"v_fma_f32 v7, v7, s80, v38\n"
"v_fma_f32 v16, v16, s80, v38\n"
"v_mfma_f32_16x16x32_bf16 v[80:83], a[144:147], a[96:99], v[80:83]\n"
"ds_read_b128 a[172:175], v0 offset:1024\n"
"v_fma_f32 v37, v37, s80, v38\n"
"v_mfma_f32_16x16x32_bf16 v[84:87], a[144:147], a[100:103], v[84:87]\n"
"v_mul_f32 v7, v7, v169\n"
"s_waitcnt lgkmcnt(4)\n"
"v_mfma_f32_16x16x32_bf16 v[88:91], a[148:151], a[96:99], v[88:91]\n"
"ds_read_b128 a[144:147], v0 offset:2048\n"
"buffer_load_dwordx4 v2, s[16:19], s61 offen offset:2048 sc1 lds\n"
"v_mfma_f32_16x16x32_bf16 v[92:95], a[148:151], a[100:103], v[92:95]\n"
"v_mul_f32 v16, v16, v170\n"
"v_mul_f32 v37, v37, v171\n"
"v_mfma_f32_16x16x32_bf16 v[96:99], a[152:155], a[96:99], v[96:99]\n"
"ds_read_b128 a[148:151], v0 offset:3072\n"
"v_mfma_f32_16x16x32_bf16 v[100:103], a[152:155], a[100:103], v[100:103]\n"
"v_exp_f32 v7, v7\n"
"v_mfma_f32_16x16x32_bf16 v[104:107], a[156:159], a[96:99], v[104:107]\n"
"ds_read_b128 a[152:155], v0 offset:4096\n"
"v_mfma_f32_16x16x32_bf16 v[108:111], a[156:159], a[100:103], v[108:111]\n"
"buffer_load_dwordx4 v2, s[16:19], s61 offen offset:3072 sc1 lds\n"
"s_waitcnt lgkmcnt(4)\n"
"v_mfma_f32_16x16x32_bf16 v[40:43], a[160:163], a[104:107], v[40:43]\n"
"ds_read_b128 a[156:159], v0 offset:5120\n"
"s_add_u32 m0, s60, 40960\n"
"v_mfma_f32_16x16x32_bf16 v[44:47], a[160:163], a[108:111], v[44:47]\n"
"v_exp_f32 v16, v16\n"
"v_mfma_f32_16x16x32_bf16 v[48:51], a[164:167], a[104:107], v[48:51]\n"
"ds_read_b128 a[160:163], v0 offset:6144\n"
"buffer_load_dwordx4 v3, s[16:19], s61 offen offset:0 sc1 lds\n"
"v_mfma_f32_16x16x32_bf16 v[52:55], a[164:167], a[108:111], v[52:55]\n"
"v_exp_f32 v37, v37\n"
"v_add_f32 v7, 1.0, v7\n"
"v_mfma_f32_16x16x32_bf16 v[56:59], a[168:171], a[104:107], v[56:59]\n"
"ds_read_b128 a[164:167], v0 offset:7168\n"
"v_add_f32 v16, 1.0, v16\n"
"v_mfma_f32_16x16x32_bf16 v[60:63], a[168:171], a[108:111], v[60:63]\n"
"buffer_load_dwordx4 v3, s[16:19], s61 offen offset:1024 sc1 lds\n"
"s_waitcnt lgkmcnt(4)\n"
"v_mfma_f32_16x16x32_bf16 v[64:67], a[172:175], a[104:107], v[64:67]\n"
"ds_read_b128 a[168:171], v0 offset:8192\n"
"v_add_f32 v37, 1.0, v37\n"
"v_mfma_f32_16x16x32_bf16 v[68:71], a[172:175], a[108:111], v[68:71]\n"
"v_rcp_f32 v7, v7\n"
"v_mfma_f32_16x16x32_bf16 v[72:75], a[144:147], a[104:107], v[72:75]\n"
"ds_read_b128 a[172:175], v0 offset:9216\n"
"buffer_load_dwordx4 v3, s[16:19], s61 offen offset:2048 sc1 lds\n"
"v_mfma_f32_16x16x32_bf16 v[76:79], a[144:147], a[108:111], v[76:79]\n"
"v_rcp_f32 v16, v16\n"
"v_mfma_f32_16x16x32_bf16 v[80:83], a[148:151], a[104:107], v[80:83]\n"
"ds_read_b128 a[144:147], v0 offset:10240\n"
"v_mfma_f32_16x16x32_bf16 v[84:87], a[148:151], a[108:111], v[84:87]\n"
"v_rcp_f32 v37, v37\n"
"s_waitcnt lgkmcnt(4)\n"
"v_mfma_f32_16x16x32_bf16 v[88:91], a[152:155], a[104:107], v[88:91]\n"
"ds_read_b128 a[148:151], v0 offset:11264\n"
"buffer_load_dwordx4 v3, s[16:19], s61 offen offset:3072 sc1 lds\n"
"v_mfma_f32_16x16x32_bf16 v[92:95], a[152:155], a[108:111], v[92:95]\n"
"s_add_u32 m0, s60, 45056\n"
"v_mul_f32 v169, v169, v7\n"
"v_mfma_f32_16x16x32_bf16 v[96:99], a[156:159], a[104:107], v[96:99]\n"
"ds_read_b128 a[152:155], v0 offset:12288\n"
"v_mul_f32 v170, v170, v16\n"
"v_mfma_f32_16x16x32_bf16 v[100:103], a[156:159], a[108:111], v[100:103]\n"
"v_mul_f32 v171, v171, v37\n"
"v_mul_f32 v7, v172, v172\n"
"v_mul_f32 v16, v173, v173\n"
"v_mfma_f32_16x16x32_bf16 v[104:107], a[160:163], a[104:107], v[104:107]\n"
"ds_read_b128 a[156:159], v0 offset:13312\n"
"buffer_load_dwordx4 v4, s[16:19], s61 offen offset:0 sc1 lds\n"
"v_mfma_f32_16x16x32_bf16 v[108:111], a[160:163], a[108:111], v[108:111]\n"
"v_mul_f32 v37, v174, v174\n"
"s_waitcnt lgkmcnt(4)\n"
"v_mfma_f32_16x16x32_bf16 v[40:43], a[164:167], a[112:115], v[40:43]\n"
"ds_read_b128 a[160:163], v0 offset:14336\n"
"v_fma_f32 v7, v7, s80, v38\n"
"v_mfma_f32_16x16x32_bf16 v[44:47], a[164:167], a[116:119], v[44:47]\n"
"v_fma_f32 v16, v16, s80, v38\n"
"v_fma_f32 v37, v37, s80, v38\n"
"v_mul_f32 v7, v7, v172\n"
"v_mfma_f32_16x16x32_bf16 v[48:51], a[168:171], a[112:115], v[48:51]\n"
"ds_read_b128 a[164:167], v0 offset:15360\n"
"buffer_load_dwordx4 a[224:227], v2, s[16:19], s62 offen offset:2048 sc1\n"
"v_mfma_f32_16x16x32_bf16 v[52:55], a[168:171], a[116:119], v[52:55]\n"
"buffer_load_dword v34, v33, s[16:19], s70 offen sc1\n"
"v_mul_f32 v16, v16, v173\n"
"v_mfma_f32_16x16x32_bf16 v[56:59], a[172:175], a[112:115], v[56:59]\n"
"v_mul_f32 v37, v37, v174\n"
"v_mfma_f32_16x16x32_bf16 v[60:63], a[172:175], a[116:119], v[60:63]\n"
"v_exp_f32 v7, v7\n"
"s_waitcnt lgkmcnt(3)\n"
"v_mfma_f32_16x16x32_bf16 v[64:67], a[144:147], a[112:115], v[64:67]\n"
"buffer_load_dwordx4 a[228:231], v2, s[16:19], s62 offen offset:3072 sc1\n"
"v_mfma_f32_16x16x32_bf16 v[68:71], a[144:147], a[116:119], v[68:71]\n"
"s_waitcnt lgkmcnt(0)\n"
"s_barrier\n"
"v_mfma_f32_16x16x32_bf16 v[72:75], a[148:151], a[112:115], v[72:75]\n"
"ds_read_b128 a[168:171], v5 offset:8192\n"
"s_mul_i32 s61, s53, 221184\n"
"v_mfma_f32_16x16x32_bf16 v[76:79], a[148:151], a[116:119], v[76:79]\n"
"ds_read_b128 a[172:175], v5 offset:9216\n"
"s_add_u32 s61, s61, s60\n"
"v_mfma_f32_16x16x32_bf16 v[80:83], a[152:155], a[112:115], v[80:83]\n"
"ds_read_b128 a[144:147], v5 offset:10240\n"
"s_add_u32 s61, s61, 5242880\n"
"v_mfma_f32_16x16x32_bf16 v[84:87], a[152:155], a[116:119], v[84:87]\n"
"ds_read_b128 a[148:151], v5 offset:11264\n"
"v_mfma_f32_16x16x32_bf16 v[88:91], a[156:159], a[112:115], v[88:91]\n"
"ds_read_b128 a[152:155], v5 offset:12288\n"
"s_add_u32 m0, s60, 110592\n"
"v_mfma_f32_16x16x32_bf16 v[92:95], a[156:159], a[116:119], v[92:95]\n"
"buffer_load_dwordx4 a[232:235], v3, s[16:19], s62 offen offset:0 sc1\n"
"v_exp_f32 v16, v16\n"
"v_mfma_f32_16x16x32_bf16 v[96:99], a[160:163], a[112:115], v[96:99]\n"
"ds_read_b128 a[156:159], v5 offset:13312\n"
"v_mfma_f32_16x16x32_bf16 v[100:103], a[160:163], a[116:119], v[100:103]\n"
"v_exp_f32 v37, v37\n"
"v_mfma_f32_16x16x32_bf16 v[104:107], a[164:167], a[112:115], v[104:107]\n"
"ds_read_b128 a[160:163], v5 offset:14336\n"
"buffer_load_dwordx4 a[236:239], v3, s[16:19], s62 offen offset:1024 sc1\n"
"v_mfma_f32_16x16x32_bf16 v[108:111], a[164:167], a[116:119], v[108:111]\n"
"v_add_f32 v7, 1.0, v7\n"
"v_add_f32 v16, 1.0, v16\n"
"s_waitcnt lgkmcnt(4)\n"
"v_mfma_f32_16x16x32_bf16 v[40:43], a[168:171], a[120:123], v[40:43]\n"
"ds_read_b128 a[164:167], v5 offset:15360\n"
"v_add_f32 v37, 1.0, v37\n"
"v_mfma_f32_16x16x32_bf16 v[44:47], a[168:171], a[124:127], v[44:47]\n"
"buffer_load_dwordx4 a[240:243], v3, s[16:19], s62 offen offset:2048 sc1\n"
"v_mfma_f32_16x16x32_bf16 v[48:51], a[172:175], a[120:123], v[48:51]\n"
"ds_read_b128 a[168:171], v5 offset:16384\n"
"v_mfma_f32_16x16x32_bf16 v[52:55], a[172:175], a[124:127], v[52:55]\n"
"v_rcp_f32 v7, v7\n"
"v_mfma_f32_16x16x32_bf16 v[56:59], a[144:147], a[120:123], v[56:59]\n"
"ds_read_b128 a[172:175], v5 offset:17408\n"
"buffer_load_dwordx4 a[244:247], v3, s[16:19], s62 offen offset:3072 sc1\n"
"v_mfma_f32_16x16x32_bf16 v[60:63], a[144:147], a[124:127], v[60:63]\n"
"s_add_u32 s62, s62, 8192\n"
"s_waitcnt lgkmcnt(4)\n"
"v_mfma_f32_16x16x32_bf16 v[64:67], a[148:151], a[120:123], v[64:67]\n"
"ds_read_b128 a[144:147], v5 offset:18432\n"
"v_mfma_f32_16x16x32_bf16 v[68:71], a[148:151], a[124:127], v[68:71]\n"
"v_rcp_f32 v16, v16\n"
"v_mfma_f32_16x16x32_bf16 v[72:75], a[152:155], a[120:123], v[72:75]\n"
"ds_read_b128 a[148:151], v5 offset:19456\n"
"v_mfma_f32_16x16x32_bf16 v[76:79], a[152:155], a[124:127], v[76:79]\n"
"v_rcp_f32 v37, v37\n"
"v_mfma_f32_16x16x32_bf16 v[80:83], a[156:159], a[120:123], v[80:83]\n"
"ds_read_b128 a[152:155], v5 offset:20480\n"
"v_mul_f32 v172, v172, v7\n"
"v_mfma_f32_16x16x32_bf16 v[84:87], a[156:159], a[124:127], v[84:87]\n"
"v_mul_f32 v173, v173, v16\n"
"v_mul_f32 v174, v174, v37\n"
"s_waitcnt lgkmcnt(4)\n"
"v_mfma_f32_16x16x32_bf16 v[88:91], a[160:163], a[120:123], v[88:91]\n"
"ds_read_b128 a[156:159], v5 offset:21504\n"
"v_mul_f32 v7, v175, v175\n"
"v_mfma_f32_16x16x32_bf16 v[92:95], a[160:163], a[124:127], v[92:95]\n"
"v_mul_f32 v16, v176, v176\n"
"v_mul_f32 v37, v177, v177\n"
"v_mfma_f32_16x16x32_bf16 v[96:99], a[164:167], a[120:123], v[96:99]\n"
"ds_read_b128 a[160:163], v5 offset:22528\n"
"v_fma_f32 v7, v7, s80, v38\n"
"v_mfma_f32_16x16x32_bf16 v[100:103], a[164:167], a[124:127], v[100:103]\n"
"v_fma_f32 v16, v16, s80, v38\n"
"v_fma_f32 v37, v37, s80, v38\n"
"v_mul_f32 v7, v7, v175\n"
"v_mfma_f32_16x16x32_bf16 v[104:107], a[168:171], a[120:123], v[104:107]\n"
"ds_read_b128 a[164:167], v5 offset:23552\n"
"buffer_load_dwordx4 v2, s[16:19], s61 offen offset:0 sc1 lds\n"
"v_mfma_f32_16x16x32_bf16 v[108:111], a[168:171], a[124:127], v[108:111]\n"
"v_mul_f32 v16, v16, v176\n"
"s_waitcnt lgkmcnt(4)\n"
"v_mfma_f32_16x16x32_bf16 v[40:43], a[172:175], a[128:131], v[40:43]\n"
"ds_read_b128 a[168:171], v5 offset:24576\n"
"v_mul_f32 v37, v37, v177\n"
"v_mfma_f32_16x16x32_bf16 v[44:47], a[172:175], a[132:135], v[44:47]\n"
"v_exp_f32 v7, v7\n"
"v_mfma_f32_16x16x32_bf16 v[48:51], a[144:147], a[128:131], v[48:51]\n"
"ds_read_b128 a[172:175], v5 offset:25600\n"
"v_mfma_f32_16x16x32_bf16 v[52:55], a[144:147], a[132:135], v[52:55]\n"
"buffer_load_dwordx4 v2, s[16:19], s61 offen offset:1024 sc1 lds\n"
"v_mfma_f32_16x16x32_bf16 v[56:59], a[148:151], a[128:131], v[56:59]\n"
"ds_read_b128 a[144:147], v5 offset:26624\n"
"v_mfma_f32_16x16x32_bf16 v[60:63], a[148:151], a[132:135], v[60:63]\n"
"v_exp_f32 v16, v16\n"
"s_waitcnt lgkmcnt(4)\n"
"v_mfma_f32_16x16x32_bf16 v[64:67], a[152:155], a[128:131], v[64:67]\n"
"ds_read_b128 a[148:151], v5 offset:27648\n"
"v_mfma_f32_16x16x32_bf16 v[68:71], a[152:155], a[132:135], v[68:71]\n"
"buffer_load_dwordx4 v2, s[16:19], s61 offen offset:2048 sc1 lds\n"
"v_mfma_f32_16x16x32_bf16 v[72:75], a[156:159], a[128:131], v[72:75]\n"
"ds_read_b128 a[152:155], v5 offset:28672\n"
"v_mfma_f32_16x16x32_bf16 v[76:79], a[156:159], a[132:135], v[76:79]\n"
"v_exp_f32 v37, v37\n"
"v_add_f32 v7, 1.0, v7\n"
"v_mfma_f32_16x16x32_bf16 v[80:83], a[160:163], a[128:131], v[80:83]\n"
"ds_read_b128 a[156:159], v5 offset:29696\n"
"v_add_f32 v16, 1.0, v16\n"
"v_mfma_f32_16x16x32_bf16 v[84:87], a[160:163], a[132:135], v[84:87]\n"
"buffer_load_dwordx4 v2, s[16:19], s61 offen offset:3072 sc1 lds\n"
"s_waitcnt lgkmcnt(4)\n"
"v_mfma_f32_16x16x32_bf16 v[88:91], a[164:167], a[128:131], v[88:91]\n"
"ds_read_b128 a[160:163], v5 offset:30720\n"
"s_add_u32 m0, s60, 114688\n"
"v_mfma_f32_16x16x32_bf16 v[92:95], a[164:167], a[132:135], v[92:95]\n"
"v_add_f32 v37, 1.0, v37\n"
"v_rcp_f32 v7, v7\n"
"v_mfma_f32_16x16x32_bf16 v[96:99], a[168:171], a[128:131], v[96:99]\n"
"ds_read_b128 a[164:167], v5 offset:31744\n"
"v_mfma_f32_16x16x32_bf16 v[100:103], a[168:171], a[132:135], v[100:103]\n"
"v_rcp_f32 v16, v16\n"
"v_mfma_f32_16x16x32_bf16 v[104:107], a[172:175], a[128:131], v[104:107]\n"
"ds_read_b128 a[168:171], v5 offset:32768\n"
"buffer_load_dwordx4 v3, s[16:19], s61 offen offset:0 sc1 lds\n"
"v_mfma_f32_16x16x32_bf16 v[108:111], a[172:175], a[132:135], v[108:111]\n"
"s_waitcnt vmcnt(10)\n"
"v_cmp_ne_u32 vcc, s55, v34\n"
"s_cbranch_vccnz .Lmk_SLOW_6\n"
".Lmk_BACK_6:\n"
"s_waitcnt lgkmcnt(4)\n"
"v_mfma_f32_16x16x32_bf16 v[40:43], a[144:147], a[136:139], v[40:43]\n"
"ds_read_b128 a[172:175], v5 offset:33792\n"
"v_mfma_f32_16x16x32_bf16 v[44:47], a[144:147], a[140:143], v[44:47]\n"
"v_rcp_f32 v37, v37\n"
"v_mfma_f32_16x16x32_bf16 v[48:51], a[148:151], a[136:139], v[48:51]\n"
"ds_read_b128 a[144:147], v5 offset:34816\n"
"buffer_load_dwordx4 v3, s[16:19], s61 offen offset:1024 sc1 lds\n"
"v_mfma_f32_16x16x32_bf16 v[52:55], a[148:151], a[140:143], v[52:55]\n"
"v_mul_f32 v175, v175, v7\n"
"v_mul_f32 v176, v176, v16\n"
"v_mul_f32 v177, v177, v37\n"
"v_mfma_f32_16x16x32_bf16 v[56:59], a[152:155], a[136:139], v[56:59]\n"
"ds_read_b128 a[148:151], v5 offset:35840\n"
"v_mul_f32 v7, v178, v178\n"
"v_mfma_f32_16x16x32_bf16 v[60:63], a[152:155], a[140:143], v[60:63]\n"
"v_mul_f32 v16, v179, v179\n"
"s_waitcnt lgkmcnt(4)\n"
"v_mfma_f32_16x16x32_bf16 v[64:67], a[156:159], a[136:139], v[64:67]\n"
"ds_read_b128 a[152:155], v5 offset:36864\n"
"buffer_load_dwordx4 v3, s[16:19], s61 offen offset:2048 sc1 lds\n"
"v_mfma_f32_16x16x32_bf16 v[68:71], a[156:159], a[140:143], v[68:71]\n"
"v_mul_f32 v37, v180, v180\n"
"v_fma_f32 v7, v7, s80, v38\n"
"v_fma_f32 v16, v16, s80, v38\n"
"v_mfma_f32_16x16x32_bf16 v[72:75], a[160:163], a[136:139], v[72:75]\n"
"ds_read_b128 a[156:159], v5 offset:37888\n"
"v_fma_f32 v37, v37, s80, v38\n"
"v_mfma_f32_16x16x32_bf16 v[76:79], a[160:163], a[140:143], v[76:79]\n"
"v_mul_f32 v7, v7, v178\n"
"v_mul_f32 v16, v16, v179\n"
"v_mfma_f32_16x16x32_bf16 v[80:83], a[164:167], a[136:139], v[80:83]\n"
"ds_read_b128 a[160:163], v5 offset:38912\n"
"buffer_load_dwordx4 v3, s[16:19], s61 offen offset:3072 sc1 lds\n"
"v_mfma_f32_16x16x32_bf16 v[84:87], a[164:167], a[140:143], v[84:87]\n"
"s_add_u32 m0, s60, 118784\n"
"v_mul_f32 v37, v37, v180\n"
"s_waitcnt lgkmcnt(4)\n"
"v_mfma_f32_16x16x32_bf16 v[88:91], a[168:171], a[136:139], v[88:91]\n"
"ds_read_b128 a[164:167], v5 offset:39936\n"
"v_mfma_f32_16x16x32_bf16 v[92:95], a[168:171], a[140:143], v[92:95]\n"
"v_exp_f32 v7, v7\n"
"v_mfma_f32_16x16x32_bf16 v[96:99], a[172:175], a[136:139], v[96:99]\n"
"ds_read_b128 a[168:171], v5 offset:40960\n"
"buffer_load_dwordx4 v4, s[16:19], s61 offen offset:0 sc1 lds\n"
"v_mfma_f32_16x16x32_bf16 v[100:103], a[172:175], a[140:143], v[100:103]\n"
"v_exp_f32 v16, v16\n"
"v_mfma_f32_16x16x32_bf16 v[104:107], a[144:147], a[136:139], v[104:107]\n"
"ds_read_b128 a[172:175], v5 offset:41984\n"
"v_mfma_f32_16x16x32_bf16 v[108:111], a[144:147], a[140:143], v[108:111]\n"
"s_waitcnt lgkmcnt(4)\n"
"v_mfma_f32_16x16x32_bf16 v[40:43], a[148:151], a[176:179], v[40:43]\n"
"ds_read_b128 a[144:147], v5 offset:43008\n"
"v_mfma_f32_16x16x32_bf16 v[44:47], a[148:151], a[180:183], v[44:47]\n"
"buffer_load_dwordx4 a[248:251], v4, s[16:19], s89 offen offset:2048 sc1\n"
"v_exp_f32 v37, v37\n"
"v_mfma_f32_16x16x32_bf16 v[48:51], a[152:155], a[176:179], v[48:51]\n"
"ds_read_b128 a[148:151], v5 offset:44032\n"
"v_add_f32 v7, 1.0, v7\n"
"v_mfma_f32_16x16x32_bf16 v[52:55], a[152:155], a[180:183], v[52:55]\n"
"v_add_f32 v16, 1.0, v16\n"
"v_add_f32 v37, 1.0, v37\n"
"v_mfma_f32_16x16x32_bf16 v[56:59], a[156:159], a[176:179], v[56:59]\n"
"v_rcp_f32 v7, v7\n"
"v_mfma_f32_16x16x32_bf16 v[60:63], a[156:159], a[180:183], v[60:63]\n"
"buffer_load_dwordx4 a[0:3], v4, s[16:19], s89 offen offset:3072 sc1\n"
"s_waitcnt lgkmcnt(3)\n"
"v_mfma_f32_16x16x32_bf16 v[64:67], a[160:163], a[176:179], v[64:67]\n"
"v_rcp_f32 v16, v16\n"
"v_mfma_f32_16x16x32_bf16 v[68:71], a[160:163], a[180:183], v[68:71]\n"
"s_waitcnt lgkmcnt(0)\n"
"s_barrier\n"
"v_mfma_f32_16x16x32_bf16 v[72:75], a[164:167], a[176:179], v[72:75]\n"
".Lmk_PCHERE_7:\n"
"s_add_u32 s0, s0, .Lmk_PCHERE_7-.Lmk_PCHERE_6\n"
"s_addc_u32 s1, s1, 0\n"
"s_sub_u32 s2, s2, .Lmk_PCHERE_7-.Lmk_PCHERE_6\n"
"s_max_i32 s2, s2, 0\n"
"buffer_load_dword a255, off, s[0:3], 0\n"
"ds_read_b128 a[152:155], v2 offset:0\n"
"s_add_u32 s61, s61, 36864\n"
"v_mfma_f32_16x16x32_bf16 v[76:79], a[164:167], a[180:183], v[76:79]\n"
"ds_read_b128 a[156:159], v2 offset:9216\n"
"s_add_u32 m0, s60, 73728\n"
"v_mfma_f32_16x16x32_bf16 v[80:83], a[168:171], a[176:179], v[80:83]\n"
"ds_read_b128 a[160:163], v2 offset:18432\n"
"buffer_load_dwordx4 a[4:7], v4, s[16:19], s90 offen offset:2048 sc1\n"
"v_mfma_f32_16x16x32_bf16 v[84:87], a[168:171], a[180:183], v[84:87]\n"
"ds_read_b128 a[164:167], v2 offset:27648\n"
"v_mfma_f32_16x16x32_bf16 v[88:91], a[172:175], a[176:179], v[88:91]\n"
"ds_read_b128 a[168:171], v2 offset:36864\n"
"v_mfma_f32_16x16x32_bf16 v[92:95], a[172:175], a[180:183], v[92:95]\n"
"v_rcp_f32 v37, v37\n"
"v_mul_f32 v178, v178, v7\n"
"v_mfma_f32_16x16x32_bf16 v[96:99], a[144:147], a[176:179], v[96:99]\n"
"ds_read_b128 a[172:175], v2 offset:46080\n"
"buffer_load_dwordx4 a[8:11], v4, s[16:19], s90 offen offset:3072 sc1\n"
"v_mfma_f32_16x16x32_bf16 v[100:103], a[144:147], a[180:183], v[100:103]\n"
"v_mul_f32 v179, v179, v16\n"
"v_mul_f32 v180, v180, v37\n"
"v_mfma_f32_16x16x32_bf16 v[104:107], a[148:151], a[176:179], v[104:107]\n"
"ds_read_b128 a[144:147], v2 offset:55296\n"
"v_mul_f32 v7, v181, v181\n"
"v_mfma_f32_16x16x32_bf16 v[108:111], a[148:151], a[180:183], v[108:111]\n"
"v_mul_f32 v16, v182, v182\n"
"v_mul_f32 v37, v183, v183\n"
"s_waitcnt lgkmcnt(4)\n"
"v_fma_f32 v7, v7, s80, v38\n"
"v_fma_f32 v16, v16, s80, v38\n"
"v_fma_f32 v37, v37, s80, v38\n"
"v_mul_f32 v7, v7, v181\n"
"v_mul_f32 v16, v16, v182\n"
"v_mul_f32 v37, v37, v183\n"
"v_exp_f32 v7, v7\n"
"v_exp_f32 v16, v16\n"
"v_exp_f32 v37, v37\n"
"v_add_f32 v7, 1.0, v7\n"
"v_add_f32 v16, 1.0, v16\n"
"v_add_f32 v37, 1.0, v37\n"
"v_rcp_f32 v7, v7\n"
"v_rcp_f32 v16, v16\n"
"v_rcp_f32 v37, v37\n"
"v_mul_f32 v181, v181, v7\n"
"v_mul_f32 v182, v182, v16\n"
"v_mul_f32 v183, v183, v37\n"
"v_mfma_f32_16x16x32_bf16 v[40:43], a[152:155], a[184:187], v[40:43]\n"
"ds_read_b128 a[148:151], v2 offset:64512\n"
"buffer_load_dwordx4 v2, s[16:19], s61 offen offset:0 sc1 lds\n"
"v_mfma_f32_16x16x32_bf16 v[44:47], a[152:155], a[188:191], v[44:47]\n"
"buffer_load_dwordx4 v2, s[16:19], s61 offen offset:1024 sc1 lds\n"
"v_lshrrev_b32 v20, 4, v1\n"
"v_mfma_f32_16x16x32_bf16 v[40:43], a[156:159], a[192:195], v[40:43]\n"
"ds_read_b128 a[152:155], v2 offset:1024\n"
"buffer_load_dwordx4 v2, s[16:19], s61 offen offset:2048 sc1 lds\n"
"v_mfma_f32_16x16x32_bf16 v[44:47], a[156:159], a[196:199], v[44:47]\n"
"v_and_b32 v21, 15, v1\n"
"v_lshrrev_b32 v22, 1, v20\n"
"v_lshlrev_b32 v22, 8, v22\n"
"v_mfma_f32_16x16x32_bf16 v[40:43], a[160:163], a[200:203], v[40:43]\n"
"ds_read_b128 a[156:159], v2 offset:10240\n"
"v_lshl_add_u32 v22, v21, 4, v22\n"
"v_mfma_f32_16x16x32_bf16 v[44:47], a[160:163], a[204:207], v[44:47]\n"
"v_and_b32 v23, 1, v20\n"
"s_waitcnt vmcnt(21) lgkmcnt(4)\n"
"v_mfma_f32_16x16x32_bf16 v[40:43], a[164:167], a[208:211], v[40:43]\n"
"ds_read_b128 a[160:163], v2 offset:19456\n"
"buffer_load_dwordx4 v2, s[16:19], s61 offen offset:3072 sc1 lds\n"
"v_mfma_f32_16x16x32_bf16 v[44:47], a[164:167], a[212:215], v[44:47]\n"
"s_add_u32 m0, s60, 77824\n"
"v_lshl_add_u32 v22, v23, 10, v22\n"
"v_mfma_f32_16x16x32_bf16 v[40:43], a[168:171], a[216:219], v[40:43]\n"
"ds_read_b128 a[164:167], v2 offset:28672\n"
"v_mfma_f32_16x16x32_bf16 v[44:47], a[168:171], a[220:223], v[44:47]\n"
"v_mfma_f32_16x16x32_bf16 v[40:43], a[172:175], a[224:227], v[40:43]\n"
"ds_read_b128 a[168:171], v2 offset:37888\n"
"v_mfma_f32_16x16x32_bf16 v[44:47], a[172:175], a[228:231], v[44:47]\n"
"buffer_load_dwordx4 v3, s[16:19], s61 offen offset:0 sc1 lds\n"
"s_waitcnt vmcnt(19) lgkmcnt(4)\n"
"v_mfma_f32_16x16x32_bf16 v[40:43], a[144:147], a[232:235], v[40:43]\n"
"ds_read_b128 a[172:175], v2 offset:47104\n"
"v_mfma_f32_16x16x32_bf16 v[44:47], a[144:147], a[236:239], v[44:47]\n"
"v_mfma_f32_16x16x32_bf16 v[40:43], a[148:151], a[240:243], v[40:43]\n"
"ds_read_b128 a[144:147], v2 offset:56320\n"
"v_mfma_f32_16x16x32_bf16 v[44:47], a[148:151], a[244:247], v[44:47]\n"
"v_mfma_f32_16x16x32_bf16 v[48:51], a[152:155], a[184:187], v[48:51]\n"
"ds_read_b128 a[148:151], v5 offset:0\n"
"buffer_load_dwordx4 v3, s[16:19], s61 offen offset:1024 sc1 lds\n"
"v_mfma_f32_16x16x32_bf16 v[52:55], a[152:155], a[188:191], v[52:55]\n"
"s_waitcnt lgkmcnt(4)\n"
"v_mfma_f32_16x16x32_bf16 v[48:51], a[156:159], a[192:195], v[48:51]\n"
"ds_read_b128 a[152:155], v2 offset:2048\n"
"v_mul_f32 v40, v40, v112\n"
"v_mfma_f32_16x16x32_bf16 v[52:55], a[156:159], a[196:199], v[52:55]\n"
"v_mul_f32 v41, v41, v113\n"
"v_mul_f32 v42, v42, v114\n"
"v_mul_f32 v43, v43, v115\n"
"v_mfma_f32_16x16x32_bf16 v[48:51], a[160:163], a[200:203], v[48:51]\n"
"ds_read_b128 a[156:159], v2 offset:11264\n"
"buffer_load_dwordx4 v3, s[16:19], s61 offen offset:2048 sc1 lds\n"
"v_mfma_f32_16x16x32_bf16 v[52:55], a[160:163], a[204:207], v[52:55]\n"
"v_mul_f32 v44, v44, v116\n"
"v_mul_f32 v45, v45, v117\n"
"v_mfma_f32_16x16x32_bf16 v[48:51], a[164:167], a[208:211], v[48:51]\n"
"ds_read_b128 a[160:163], v2 offset:20480\n"
"v_mul_f32 v46, v46, v118\n"
"v_mfma_f32_16x16x32_bf16 v[52:55], a[164:167], a[212:215], v[52:55]\n"
"v_mul_f32 v47, v47, v119\n"
"v_cvt_pk_bf16_f32 v184, v40, v41\n"
"s_waitcnt lgkmcnt(4)\n"
"v_mfma_f32_16x16x32_bf16 v[48:51], a[168:171], a[216:219], v[48:51]\n"
"ds_read_b128 a[164:167], v2 offset:29696\n"
"buffer_load_dwordx4 v3, s[16:19], s61 offen offset:3072 sc1 lds\n"
"v_mfma_f32_16x16x32_bf16 v[52:55], a[168:171], a[220:223], v[52:55]\n"
"s_add_u32 m0, s60, 81920\n"
"v_cvt_pk_bf16_f32 v185, v42, v43\n"
"v_mfma_f32_16x16x32_bf16 v[48:51], a[172:175], a[224:227], v[48:51]\n"
"ds_read_b128 a[168:171], v2 offset:38912\n"
"v_cvt_pk_bf16_f32 v188, v44, v45\n"
"v_mfma_f32_16x16x32_bf16 v[52:55], a[172:175], a[228:231], v[52:55]\n"
"v_cvt_pk_bf16_f32 v189, v46, v47\n"
"v_mfma_f32_16x16x32_bf16 v[48:51], a[144:147], a[232:235], v[48:51]\n"
"ds_read_b128 a[172:175], v2 offset:48128\n"
"buffer_load_dwordx4 v4, s[16:19], s61 offen offset:0 sc1 lds\n"
"v_mfma_f32_16x16x32_bf16 v[52:55], a[144:147], a[236:239], v[52:55]\n"
"s_waitcnt lgkmcnt(4)\n"
"v_mfma_f32_16x16x32_bf16 v[48:51], a[148:151], a[240:243], v[48:51]\n"
"ds_read_b128 a[144:147], v2 offset:57344\n"
"v_mfma_f32_16x16x32_bf16 v[52:55], a[148:151], a[244:247], v[52:55]\n"
"v_mfma_f32_16x16x32_bf16 v[56:59], a[152:155], a[184:187], v[56:59]\n"
"ds_read_b128 a[148:151], v5 offset:1024\n"
"v_mfma_f32_16x16x32_bf16 v[60:63], a[152:155], a[188:191], v[60:63]\n"
"buffer_load_dwordx4 a[12:15], v4, s[16:19], s91 offen offset:2048 sc1\n"
"v_mfma_f32_16x16x32_bf16 v[56:59], a[156:159], a[192:195], v[56:59]\n"
"ds_read_b128 a[152:155], v2 offset:3072\n"
"v_mul_f32 v48, v48, v120\n"
"v_mfma_f32_16x16x32_bf16 v[60:63], a[156:159], a[196:199], v[60:63]\n"
"v_mul_f32 v49, v49, v121\n"
"v_mul_f32 v50, v50, v122\n"
"s_waitcnt lgkmcnt(4)\n"
"v_mfma_f32_16x16x32_bf16 v[56:59], a[160:163], a[200:203], v[56:59]\n"
"ds_read_b128 a[156:159], v2 offset:12288\n"
"v_mul_f32 v51, v51, v123\n"
"v_mfma_f32_16x16x32_bf16 v[60:63], a[160:163], a[204:207], v[60:63]\n"
"v_mul_f32 v52, v52, v124\n"
"v_mul_f32 v53, v53, v125\n"
"v_mfma_f32_16x16x32_bf16 v[56:59], a[164:167], a[208:211], v[56:59]\n"
"ds_read_b128 a[160:163], v2 offset:21504\n"
"v_mul_f32 v54, v54, v126\n"
"v_mfma_f32_16x16x32_bf16 v[60:63], a[164:167], a[212:215], v[60:63]\n"
"v_mul_f32 v55, v55, v127\n"
"v_cvt_pk_bf16_f32 v186, v48, v49\n"
"v_cvt_pk_bf16_f32 v187, v50, v51\n"
"v_mfma_f32_16x16x32_bf16 v[56:59], a[168:171], a[216:219], v[56:59]\n"
"ds_read_b128 a[164:167], v2 offset:30720\n"
"v_cvt_pk_bf16_f32 v190, v52, v53\n"
"v_mfma_f32_16x16x32_bf16 v[60:63], a[168:171], a[220:223], v[60:63]\n"
"v_cvt_pk_bf16_f32 v191, v54, v55\n"
"s_waitcnt lgkmcnt(4)\n"
"v_mfma_f32_16x16x32_bf16 v[56:59], a[172:175], a[224:227], v[56:59]\n"
"ds_read_b128 a[168:171], v2 offset:39936\n"
"buffer_store_dwordx4 v[184:187], v2, s[16:19], s89 offen offset:0 sc1\n"
"v_mfma_f32_16x16x32_bf16 v[60:63], a[172:175], a[228:231], v[60:63]\n"
"v_mfma_f32_16x16x32_bf16 v[56:59], a[144:147], a[232:235], v[56:59]\n"
"ds_read_b128 a[172:175], v2 offset:49152\n"
"buffer_store_dwordx4 v[188:191], v2, s[16:19], s89 offen offset:1024 sc1\n"
"v_mfma_f32_16x16x32_bf16 v[60:63], a[144:147], a[236:239], v[60:63]\n"
"v_mfma_f32_16x16x32_bf16 v[56:59], a[148:151], a[240:243], v[56:59]\n"
"ds_read_b128 a[144:147], v2 offset:58368\n"
"v_mfma_f32_16x16x32_bf16 v[60:63], a[148:151], a[244:247], v[60:63]\n"
"s_waitcnt lgkmcnt(4)\n"
"v_mfma_f32_16x16x32_bf16 v[64:67], a[152:155], a[184:187], v[64:67]\n"
"ds_read_b128 a[148:151], v5 offset:2048\n"
"v_mfma_f32_16x16x32_bf16 v[68:71], a[152:155], a[188:191], v[68:71]\n"
"v_mfma_f32_16x16x32_bf16 v[64:67], a[156:159], a[192:195], v[64:67]\n"
"ds_read_b128 a[152:155], v2 offset:4096\n"
"v_mul_f32 v56, v56, v128\n"
"v_mfma_f32_16x16x32_bf16 v[68:71], a[156:159], a[196:199], v[68:71]\n"
"v_mul_f32 v57, v57, v129\n"
"v_mul_f32 v58, v58, v130\n"
"v_mul_f32 v59, v59, v131\n"
"v_mfma_f32_16x16x32_bf16 v[64:67], a[160:163], a[200:203], v[64:67]\n"
"ds_read_b128 a[156:159], v2 offset:13312\n"
"v_mul_f32 v60, v60, v132\n"
"v_mfma_f32_16x16x32_bf16 v[68:71], a[160:163], a[204:207], v[68:71]\n"
"v_mul_f32 v61, v61, v133\n"
"s_waitcnt lgkmcnt(4)\n"
"v_mfma_f32_16x16x32_bf16 v[64:67], a[164:167], a[208:211], v[64:67]\n"
"ds_read_b128 a[160:163], v2 offset:22528\n"
"v_mul_f32 v62, v62, v134\n"
"v_mfma_f32_16x16x32_bf16 v[68:71], a[164:167], a[212:215], v[68:71]\n"
"v_mul_f32 v63, v63, v135\n"
"v_cvt_pk_bf16_f32 v192, v56, v57\n"
"v_cvt_pk_bf16_f32 v193, v58, v59\n"
"v_mfma_f32_16x16x32_bf16 v[64:67], a[168:171], a[216:219], v[64:67]\n"
"ds_read_b128 a[164:167], v2 offset:31744\n"
"v_cvt_pk_bf16_f32 v196, v60, v61\n"
"v_mfma_f32_16x16x32_bf16 v[68:71], a[168:171], a[220:223], v[68:71]\n"
"v_cvt_pk_bf16_f32 v197, v62, v63\n"
"v_mfma_f32_16x16x32_bf16 v[64:67], a[172:175], a[224:227], v[64:67]\n"
"ds_read_b128 a[168:171], v2 offset:40960\n"
"buffer_load_dwordx4 a[16:19], v4, s[16:19], s91 offen offset:3072 sc1\n"
"v_mfma_f32_16x16x32_bf16 v[68:71], a[172:175], a[228:231], v[68:71]\n"
"buffer_load_dwordx4 a[20:23], v4, s[16:19], s92 offen offset:2048 sc1\n"
"s_waitcnt lgkmcnt(4)\n"
"v_mfma_f32_16x16x32_bf16 v[64:67], a[144:147], a[232:235], v[64:67]\n"
"ds_read_b128 a[172:175], v2 offset:50176\n"
"buffer_load_dwordx4 a[24:27], v4, s[16:19], s92 offen offset:3072 sc1\n"
"v_mfma_f32_16x16x32_bf16 v[68:71], a[144:147], a[236:239], v[68:71]\n"
"v_mfma_f32_16x16x32_bf16 v[64:67], a[148:151], a[240:243], v[64:67]\n"
"ds_read_b128 a[144:147], v2 offset:59392\n"
"v_mfma_f32_16x16x32_bf16 v[68:71], a[148:151], a[244:247], v[68:71]\n"
"v_mfma_f32_16x16x32_bf16 v[72:75], a[152:155], a[184:187], v[72:75]\n"
"ds_read_b128 a[148:151], v5 offset:3072\n"
"v_mfma_f32_16x16x32_bf16 v[76:79], a[152:155], a[188:191], v[76:79]\n"
"s_waitcnt lgkmcnt(4)\n"
"v_mfma_f32_16x16x32_bf16 v[72:75], a[156:159], a[192:195], v[72:75]\n"
"ds_read_b128 a[152:155], v2 offset:5120\n"
"v_mul_f32 v64, v64, v136\n"
"v_mfma_f32_16x16x32_bf16 v[76:79], a[156:159], a[196:199], v[76:79]\n"
"v_mul_f32 v65, v65, v137\n"
"v_mul_f32 v66, v66, v138\n"
"v_mul_f32 v67, v67, v139\n"
"v_mfma_f32_16x16x32_bf16 v[72:75], a[160:163], a[200:203], v[72:75]\n"
"ds_read_b128 a[156:159], v2 offset:14336\n"
"v_mul_f32 v68, v68, v140\n"
"v_mfma_f32_16x16x32_bf16 v[76:79], a[160:163], a[204:207], v[76:79]\n"
"v_mul_f32 v69, v69, v141\n"
"v_mul_f32 v70, v70, v142\n"
"v_mfma_f32_16x16x32_bf16 v[72:75], a[164:167], a[208:211], v[72:75]\n"
"ds_read_b128 a[160:163], v2 offset:23552\n"
"v_mul_f32 v71, v71, v143\n"
"v_mfma_f32_16x16x32_bf16 v[76:79], a[164:167], a[212:215], v[76:79]\n"
"v_cvt_pk_bf16_f32 v194, v64, v65\n"
"v_cvt_pk_bf16_f32 v195, v66, v67\n"
"s_waitcnt lgkmcnt(4)\n"
"v_mfma_f32_16x16x32_bf16 v[72:75], a[168:171], a[216:219], v[72:75]\n"
"ds_read_b128 a[164:167], v2 offset:32768\n"
"v_cvt_pk_bf16_f32 v198, v68, v69\n"
"v_mfma_f32_16x16x32_bf16 v[76:79], a[168:171], a[220:223], v[76:79]\n"
"v_cvt_pk_bf16_f32 v199, v70, v71\n"
"v_mfma_f32_16x16x32_bf16 v[72:75], a[172:175], a[224:227], v[72:75]\n"
"ds_read_b128 a[168:171], v2 offset:41984\n"
"buffer_store_dwordx4 v[192:195], v2, s[16:19], s89 offen offset:2048 sc1\n"
"v_mfma_f32_16x16x32_bf16 v[76:79], a[172:175], a[228:231], v[76:79]\n"
"v_mfma_f32_16x16x32_bf16 v[72:75], a[144:147], a[232:235], v[72:75]\n"
"ds_read_b128 a[172:175], v2 offset:51200\n"
"buffer_store_dwordx4 v[196:199], v2, s[16:19], s89 offen offset:3072 sc1\n"
"v_mfma_f32_16x16x32_bf16 v[76:79], a[144:147], a[236:239], v[76:79]\n"
"s_add_u32 s71, s67, 2621440\n"
"s_waitcnt lgkmcnt(4)\n"
"v_mfma_f32_16x16x32_bf16 v[72:75], a[148:151], a[240:243], v[72:75]\n"
"ds_read_b128 a[144:147], v2 offset:60416\n"
"s_add_u32 s72, s66, 2621440\n"
"v_mfma_f32_16x16x32_bf16 v[76:79], a[148:151], a[244:247], v[76:79]\n"
"v_mfma_f32_16x16x32_bf16 v[80:83], a[152:155], a[184:187], v[80:83]\n"
"ds_read_b128 a[148:151], v5 offset:4096\n"
"v_mfma_f32_16x16x32_bf16 v[84:87], a[152:155], a[188:191], v[84:87]\n"
"v_mfma_f32_16x16x32_bf16 v[80:83], a[156:159], a[192:195], v[80:83]\n"
"ds_read_b128 a[152:155], v2 offset:6144\n"
"v_mul_f32 v72, v72, v144\n"
"v_mfma_f32_16x16x32_bf16 v[84:87], a[156:159], a[196:199], v[84:87]\n"
"v_mul_f32 v73, v73, v145\n"
"v_mul_f32 v74, v74, v146\n"
"s_waitcnt lgkmcnt(4)\n"
"v_mfma_f32_16x16x32_bf16 v[80:83], a[160:163], a[200:203], v[80:83]\n"
"ds_read_b128 a[156:159], v2 offset:15360\n"
"v_mul_f32 v75, v75, v147\n"
"v_mfma_f32_16x16x32_bf16 v[84:87], a[160:163], a[204:207], v[84:87]\n"
"v_mul_f32 v76, v76, v148\n"
"v_mul_f32 v77, v77, v149\n"
"v_mfma_f32_16x16x32_bf16 v[80:83], a[164:167], a[208:211], v[80:83]\n"
"ds_read_b128 a[160:163], v2 offset:24576\n"
"v_mul_f32 v78, v78, v150\n"
"v_mfma_f32_16x16x32_bf16 v[84:87], a[164:167], a[212:215], v[84:87]\n"
"v_mul_f32 v79, v79, v151\n"
"v_cvt_pk_bf16_f32 v200, v72, v73\n"
"v_cvt_pk_bf16_f32 v201, v74, v75\n"
"v_mfma_f32_16x16x32_bf16 v[80:83], a[168:171], a[216:219], v[80:83]\n"
"ds_read_b128 a[164:167], v2 offset:33792\n"
"v_cvt_pk_bf16_f32 v204, v76, v77\n"
"v_mfma_f32_16x16x32_bf16 v[84:87], a[168:171], a[220:223], v[84:87]\n"
"v_cvt_pk_bf16_f32 v205, v78, v79\n"
"s_waitcnt lgkmcnt(4)\n"
"v_mfma_f32_16x16x32_bf16 v[80:83], a[172:175], a[224:227], v[80:83]\n"
"ds_read_b128 a[168:171], v2 offset:43008\n"
"v_mfma_f32_16x16x32_bf16 v[84:87], a[172:175], a[228:231], v[84:87]\n"
"v_mfma_f32_16x16x32_bf16 v[80:83], a[144:147], a[232:235], v[80:83]\n"
"ds_read_b128 a[172:175], v2 offset:52224\n"
"v_mfma_f32_16x16x32_bf16 v[84:87], a[144:147], a[236:239], v[84:87]\n"
"v_mfma_f32_16x16x32_bf16 v[80:83], a[148:151], a[240:243], v[80:83]\n"
"ds_read_b128 a[144:147], v2 offset:61440\n"
"v_mfma_f32_16x16x32_bf16 v[84:87], a[148:151], a[244:247], v[84:87]\n"
"s_waitcnt lgkmcnt(4)\n"
"v_mfma_f32_16x16x32_bf16 v[88:91], a[152:155], a[184:187], v[88:91]\n"
"ds_read_b128 a[148:151], v5 offset:5120\n"
"v_mfma_f32_16x16x32_bf16 v[92:95], a[152:155], a[188:191], v[92:95]\n"
"v_mfma_f32_16x16x32_bf16 v[88:91], a[156:159], a[192:195], v[88:91]\n"
"ds_read_b128 a[152:155], v2 offset:7168\n"
"v_mul_f32 v80, v80, v152\n"
"v_mfma_f32_16x16x32_bf16 v[92:95], a[156:159], a[196:199], v[92:95]\n"
"v_mul_f32 v81, v81, v153\n"
"v_mul_f32 v82, v82, v154\n"
"v_mul_f32 v83, v83, v155\n"
"v_mfma_f32_16x16x32_bf16 v[88:91], a[160:163], a[200:203], v[88:91]\n"
"ds_read_b128 a[156:159], v2 offset:16384\n"
"v_mul_f32 v84, v84, v156\n"
"v_mfma_f32_16x16x32_bf16 v[92:95], a[160:163], a[204:207], v[92:95]\n"
"v_mul_f32 v85, v85, v157\n"
"s_waitcnt lgkmcnt(4)\n"
"v_mfma_f32_16x16x32_bf16 v[88:91], a[164:167], a[208:211], v[88:91]\n"
"ds_read_b128 a[160:163], v2 offset:25600\n"
"v_mul_f32 v86, v86, v158\n"
"v_mfma_f32_16x16x32_bf16 v[92:95], a[164:167], a[212:215], v[92:95]\n"
"v_mul_f32 v87, v87, v159\n"
"v_cvt_pk_bf16_f32 v202, v80, v81\n"
"v_cvt_pk_bf16_f32 v203, v82, v83\n"
"v_mfma_f32_16x16x32_bf16 v[88:91], a[168:171], a[216:219], v[88:91]\n"
"ds_read_b128 a[164:167], v2 offset:34816\n"
"v_cvt_pk_bf16_f32 v206, v84, v85\n"
"v_mfma_f32_16x16x32_bf16 v[92:95], a[168:171], a[220:223], v[92:95]\n"
"v_cvt_pk_bf16_f32 v207, v86, v87\n"
"v_mfma_f32_16x16x32_bf16 v[88:91], a[172:175], a[224:227], v[88:91]\n"
"ds_read_b128 a[168:171], v2 offset:44032\n"
"buffer_store_dwordx4 v[200:203], v3, s[16:19], s89 offen offset:0 sc1\n"
"v_mfma_f32_16x16x32_bf16 v[92:95], a[172:175], a[228:231], v[92:95]\n"
"s_waitcnt lgkmcnt(4)\n"
"v_mfma_f32_16x16x32_bf16 v[88:91], a[144:147], a[232:235], v[88:91]\n"
"ds_read_b128 a[172:175], v2 offset:53248\n"
"buffer_store_dwordx4 v[204:207], v3, s[16:19], s89 offen offset:1024 sc1\n"
"v_mfma_f32_16x16x32_bf16 v[92:95], a[144:147], a[236:239], v[92:95]\n"
"v_mfma_f32_16x16x32_bf16 v[88:91], a[148:151], a[240:243], v[88:91]\n"
"ds_read_b128 a[144:147], v2 offset:62464\n"
"v_mfma_f32_16x16x32_bf16 v[92:95], a[148:151], a[244:247], v[92:95]\n"
"v_mfma_f32_16x16x32_bf16 v[96:99], a[152:155], a[184:187], v[96:99]\n"
"ds_read_b128 a[148:151], v5 offset:6144\n"
"v_mfma_f32_16x16x32_bf16 v[100:103], a[152:155], a[188:191], v[100:103]\n"
"s_waitcnt lgkmcnt(4)\n"
"v_mfma_f32_16x16x32_bf16 v[96:99], a[156:159], a[192:195], v[96:99]\n"
"ds_read_b128 a[152:155], v2 offset:8192\n"
"v_mul_f32 v88, v88, v160\n"
"v_mfma_f32_16x16x32_bf16 v[100:103], a[156:159], a[196:199], v[100:103]\n"
"v_mul_f32 v89, v89, v161\n"
"v_mul_f32 v90, v90, v162\n"
"v_mul_f32 v91, v91, v163\n"
"v_mfma_f32_16x16x32_bf16 v[96:99], a[160:163], a[200:203], v[96:99]\n"
"ds_read_b128 a[156:159], v2 offset:17408\n"
"v_mul_f32 v92, v92, v164\n"
"v_mfma_f32_16x16x32_bf16 v[100:103], a[160:163], a[204:207], v[100:103]\n"
"v_mul_f32 v93, v93, v165\n"
"v_mul_f32 v94, v94, v166\n"
"v_mfma_f32_16x16x32_bf16 v[96:99], a[164:167], a[208:211], v[96:99]\n"
"ds_read_b128 a[160:163], v2 offset:26624\n"
"v_mul_f32 v95, v95, v167\n"
"v_mfma_f32_16x16x32_bf16 v[100:103], a[164:167], a[212:215], v[100:103]\n"
"v_cvt_pk_bf16_f32 v208, v88, v89\n"
"v_cvt_pk_bf16_f32 v209, v90, v91\n"
"s_waitcnt lgkmcnt(4)\n"
"v_mfma_f32_16x16x32_bf16 v[96:99], a[168:171], a[216:219], v[96:99]\n"
"ds_read_b128 a[164:167], v2 offset:35840\n"
"v_cvt_pk_bf16_f32 v212, v92, v93\n"
"v_mfma_f32_16x16x32_bf16 v[100:103], a[168:171], a[220:223], v[100:103]\n"
"v_cvt_pk_bf16_f32 v213, v94, v95\n"
"v_mfma_f32_16x16x32_bf16 v[96:99], a[172:175], a[224:227], v[96:99]\n"
"ds_read_b128 a[168:171], v2 offset:45056\n"
"v_mfma_f32_16x16x32_bf16 v[100:103], a[172:175], a[228:231], v[100:103]\n"
"v_mfma_f32_16x16x32_bf16 v[96:99], a[144:147], a[232:235], v[96:99]\n"
"ds_read_b128 a[172:175], v2 offset:54272\n"
"v_mfma_f32_16x16x32_bf16 v[100:103], a[144:147], a[236:239], v[100:103]\n"
"s_waitcnt lgkmcnt(4)\n"
"v_mfma_f32_16x16x32_bf16 v[96:99], a[148:151], a[240:243], v[96:99]\n"
"ds_read_b128 a[144:147], v2 offset:63488\n"
"v_mfma_f32_16x16x32_bf16 v[100:103], a[148:151], a[244:247], v[100:103]\n"
"v_mfma_f32_16x16x32_bf16 v[104:107], a[152:155], a[184:187], v[104:107]\n"
"ds_read_b128 a[148:151], v5 offset:7168\n"
"v_mfma_f32_16x16x32_bf16 v[108:111], a[152:155], a[188:191], v[108:111]\n"
"v_mfma_f32_16x16x32_bf16 v[104:107], a[156:159], a[192:195], v[104:107]\n"
"s_nop 1\n"
"v_mul_f32 v96, v96, v168\n"
"v_mul_f32 v97, v97, v169\n"
"v_mfma_f32_16x16x32_bf16 v[108:111], a[156:159], a[196:199], v[108:111]\n"
"v_mul_f32 v98, v98, v170\n"
"v_mul_f32 v99, v99, v171\n"
"s_waitcnt lgkmcnt(3)\n"
"v_mfma_f32_16x16x32_bf16 v[104:107], a[160:163], a[200:203], v[104:107]\n"
"v_mul_f32 v100, v100, v172\n"
"v_mul_f32 v101, v101, v173\n"
"v_mfma_f32_16x16x32_bf16 v[108:111], a[160:163], a[204:207], v[108:111]\n"
"s_waitcnt vmcnt(24)\n"
"s_waitcnt lgkmcnt(0)\n"
"s_barrier\n"
"v_mfma_f32_16x16x32_bf16 v[104:107], a[164:167], a[208:211], v[104:107]\n"
"ds_read_b128 a[152:155], v5 offset:45056\n"
"s_add_u32 s61, s61, 36864\n"
"v_mfma_f32_16x16x32_bf16 v[108:111], a[164:167], a[212:215], v[108:111]\n"
"ds_read_b128 a[156:159], v5 offset:46080\n"
"s_add_u32 m0, s60, 0\n"
"v_mul_f32 v102, v102, v174\n"
"v_mfma_f32_16x16x32_bf16 v[104:107], a[168:171], a[216:219], v[104:107]\n"
"ds_read_b128 a[160:163], v5 offset:47104\n"
"buffer_load_dwordx4 v2, s[16:19], s61 offen offset:0 sc1 lds\n"
"v_mfma_f32_16x16x32_bf16 v[108:111], a[168:171], a[220:223], v[108:111]\n"
"ds_read_b128 a[164:167], v5 offset:48128\n"
"v_mfma_f32_16x16x32_bf16 v[104:107], a[172:175], a[224:227], v[104:107]\n"
"ds_read_b128 a[168:171], v5 offset:49152\n"
"buffer_load_dwordx4 v2, s[16:19], s61 offen offset:1024 sc1 lds\n"
"v_mfma_f32_16x16x32_bf16 v[108:111], a[172:175], a[228:231], v[108:111]\n"
"buffer_load_dwordx4 v2, s[16:19], s61 offen offset:2048 sc1 lds\n"
"buffer_load_dwordx4 v2, s[16:19], s61 offen offset:3072 sc1 lds\n"
"v_mul_f32 v103, v103, v175\n"
"v_mfma_f32_16x16x32_bf16 v[104:107], a[144:147], a[232:235], v[104:107]\n"
"ds_read_b128 a[172:175], v5 offset:50176\n"
"s_add_u32 m0, s60, 4096\n"
"v_mfma_f32_16x16x32_bf16 v[108:111], a[144:147], a[236:239], v[108:111]\n"
"buffer_load_dwordx4 v3, s[16:19], s61 offen offset:0 sc1 lds\n"
"v_cvt_pk_bf16_f32 v210, v96, v97\n"
"v_mfma_f32_16x16x32_bf16 v[104:107], a[148:151], a[240:243], v[104:107]\n"
"ds_read_b128 a[144:147], v5 offset:51200\n"
"v_cvt_pk_bf16_f32 v211, v98, v99\n"
"v_mfma_f32_16x16x32_bf16 v[108:111], a[148:151], a[244:247], v[108:111]\n"
"v_cvt_pk_bf16_f32 v214, v100, v101\n"
"v_cvt_pk_bf16_f32 v215, v102, v103\n"
"s_waitcnt lgkmcnt(4)\n"
"v_mfma_f32_16x16x32_bf16 v[112:115], a[152:155], v[184:187], 0\n"
"ds_read_b128 a[148:151], v5 offset:52224\n"
"buffer_load_dwordx4 v3, s[16:19], s61 offen offset:1024 sc1 lds\n"
"v_mfma_f32_16x16x32_bf16 v[116:119], a[152:155], v[188:191], 0\n"
"buffer_store_dwordx4 v[208:211], v3, s[16:19], s89 offen offset:2048 sc1\n"
"v_mfma_f32_16x16x32_bf16 v[120:123], a[156:159], v[184:187], 0\n"
"ds_read_b128 a[152:155], v5 offset:53248\n"
"v_mfma_f32_16x16x32_bf16 v[124:127], a[156:159], v[188:191], 0\n"
"buffer_store_dwordx4 v[212:215], v3, s[16:19], s89 offen offset:3072 sc1\n"
"v_mfma_f32_16x16x32_bf16 v[128:131], a[160:163], v[184:187], 0\n"
"ds_read_b128 a[156:159], v5 offset:54272\n"
"s_waitcnt vmcnt(10)\n"
"v_mfma_f32_16x16x32_bf16 v[132:135], a[160:163], v[188:191], 0\n"
"s_waitcnt lgkmcnt(4)\n"
"v_mfma_f32_16x16x32_bf16 v[136:139], a[164:167], v[184:187], 0\n"
"ds_read_b128 a[160:163], v5 offset:55296\n"
"v_mul_f32 v104, v104, v176\n"
"v_mfma_f32_16x16x32_bf16 v[140:143], a[164:167], v[188:191], 0\n"
"v_mul_f32 v105, v105, v177\n"
"v_mul_f32 v106, v106, v178\n"
"v_mul_f32 v107, v107, v179\n"
"v_mfma_f32_16x16x32_bf16 v[144:147], a[168:171], v[184:187], 0\n"
"ds_read_b128 a[164:167], v5 offset:56320\n"
"v_mul_f32 v108, v108, v180\n"
"v_mfma_f32_16x16x32_bf16 v[148:151], a[168:171], v[188:191], 0\n"
"v_mul_f32 v109, v109, v181\n"
"v_mul_f32 v110, v110, v182\n"
"v_mfma_f32_16x16x32_bf16 v[152:155], a[172:175], v[184:187], 0\n"
"ds_read_b128 a[168:171], v5 offset:57344\n"
"v_mul_f32 v111, v111, v183\n"
"v_mfma_f32_16x16x32_bf16 v[156:159], a[172:175], v[188:191], 0\n"
"v_cvt_pk_bf16_f32 v216, v104, v105\n"
"v_cvt_pk_bf16_f32 v217, v106, v107\n"
"s_waitcnt lgkmcnt(4)\n"
"v_mfma_f32_16x16x32_bf16 v[160:163], a[144:147], v[184:187], 0\n"
"ds_read_b128 a[172:175], v5 offset:58368\n"
"v_cvt_pk_bf16_f32 v218, v108, v109\n"
"v_mfma_f32_16x16x32_bf16 v[164:167], a[144:147], v[188:191], 0\n"
"v_cvt_pk_bf16_f32 v219, v110, v111\n"
"v_mfma_f32_16x16x32_bf16 v[168:171], a[148:151], v[184:187], 0\n"
"ds_read_b128 a[144:147], v5 offset:59392\n"
"buffer_load_dwordx4 v3, s[16:19], s61 offen offset:2048 sc1 lds\n"
"v_mfma_f32_16x16x32_bf16 v[172:175], a[148:151], v[188:191], 0\n"
"v_permlane16_swap_b32 v216, v218\n"
"v_mfma_f32_16x16x32_bf16 v[176:179], a[152:155], v[184:187], 0\n"
"ds_read_b128 a[148:151], v5 offset:60416\n"
"v_mfma_f32_16x16x32_bf16 v[180:183], a[152:155], v[188:191], 0\n"
"s_waitcnt lgkmcnt(4)\n"
"v_mfma_f32_16x16x32_bf16 v[112:115], a[156:159], v[192:195], v[112:115]\n"
"ds_read_b128 a[152:155], v5 offset:61440\n"
"buffer_load_dwordx4 v3, s[16:19], s61 offen offset:3072 sc1 lds\n"
"v_mfma_f32_16x16x32_bf16 v[116:119], a[156:159], v[196:199], v[116:119]\n"
"s_add_u32 m0, s60, 8192\n"
"v_permlane16_swap_b32 v217, v219\n"
"v_mfma_f32_16x16x32_bf16 v[120:123], a[160:163], v[192:195], v[120:123]\n"
"ds_read_b128 a[156:159], v5 offset:62464\n"
"buffer_store_dwordx4 v[216:219], v22, s[16:19], s69 offen sc1\n"
"v_mfma_f32_16x16x32_bf16 v[124:127], a[160:163], v[196:199], v[124:127]\n"
"s_mov_b64 exec, 0xff\n"
"buffer_store_dwordx4 v[16:19], v2, s[16:19], s71 offen sc1\n"
"s_mov_b64 exec, -1\n"
"v_mfma_f32_16x16x32_bf16 v[128:131], a[164:167], v[192:195], v[128:131]\n"
"ds_read_b128 a[160:163], v5 offset:63488\n"
"s_add_u32 s73, s67, 2752512\n"
"v_mfma_f32_16x16x32_bf16 v[132:135], a[164:167], v[196:199], v[132:135]\n"
"s_add_u32 s74, s66, 2752512\n"
"v_and_b32 v27, 1, v21\n"
"s_waitcnt lgkmcnt(4)\n"
"v_mfma_f32_16x16x32_bf16 v[136:139], a[168:171], v[192:195], v[136:139]\n"
"ds_read_b128 a[164:167], v5 offset:64512\n"
"v_lshlrev_b32 v27, 4, v27\n"
"v_mfma_f32_16x16x32_bf16 v[140:143], a[168:171], v[196:199], v[140:143]\n"
"v_mov_b32 v28, 0x3f80\n"
"v_lshlrev_b32 v27, v27, v28\n"
"v_mfma_f32_16x16x32_bf16 v[144:147], a[172:175], v[192:195], v[144:147]\n"
"ds_read_b128 a[168:171], v0 offset:0\n"
"v_lshrrev_b32 v28, 1, v21\n"
"v_mfma_f32_16x16x32_bf16 v[148:151], a[172:175], v[196:199], v[148:151]\n"
"v_cmp_eq_u32 s[82:83], 0, v28\n"
"v_cmp_eq_u32 s[84:85], 1, v28\n"
"v_mfma_f32_16x16x32_bf16 v[152:155], a[144:147], v[192:195], v[152:155]\n"
"ds_read_b128 a[172:175], v0 offset:1024\n"
"v_cndmask_b32 v8, 0, v27, s[82:83]\n"
"v_mfma_f32_16x16x32_bf16 v[156:159], a[144:147], v[196:199], v[156:159]\n"
"v_cndmask_b32 v9, 0, v27, s[84:85]\n"
"s_waitcnt lgkmcnt(4)\n"
"v_mfma_f32_16x16x32_bf16 v[160:163], a[148:151], v[192:195], v[160:163]\n"
"ds_read_b128 a[144:147], v0 offset:2048\n"
"buffer_load_dwordx4 v4, s[16:19], s61 offen offset:0 sc1 lds\n"
"v_mfma_f32_16x16x32_bf16 v[164:167], a[148:151], v[196:199], v[164:167]\n"
"s_add_u32 s61, s61, 36864\n"
"s_add_u32 m0, s60, 36864\n"
"v_cmp_eq_u32 s[82:83], 2, v28\n"
"v_mfma_f32_16x16x32_bf16 v[168:171], a[152:155], v[192:195], v[168:171]\n"
"ds_read_b128 a[148:151], v0 offset:3072\n"
"v_cmp_eq_u32 s[84:85], 3, v28\n"
"v_mfma_f32_16x16x32_bf16 v[172:175], a[152:155], v[196:199], v[172:175]\n"
"v_cndmask_b32 v10, 0, v27, s[82:83]\n"
"v_cndmask_b32 v11, 0, v27, s[84:85]\n"
"v_mfma_f32_16x16x32_bf16 v[176:179], a[156:159], v[192:195], v[176:179]\n"
"ds_read_b128 a[152:155], v0 offset:4096\n"
"buffer_load_dwordx4 v2, s[16:19], s61 offen offset:0 sc1 lds\n"
"v_mfma_f32_16x16x32_bf16 v[180:183], a[156:159], v[196:199], v[180:183]\n"
"v_cmp_eq_u32 s[82:83], 4, v28\n"
"v_cmp_eq_u32 s[84:85], 5, v28\n"
"s_waitcnt lgkmcnt(4)\n"
"v_mfma_f32_16x16x32_bf16 v[112:115], a[160:163], v[200:203], v[112:115]\n"
"ds_read_b128 a[156:159], v0 offset:5120\n"
"v_cndmask_b32 v12, 0, v27, s[82:83]\n"
"v_mfma_f32_16x16x32_bf16 v[116:119], a[160:163], v[204:207], v[116:119]\n"
"v_cndmask_b32 v13, 0, v27, s[84:85]\n"
"v_cmp_eq_u32 s[82:83], 6, v28\n"
"v_mfma_f32_16x16x32_bf16 v[120:123], a[164:167], v[200:203], v[120:123]\n"
"ds_read_b128 a[160:163], v0 offset:6144\n"
"buffer_load_dwordx4 v2, s[16:19], s61 offen offset:1024 sc1 lds\n"
"v_mfma_f32_16x16x32_bf16 v[124:127], a[164:167], v[204:207], v[124:127]\n"
"v_cmp_eq_u32 s[84:85], 7, v28\n"
"v_mfma_f32_16x16x32_bf16 v[128:131], a[168:171], v[200:203], v[128:131]\n"
"ds_read_b128 a[164:167], v0 offset:7168\n"
"v_cndmask_b32 v14, 0, v27, s[82:83]\n"
"v_mfma_f32_16x16x32_bf16 v[132:135], a[168:171], v[204:207], v[132:135]\n"
"buffer_load_dwordx4 v2, s[16:19], s61 offen offset:2048 sc1 lds\n"
"s_waitcnt lgkmcnt(4)\n"
"v_mfma_f32_16x16x32_bf16 v[136:139], a[172:175], v[200:203], v[136:139]\n"
"ds_read_b128 a[168:171], v0 offset:8192\n"
"v_cndmask_b32 v15, 0, v27, s[84:85]\n"
"v_mfma_f32_16x16x32_bf16 v[140:143], a[172:175], v[204:207], v[140:143]\n"
"v_mov_b32 v28, v8\n"
"v_mov_b32 v29, v10\n"
"v_mov_b32 v30, v12\n"
"v_mfma_f32_16x16x32_bf16 v[144:147], a[144:147], v[200:203], v[144:147]\n"
"ds_read_b128 a[172:175], v0 offset:9216\n"
"v_mov_b32 v31, v14\n"
"v_mfma_f32_16x16x32_bf16 v[148:151], a[144:147], v[204:207], v[148:151]\n"
"buffer_load_dwordx4 v2, s[16:19], s61 offen offset:3072 sc1 lds\n"
"s_add_u32 m0, s60, 40960\n"
"v_mfma_f32_16x16x32_bf16 v[152:155], a[148:151], v[200:203], v[152:155]\n"
"ds_read_b128 a[144:147], v0 offset:10240\n"
"v_and_b32 v27, 3, v21\n"
"v_mfma_f32_16x16x32_bf16 v[156:159], a[148:151], v[204:207], v[156:159]\n"
"v_lshrrev_b32 v32, 2, v21\n"
"v_mul_u32_u24 v24, 9216, v32\n"
"s_waitcnt lgkmcnt(4)\n"
"v_mfma_f32_16x16x32_bf16 v[160:163], a[152:155], v[200:203], v[160:163]\n"
"ds_read_b128 a[148:151], v0 offset:11264\n"
"buffer_load_dwordx4 v3, s[16:19], s61 offen offset:0 sc1 lds\n"
"v_mfma_f32_16x16x32_bf16 v[164:167], a[152:155], v[204:207], v[164:167]\n"
"v_lshl_add_u32 v24, v20, 4, v24\n"
"v_lshl_add_u32 v24, v27, 6, v24\n"
"v_mfma_f32_16x16x32_bf16 v[168:171], a[156:159], v[200:203], v[168:171]\n"
"ds_read_b128 a[152:155], v0 offset:12288\n"
"v_mul_u32_u24 v32, 1152, v32\n"
"v_mfma_f32_16x16x32_bf16 v[172:175], a[156:159], v[204:207], v[172:175]\n"
"v_lshl_add_u32 v27, v27, 5, v32\n"
"v_lshl_add_u32 v25, v20, 3, v27\n"
"v_lshl_add_u32 v26, v23, 4, v27\n"
"v_mfma_f32_16x16x32_bf16 v[176:179], a[160:163], v[200:203], v[176:179]\n"
"ds_read_b128 a[156:159], v0 offset:13312\n"
"buffer_load_dwordx4 v3, s[16:19], s61 offen offset:1024 sc1 lds\n"
"v_mfma_f32_16x16x32_bf16 v[180:183], a[160:163], v[204:207], v[180:183]\n"
"v_lshrrev_b32 v27, 1, v20\n"
"s_waitcnt lgkmcnt(4)\n"
"v_mfma_f32_16x16x32_bf16 v[112:115], a[164:167], v[208:211], v[112:115]\n"
"ds_read_b128 a[160:163], v0 offset:14336\n"
"v_mul_u32_u24 v27, 4608, v27\n"
"v_mfma_f32_16x16x32_bf16 v[116:119], a[164:167], v[212:215], v[116:119]\n"
"v_add_u32 v26, v26, v27\n"
"s_lshl_b32 s20, s63, 2\n"
"s_add_u32 s21, s20, s56\n"
"v_mfma_f32_16x16x32_bf16 v[120:123], a[168:171], v[208:211], v[120:123]\n"
"ds_read_b128 a[164:167], v0 offset:15360\n"
"buffer_load_dwordx4 v3, s[16:19], s61 offen offset:2048 sc1 lds\n"
"v_mfma_f32_16x16x32_bf16 v[124:127], a[168:171], v[212:215], v[124:127]\n"
"s_mul_i32 s21, s21, 9216\n"
"s_lshl_b32 s22, s57, 1\n"
"v_mfma_f32_16x16x32_bf16 v[128:131], a[172:175], v[208:211], v[128:131]\n"
"s_add_u32 s22, s22, s21\n"
"s_add_u32 s22, s22, 1024\n"
"v_mfma_f32_16x16x32_bf16 v[132:135], a[172:175], v[212:215], v[132:135]\n"
"buffer_load_dwordx4 v3, s[16:19], s61 offen offset:3072 sc1 lds\n"
"s_add_u32 s27, s20, s57\n"
"s_waitcnt lgkmcnt(3)\n"
"v_mfma_f32_16x16x32_bf16 v[136:139], a[144:147], v[208:211], v[136:139]\n"
"s_add_u32 m0, s60, 45056\n"
"s_lshl_b32 s20, s57, 8\n"
"v_mfma_f32_16x16x32_bf16 v[140:143], a[144:147], v[212:215], v[140:143]\n"
"s_waitcnt lgkmcnt(0)\n"
"s_barrier\n"
"v_mfma_f32_16x16x32_bf16 v[144:147], a[148:151], v[208:211], v[144:147]\n"
"ds_read_b128 a[168:171], v5 offset:8192\n"
"buffer_load_dwordx4 v4, s[16:19], s61 offen offset:0 sc1 lds\n"
"v_mfma_f32_16x16x32_bf16 v[148:151], a[148:151], v[212:215], v[148:151]\n"
"ds_read_b128 a[172:175], v5 offset:9216\n"
"s_add_u32 s61, s61, 36864\n"
"s_add_u32 s20, s20, s21\n"
"v_mfma_f32_16x16x32_bf16 v[152:155], a[152:155], v[208:211], v[152:155]\n"
"ds_read_b128 a[144:147], v5 offset:10240\n"
"s_add_u32 m0, s60, 110592\n"
"v_mfma_f32_16x16x32_bf16 v[156:159], a[152:155], v[212:215], v[156:159]\n"
"ds_read_b128 a[148:151], v5 offset:11264\n"
"v_mfma_f32_16x16x32_bf16 v[160:163], a[156:159], v[208:211], v[160:163]\n"
"ds_read_b128 a[152:155], v5 offset:12288\n"
"buffer_load_dwordx4 v2, s[16:19], s61 offen offset:0 sc1 lds\n"
"v_mfma_f32_16x16x32_bf16 v[164:167], a[156:159], v[212:215], v[164:167]\n"
"s_add_u32 s21, s20, 4608\n"
"s_mul_i32 s26, s27, 36864\n"
"s_add_u32 s26, s26, 39845888\n"
"v_mfma_f32_16x16x32_bf16 v[168:171], a[160:163], v[208:211], v[168:171]\n"
"ds_read_b128 a[156:159], v5 offset:13312\n"
"s_lshl_b32 s23, s56, 8\n"
"v_mfma_f32_16x16x32_bf16 v[172:175], a[160:163], v[212:215], v[172:175]\n"
"buffer_load_dwordx4 v2, s[16:19], s61 offen offset:1024 sc1 lds\n"
"s_add_u32 s23, s23, s26\n"
"v_mfma_f32_16x16x32_bf16 v[176:179], a[164:167], v[208:211], v[176:179]\n"
"ds_read_b128 a[160:163], v5 offset:14336\n"
"s_add_u32 s24, s23, 4096\n"
"v_mfma_f32_16x16x32_bf16 v[180:183], a[164:167], v[212:215], v[180:183]\n"
"buffer_load_dword v35, v33, s[16:19], s72 offen sc1\n"
"s_add_u32 s25, s23, 8192\n"
"s_waitcnt lgkmcnt(4)\n"
"v_mfma_f32_16x16x32_bf16 v[112:115], a[168:171], a[248:251], v[112:115]\n"
"ds_read_b128 a[164:167], v5 offset:15360\n"
"s_mul_i32 s36, s56, 9216\n"
"v_mfma_f32_16x16x32_bf16 v[116:119], a[168:171], a[0:3], v[116:119]\n"
"s_add_u32 s26, s26, s36\n"
"s_lshl_b32 s37, s27, 9\n"
"v_mfma_f32_16x16x32_bf16 v[120:123], a[172:175], a[248:251], v[120:123]\n"
"ds_read_b128 a[168:171], v5 offset:16384\n"
"buffer_load_dwordx4 v2, s[16:19], s61 offen offset:2048 sc1 lds\n"
"v_mfma_f32_16x16x32_bf16 v[124:127], a[172:175], a[0:3], v[124:127]\n"
"s_add_u32 s37, s37, 2883584\n"
"s_lshl_b32 s36, s56, 7\n"
"s_add_u32 s36, s36, s37\n"
"v_mfma_f32_16x16x32_bf16 v[128:131], a[144:147], a[248:251], v[128:131]\n"
"ds_read_b128 a[172:175], v5 offset:17408\n"
"v_mfma_f32_16x16x32_bf16 v[132:135], a[144:147], a[0:3], v[132:135]\n"
"s_waitcnt lgkmcnt(4)\n"
"v_mfma_f32_16x16x32_bf16 v[136:139], a[148:151], a[248:251], v[136:139]\n"
"ds_read_b128 a[144:147], v5 offset:18432\n"
"buffer_load_dwordx4 v2, s[16:19], s61 offen offset:3072 sc1 lds\n"
"v_mfma_f32_16x16x32_bf16 v[140:143], a[148:151], a[0:3], v[140:143]\n"
"s_add_u32 m0, s60, 114688\n"
"v_mfma_f32_16x16x32_bf16 v[144:147], a[152:155], a[248:251], v[144:147]\n"
"ds_read_b128 a[148:151], v5 offset:19456\n"
"v_mfma_f32_16x16x32_bf16 v[148:151], a[152:155], a[0:3], v[148:151]\n"
"v_mfma_f32_16x16x32_bf16 v[152:155], a[156:159], a[248:251], v[152:155]\n"
"ds_read_b128 a[152:155], v5 offset:20480\n"
"buffer_load_dwordx4 v3, s[16:19], s61 offen offset:0 sc1 lds\n"
"v_mfma_f32_16x16x32_bf16 v[156:159], a[156:159], a[0:3], v[156:159]\n"
"s_waitcnt lgkmcnt(4)\n"
"v_mfma_f32_16x16x32_bf16 v[160:163], a[160:163], a[248:251], v[160:163]\n"
"ds_read_b128 a[156:159], v5 offset:21504\n"
"v_mfma_f32_16x16x32_bf16 v[164:167], a[160:163], a[0:3], v[164:167]\n"
"buffer_load_dwordx4 v3, s[16:19], s61 offen offset:1024 sc1 lds\n"
"v_mfma_f32_16x16x32_bf16 v[168:171], a[164:167], a[248:251], v[168:171]\n"
"ds_read_b128 a[160:163], v5 offset:22528\n"
"v_mfma_f32_16x16x32_bf16 v[172:175], a[164:167], a[0:3], v[172:175]\n"
"v_mfma_f32_16x16x32_bf16 v[176:179], a[168:171], a[248:251], v[176:179]\n"
"ds_read_b128 a[164:167], v5 offset:23552\n"
"v_mfma_f32_16x16x32_bf16 v[180:183], a[168:171], a[0:3], v[180:183]\n"
"buffer_load_dwordx4 v3, s[16:19], s61 offen offset:2048 sc1 lds\n"
"s_waitcnt lgkmcnt(4)\n"
"v_mfma_f32_16x16x32_bf16 v[112:115], a[172:175], a[4:7], v[112:115]\n"
"ds_read_b128 a[168:171], v5 offset:24576\n"
"v_mfma_f32_16x16x32_bf16 v[116:119], a[172:175], a[8:11], v[116:119]\n"
"v_mfma_f32_16x16x32_bf16 v[120:123], a[144:147], a[4:7], v[120:123]\n"
"ds_read_b128 a[172:175], v5 offset:25600\n"
"v_mfma_f32_16x16x32_bf16 v[124:127], a[144:147], a[8:11], v[124:127]\n"
"buffer_load_dwordx4 v3, s[16:19], s61 offen offset:3072 sc1 lds\n"
"s_add_u32 m0, s60, 118784\n"
"v_mfma_f32_16x16x32_bf16 v[128:131], a[148:151], a[4:7], v[128:131]\n"
"ds_read_b128 a[144:147], v5 offset:26624\n"
"s_waitcnt vmcnt(20)\n"
"v_mfma_f32_16x16x32_bf16 v[132:135], a[148:151], a[8:11], v[132:135]\n"
"s_waitcnt lgkmcnt(4)\n"
"v_mfma_f32_16x16x32_bf16 v[136:139], a[152:155], a[4:7], v[136:139]\n"
"ds_read_b128 a[148:151], v5 offset:27648\n"
"buffer_load_dwordx4 v4, s[16:19], s61 offen offset:0 sc1 lds\n"
"v_mfma_f32_16x16x32_bf16 v[140:143], a[152:155], a[8:11], v[140:143]\n"
"s_mov_b64 exec, 0xff\n"
"buffer_store_dwordx4 v[16:19], v2, s[16:19], s73 offen sc1\n"
"s_mov_b64 exec, -1\n"
"v_mfma_f32_16x16x32_bf16 v[144:147], a[156:159], a[4:7], v[144:147]\n"
"ds_read_b128 a[152:155], v5 offset:28672\n"
"v_mfma_f32_16x16x32_bf16 v[148:151], a[156:159], a[8:11], v[148:151]\n"
"v_mfma_f32_16x16x32_bf16 v[152:155], a[160:163], a[4:7], v[152:155]\n"
"ds_read_b128 a[156:159], v5 offset:29696\n"
"v_mfma_f32_16x16x32_bf16 v[156:159], a[160:163], a[8:11], v[156:159]\n"
"s_waitcnt lgkmcnt(4)\n"
"v_mfma_f32_16x16x32_bf16 v[160:163], a[164:167], a[4:7], v[160:163]\n"
"ds_read_b128 a[160:163], v5 offset:30720\n"
"v_mfma_f32_16x16x32_bf16 v[164:167], a[164:167], a[8:11], v[164:167]\n"
"v_mfma_f32_16x16x32_bf16 v[168:171], a[168:171], a[4:7], v[168:171]\n"
"ds_read_b128 a[164:167], v5 offset:31744\n"
"v_mfma_f32_16x16x32_bf16 v[172:175], a[168:171], a[8:11], v[172:175]\n"
"v_mfma_f32_16x16x32_bf16 v[176:179], a[172:175], a[4:7], v[176:179]\n"
"ds_read_b128 a[168:171], v5 offset:32768\n"
"v_mfma_f32_16x16x32_bf16 v[180:183], a[172:175], a[8:11], v[180:183]\n"
"s_waitcnt lgkmcnt(4)\n"
"v_mfma_f32_16x16x32_bf16 v[112:115], a[144:147], a[12:15], v[112:115]\n"
"ds_read_b128 a[172:175], v5 offset:33792\n"
"v_mfma_f32_16x16x32_bf16 v[116:119], a[144:147], a[16:19], v[116:119]\n"
"v_mfma_f32_16x16x32_bf16 v[120:123], a[148:151], a[12:15], v[120:123]\n"
"ds_read_b128 a[144:147], v5 offset:34816\n"
"v_mfma_f32_16x16x32_bf16 v[124:127], a[148:151], a[16:19], v[124:127]\n"
"v_mfma_f32_16x16x32_bf16 v[128:131], a[152:155], a[12:15], v[128:131]\n"
"ds_read_b128 a[148:151], v5 offset:35840\n"
"v_mfma_f32_16x16x32_bf16 v[132:135], a[152:155], a[16:19], v[132:135]\n"
"s_waitcnt lgkmcnt(4)\n"
"v_mfma_f32_16x16x32_bf16 v[136:139], a[156:159], a[12:15], v[136:139]\n"
"ds_read_b128 a[152:155], v5 offset:36864\n"
"v_mfma_f32_16x16x32_bf16 v[140:143], a[156:159], a[16:19], v[140:143]\n"
"v_mfma_f32_16x16x32_bf16 v[144:147], a[160:163], a[12:15], v[144:147]\n"
"ds_read_b128 a[156:159], v5 offset:37888\n"
"v_mfma_f32_16x16x32_bf16 v[148:151], a[160:163], a[16:19], v[148:151]\n"
"s_waitcnt vmcnt(8)\n"
"v_cmp_ne_u32 vcc, s55, v35\n"
"s_cbranch_vccnz .Lmk_SLOW_7\n"
".Lmk_BACK_7:\n"
"v_mfma_f32_16x16x32_bf16 v[152:155], a[164:167], a[12:15], v[152:155]\n"
"ds_read_b128 a[160:163], v5 offset:38912\n"
"buffer_load_dwordx4 a[28:31], v2, s[16:19], s90 offen offset:0 sc1\n"
"v_mfma_f32_16x16x32_bf16 v[156:159], a[164:167], a[16:19], v[156:159]\n"
"buffer_load_dwordx4 a[32:35], v2, s[16:19], s90 offen offset:1024 sc1\n"
"s_waitcnt lgkmcnt(4)\n"
"v_mfma_f32_16x16x32_bf16 v[160:163], a[168:171], a[12:15], v[160:163]\n"
"ds_read_b128 a[164:167], v5 offset:39936\n"
"buffer_load_dwordx4 a[36:39], v2, s[16:19], s90 offen offset:2048 sc1\n"
"v_mfma_f32_16x16x32_bf16 v[164:167], a[168:171], a[16:19], v[164:167]\n"
"buffer_load_dwordx4 a[40:43], v2, s[16:19], s90 offen offset:3072 sc1\n"
"buffer_load_dwordx4 a[44:47], v2, s[16:19], s91 offen offset:0 sc1\n"
"v_mfma_f32_16x16x32_bf16 v[168:171], a[172:175], a[12:15], v[168:171]\n"
"ds_read_b128 a[168:171], v5 offset:40960\n"
"v_mfma_f32_16x16x32_bf16 v[172:175], a[172:175], a[16:19], v[172:175]\n"
"buffer_load_dwordx4 a[48:51], v2, s[16:19], s91 offen offset:1024 sc1\n"
"v_mfma_f32_16x16x32_bf16 v[176:179], a[144:147], a[12:15], v[176:179]\n"
"ds_read_b128 a[172:175], v5 offset:41984\n"
"v_mfma_f32_16x16x32_bf16 v[180:183], a[144:147], a[16:19], v[180:183]\n"
"s_waitcnt lgkmcnt(4)\n"
"v_mfma_f32_16x16x32_bf16 v[112:115], a[148:151], a[20:23], v[112:115]\n"
"ds_read_b128 a[144:147], v5 offset:43008\n"
"buffer_load_dwordx4 a[52:55], v2, s[16:19], s91 offen offset:2048 sc1\n"
"v_mfma_f32_16x16x32_bf16 v[116:119], a[148:151], a[24:27], v[116:119]\n"
"v_mfma_f32_16x16x32_bf16 v[120:123], a[152:155], a[20:23], v[120:123]\n"
"ds_read_b128 a[148:151], v5 offset:44032\n"
"v_mfma_f32_16x16x32_bf16 v[124:127], a[152:155], a[24:27], v[124:127]\n"
"v_mfma_f32_16x16x32_bf16 v[128:131], a[156:159], a[20:23], v[128:131]\n"
"buffer_load_dwordx4 a[56:59], v2, s[16:19], s91 offen offset:3072 sc1\n"
"v_mfma_f32_16x16x32_bf16 v[132:135], a[156:159], a[24:27], v[132:135]\n"
"s_waitcnt lgkmcnt(3)\n"
"v_mfma_f32_16x16x32_bf16 v[136:139], a[160:163], a[20:23], v[136:139]\n"
"v_mfma_f32_16x16x32_bf16 v[140:143], a[160:163], a[24:27], v[140:143]\n"
"s_waitcnt lgkmcnt(0)\n"
"s_barrier\n"
"v_mfma_f32_16x16x32_bf16 v[144:147], a[164:167], a[20:23], v[144:147]\n"
"ds_read_b128 a[152:155], v2 offset:0\n"
"s_add_u32 s61, s61, 36864\n"
"v_mfma_f32_16x16x32_bf16 v[148:151], a[164:167], a[24:27], v[148:151]\n"
"ds_read_b128 a[156:159], v2 offset:1024\n"
"s_add_u32 m0, s60, 73728\n"
"v_mfma_f32_16x16x32_bf16 v[152:155], a[168:171], a[20:23], v[152:155]\n"
"ds_read_b128 a[160:163], v2 offset:2048\n"
"buffer_load_dwordx4 a[60:63], v2, s[16:19], s92 offen offset:0 sc1\n"
"v_mfma_f32_16x16x32_bf16 v[156:159], a[168:171], a[24:27], v[156:159]\n"
"ds_read_b128 a[164:167], v2 offset:3072\n"
"v_mfma_f32_16x16x32_bf16 v[160:163], a[172:175], a[20:23], v[160:163]\n"
"ds_read_b128 a[168:171], v2 offset:4096\n"
"buffer_load_dwordx4 a[64:67], v2, s[16:19], s92 offen offset:1024 sc1\n"
"v_mfma_f32_16x16x32_bf16 v[164:167], a[172:175], a[24:27], v[164:167]\n"
"v_mfma_f32_16x16x32_bf16 v[168:171], a[144:147], a[20:23], v[168:171]\n"
"ds_read_b128 a[172:175], v2 offset:5120\n"
"v_mfma_f32_16x16x32_bf16 v[172:175], a[144:147], a[24:27], v[172:175]\n"
"buffer_load_dwordx4 a[68:71], v2, s[16:19], s92 offen offset:2048 sc1\n"
"v_mfma_f32_16x16x32_bf16 v[176:179], a[148:151], a[20:23], v[176:179]\n"
"ds_read_b128 a[144:147], v2 offset:6144\n"
"v_mfma_f32_16x16x32_bf16 v[180:183], a[148:151], a[24:27], v[180:183]\n"
"s_waitcnt vmcnt(9) lgkmcnt(4)\n"
"v_mfma_f32_16x16x32_bf16 v[112:115], a[152:155], a[28:31], v[112:115]\n"
"ds_read_b128 a[148:151], v2 offset:7168\n"
"buffer_load_dwordx4 a[72:75], v2, s[16:19], s92 offen offset:3072 sc1\n"
"v_mfma_f32_16x16x32_bf16 v[116:119], a[152:155], a[32:35], v[116:119]\n"
"v_mfma_f32_16x16x32_bf16 v[120:123], a[156:159], a[28:31], v[120:123]\n"
"ds_read_b128 a[152:155], v2 offset:8192\n"
"v_mfma_f32_16x16x32_bf16 v[124:127], a[156:159], a[32:35], v[124:127]\n"
"buffer_load_dword v36, v33, s[16:19], s74 offen sc1\n"
"v_mfma_f32_16x16x32_bf16 v[128:131], a[160:163], a[28:31], v[128:131]\n"
"ds_read_b128 a[156:159], v2 offset:9216\n"
"v_mfma_f32_16x16x32_bf16 v[132:135], a[160:163], a[32:35], v[132:135]\n"
"s_waitcnt lgkmcnt(4)\n"
"v_mfma_f32_16x16x32_bf16 v[136:139], a[164:167], a[28:31], v[136:139]\n"
"ds_read_b128 a[160:163], v2 offset:10240\n"
"buffer_load_dwordx4 v2, s[16:19], s61 offen offset:0 sc1 lds\n"
"v_mfma_f32_16x16x32_bf16 v[140:143], a[164:167], a[32:35], v[140:143]\n"
"v_mfma_f32_16x16x32_bf16 v[144:147], a[168:171], a[28:31], v[144:147]\n"
"ds_read_b128 a[164:167], v2 offset:11264\n"
"v_mfma_f32_16x16x32_bf16 v[148:151], a[168:171], a[32:35], v[148:151]\n"
"v_mfma_f32_16x16x32_bf16 v[152:155], a[172:175], a[28:31], v[152:155]\n"
"ds_read_b128 a[168:171], v2 offset:12288\n"
"buffer_load_dwordx4 v2, s[16:19], s61 offen offset:1024 sc1 lds\n"
"v_mfma_f32_16x16x32_bf16 v[156:159], a[172:175], a[32:35], v[156:159]\n"
"s_waitcnt lgkmcnt(4)\n"
"v_mfma_f32_16x16x32_bf16 v[160:163], a[144:147], a[28:31], v[160:163]\n"
"ds_read_b128 a[172:175], v2 offset:13312\n"
"v_mfma_f32_16x16x32_bf16 v[164:167], a[144:147], a[32:35], v[164:167]\n"
"v_mfma_f32_16x16x32_bf16 v[168:171], a[148:151], a[28:31], v[168:171]\n"
"ds_read_b128 a[144:147], v2 offset:14336\n"
"buffer_load_dwordx4 v2, s[16:19], s61 offen offset:2048 sc1 lds\n"
"v_mfma_f32_16x16x32_bf16 v[172:175], a[148:151], a[32:35], v[172:175]\n"
"v_mfma_f32_16x16x32_bf16 v[176:179], a[152:155], a[28:31], v[176:179]\n"
"ds_read_b128 a[148:151], v2 offset:15360\n"
"v_mfma_f32_16x16x32_bf16 v[180:183], a[152:155], a[32:35], v[180:183]\n"
"buffer_load_dwordx4 v2, s[16:19], s61 offen offset:3072 sc1 lds\n"
"s_waitcnt vmcnt(13) lgkmcnt(4)\n"
"v_mfma_f32_16x16x32_bf16 v[112:115], a[156:159], a[36:39], v[112:115]\n"
"ds_read_b128 a[152:155], v2 offset:16384\n"
"s_add_u32 m0, s60, 77824\n"
"v_mfma_f32_16x16x32_bf16 v[116:119], a[156:159], a[40:43], v[116:119]\n"
"v_mfma_f32_16x16x32_bf16 v[120:123], a[160:163], a[36:39], v[120:123]\n"
"ds_read_b128 a[156:159], v2 offset:17408\n"
"v_mfma_f32_16x16x32_bf16 v[124:127], a[160:163], a[40:43], v[124:127]\n"
"buffer_load_dwordx4 v3, s[16:19], s61 offen offset:0 sc1 lds\n"
"v_mfma_f32_16x16x32_bf16 v[128:131], a[164:167], a[36:39], v[128:131]\n"
"ds_read_b128 a[160:163], v2 offset:18432\n"
"v_mfma_f32_16x16x32_bf16 v[132:135], a[164:167], a[40:43], v[132:135]\n"
"s_waitcnt lgkmcnt(4)\n"
"v_mfma_f32_16x16x32_bf16 v[136:139], a[168:171], a[36:39], v[136:139]\n"
"ds_read_b128 a[164:167], v2 offset:19456\n"
"v_mfma_f32_16x16x32_bf16 v[140:143], a[168:171], a[40:43], v[140:143]\n"
"buffer_load_dwordx4 v3, s[16:19], s61 offen offset:1024 sc1 lds\n"
"v_mfma_f32_16x16x32_bf16 v[144:147], a[172:175], a[36:39], v[144:147]\n"
"ds_read_b128 a[168:171], v2 offset:20480\n"
"v_mfma_f32_16x16x32_bf16 v[148:151], a[172:175], a[40:43], v[148:151]\n"
"v_mfma_f32_16x16x32_bf16 v[152:155], a[144:147], a[36:39], v[152:155]\n"
"ds_read_b128 a[172:175], v2 offset:21504\n"
"buffer_load_dwordx4 v3, s[16:19], s61 offen offset:2048 sc1 lds\n"
"v_mfma_f32_16x16x32_bf16 v[156:159], a[144:147], a[40:43], v[156:159]\n"
"s_waitcnt lgkmcnt(4)\n"
"v_mfma_f32_16x16x32_bf16 v[160:163], a[148:151], a[36:39], v[160:163]\n"
"ds_read_b128 a[144:147], v2 offset:22528\n"
"v_mfma_f32_16x16x32_bf16 v[164:167], a[148:151], a[40:43], v[164:167]\n"
"v_mfma_f32_16x16x32_bf16 v[168:171], a[152:155], a[36:39], v[168:171]\n"
"ds_read_b128 a[148:151], v2 offset:23552\n"
"buffer_load_dwordx4 v3, s[16:19], s61 offen offset:3072 sc1 lds\n"
"v_mfma_f32_16x16x32_bf16 v[172:175], a[152:155], a[40:43], v[172:175]\n"
"s_add_u32 m0, s60, 81920\n"
"v_mfma_f32_16x16x32_bf16 v[176:179], a[156:159], a[36:39], v[176:179]\n"
"ds_read_b128 a[152:155], v2 offset:24576\n"
"v_mfma_f32_16x16x32_bf16 v[180:183], a[156:159], a[40:43], v[180:183]\n"
"s_waitcnt vmcnt(15) lgkmcnt(4)\n"
"v_mfma_f32_16x16x32_bf16 v[112:115], a[160:163], a[44:47], v[112:115]\n"
"ds_read_b128 a[156:159], v2 offset:25600\n"
"buffer_load_dwordx4 v4, s[16:19], s61 offen offset:0 sc1 lds\n"
"v_mfma_f32_16x16x32_bf16 v[116:119], a[160:163], a[48:51], v[116:119]\n"
"v_mfma_f32_16x16x32_bf16 v[120:123], a[164:167], a[44:47], v[120:123]\n"
"ds_read_b128 a[160:163], v2 offset:26624\n"
"v_mfma_f32_16x16x32_bf16 v[124:127], a[164:167], a[48:51], v[124:127]\n"
"v_mfma_f32_16x16x32_bf16 v[128:131], a[168:171], a[44:47], v[128:131]\n"
"ds_read_b128 a[164:167], v2 offset:27648\n"
"v_mfma_f32_16x16x32_bf16 v[132:135], a[168:171], a[48:51], v[132:135]\n"
"s_waitcnt lgkmcnt(4)\n"
"v_mfma_f32_16x16x32_bf16 v[136:139], a[172:175], a[44:47], v[136:139]\n"
"ds_read_b128 a[168:171], v2 offset:28672\n"
"v_mfma_f32_16x16x32_bf16 v[140:143], a[172:175], a[48:51], v[140:143]\n"
"v_mfma_f32_16x16x32_bf16 v[144:147], a[144:147], a[44:47], v[144:147]\n"
"ds_read_b128 a[172:175], v2 offset:29696\n"
"v_mfma_f32_16x16x32_bf16 v[148:151], a[144:147], a[48:51], v[148:151]\n"
"v_mfma_f32_16x16x32_bf16 v[152:155], a[148:151], a[44:47], v[152:155]\n"
"ds_read_b128 a[144:147], v2 offset:30720\n"
"v_mfma_f32_16x16x32_bf16 v[156:159], a[148:151], a[48:51], v[156:159]\n"
"s_waitcnt lgkmcnt(4)\n"
"v_mfma_f32_16x16x32_bf16 v[160:163], a[152:155], a[44:47], v[160:163]\n"
"ds_read_b128 a[148:151], v2 offset:31744\n"
"v_mfma_f32_16x16x32_bf16 v[164:167], a[152:155], a[48:51], v[164:167]\n"
"s_waitcnt vmcnt(9)\n"
"v_cmp_ne_u32 vcc, s55, v36\n"
"s_cbranch_vccnz .Lmk_SLOW_8\n"
".Lmk_BACK_8:\n"
"v_mfma_f32_16x16x32_bf16 v[168:171], a[156:159], a[44:47], v[168:171]\n"
"ds_read_b128 a[152:155], v2 offset:32768\n"
"buffer_load_dwordx4 a[76:79], v3, s[16:19], s90 offen offset:0 sc1\n"
"v_mfma_f32_16x16x32_bf16 v[172:175], a[156:159], a[48:51], v[172:175]\n"
"buffer_load_dwordx4 a[80:83], v3, s[16:19], s90 offen offset:1024 sc1\n"
"buffer_load_dwordx4 a[84:87], v3, s[16:19], s90 offen offset:2048 sc1\n"
"v_mfma_f32_16x16x32_bf16 v[176:179], a[160:163], a[44:47], v[176:179]\n"
"ds_read_b128 a[156:159], v2 offset:33792\n"
"buffer_load_dwordx4 a[88:91], v3, s[16:19], s90 offen offset:3072 sc1\n"
"v_mfma_f32_16x16x32_bf16 v[180:183], a[160:163], a[48:51], v[180:183]\n"
"s_waitcnt lgkmcnt(4)\n"
"v_mfma_f32_16x16x32_bf16 v[112:115], a[164:167], a[52:55], v[112:115]\n"
"ds_read_b128 a[160:163], v2 offset:34816\n"
"v_mfma_f32_16x16x32_bf16 v[116:119], a[164:167], a[56:59], v[116:119]\n"
"buffer_load_dwordx4 a[92:95], v3, s[16:19], s91 offen offset:0 sc1\n"
"v_mfma_f32_16x16x32_bf16 v[120:123], a[168:171], a[52:55], v[120:123]\n"
"ds_read_b128 a[164:167], v2 offset:35840\n"
"v_mfma_f32_16x16x32_bf16 v[124:127], a[168:171], a[56:59], v[124:127]\n"
"v_mfma_f32_16x16x32_bf16 v[128:131], a[172:175], a[52:55], v[128:131]\n"
"buffer_load_dwordx4 a[96:99], v3, s[16:19], s91 offen offset:1024 sc1\n"
"v_mfma_f32_16x16x32_bf16 v[132:135], a[172:175], a[56:59], v[132:135]\n"
"s_waitcnt lgkmcnt(3)\n"
"v_mfma_f32_16x16x32_bf16 v[136:139], a[144:147], a[52:55], v[136:139]\n"
"v_mfma_f32_16x16x32_bf16 v[140:143], a[144:147], a[56:59], v[140:143]\n"
"s_waitcnt lgkmcnt(0)\n"
"s_barrier\n"
"v_mfma_f32_16x16x32_bf16 v[144:147], a[148:151], a[52:55], v[144:147]\n"
"ds_read_b128 a[168:171], v2 offset:36864\n"
"buffer_load_dwordx4 a[100:103], v3, s[16:19], s91 offen offset:2048 sc1\n"
"v_mfma_f32_16x16x32_bf16 v[148:151], a[148:151], a[56:59], v[148:151]\n"
"ds_read_b128 a[172:175], v2 offset:37888\n"
"v_mfma_f32_16x16x32_bf16 v[152:155], a[152:155], a[52:55], v[152:155]\n"
"ds_read_b128 a[144:147], v2 offset:38912\n"
"v_mfma_f32_16x16x32_bf16 v[156:159], a[152:155], a[56:59], v[156:159]\n"
"ds_read_b128 a[148:151], v2 offset:39936\n"
"v_mfma_f32_16x16x32_bf16 v[160:163], a[156:159], a[52:55], v[160:163]\n"
"ds_read_b128 a[152:155], v2 offset:40960\n"
"buffer_load_dwordx4 a[104:107], v3, s[16:19], s91 offen offset:3072 sc1\n"
"v_mfma_f32_16x16x32_bf16 v[164:167], a[156:159], a[56:59], v[164:167]\n"
"v_mfma_f32_16x16x32_bf16 v[168:171], a[160:163], a[52:55], v[168:171]\n"
"ds_read_b128 a[156:159], v2 offset:41984\n"
"v_mfma_f32_16x16x32_bf16 v[172:175], a[160:163], a[56:59], v[172:175]\n"
"v_mfma_f32_16x16x32_bf16 v[176:179], a[164:167], a[52:55], v[176:179]\n"
"ds_read_b128 a[160:163], v2 offset:43008\n"
"buffer_load_dwordx4 a[108:111], v3, s[16:19], s92 offen offset:0 sc1\n"
"v_mfma_f32_16x16x32_bf16 v[180:183], a[164:167], a[56:59], v[180:183]\n"
"s_waitcnt lgkmcnt(4)\n"
"v_mfma_f32_16x16x32_bf16 v[112:115], a[168:171], a[60:63], v[112:115]\n"
"ds_read_b128 a[164:167], v2 offset:44032\n"
"v_mfma_f32_16x16x32_bf16 v[116:119], a[168:171], a[64:67], v[116:119]\n"
"buffer_load_dwordx4 a[112:115], v3, s[16:19], s92 offen offset:1024 sc1\n"
"v_mfma_f32_16x16x32_bf16 v[120:123], a[172:175], a[60:63], v[120:123]\n"
"ds_read_b128 a[168:171], v2 offset:45056\n"
"v_mfma_f32_16x16x32_bf16 v[124:127], a[172:175], a[64:67], v[124:127]\n"
"v_mfma_f32_16x16x32_bf16 v[128:131], a[144:147], a[60:63], v[128:131]\n"
"ds_read_b128 a[172:175], v2 offset:46080\n"
"buffer_load_dwordx4 a[116:119], v3, s[16:19], s92 offen offset:2048 sc1\n"
"v_mfma_f32_16x16x32_bf16 v[132:135], a[144:147], a[64:67], v[132:135]\n"
"s_waitcnt lgkmcnt(4)\n"
"v_mfma_f32_16x16x32_bf16 v[136:139], a[148:151], a[60:63], v[136:139]\n"
"ds_read_b128 a[144:147], v2 offset:47104\n"
"v_mfma_f32_16x16x32_bf16 v[140:143], a[148:151], a[64:67], v[140:143]\n"
"v_mfma_f32_16x16x32_bf16 v[144:147], a[152:155], a[60:63], v[144:147]\n"
"ds_read_b128 a[148:151], v2 offset:48128\n"
"buffer_load_dwordx4 a[120:123], v3, s[16:19], s92 offen offset:3072 sc1\n"
"v_mfma_f32_16x16x32_bf16 v[148:151], a[152:155], a[64:67], v[148:151]\n"
"v_mfma_f32_16x16x32_bf16 v[152:155], a[156:159], a[60:63], v[152:155]\n"
"ds_read_b128 a[152:155], v2 offset:49152\n"
"v_mfma_f32_16x16x32_bf16 v[156:159], a[156:159], a[64:67], v[156:159]\n"
"s_waitcnt lgkmcnt(4)\n"
"v_mfma_f32_16x16x32_bf16 v[160:163], a[160:163], a[60:63], v[160:163]\n"
"ds_read_b128 a[156:159], v2 offset:50176\n"
"buffer_load_dwordx4 a[124:127], v4, s[16:19], s89 offen offset:0 sc1\n"
"v_mfma_f32_16x16x32_bf16 v[164:167], a[160:163], a[64:67], v[164:167]\n"
"v_mfma_f32_16x16x32_bf16 v[168:171], a[164:167], a[60:63], v[168:171]\n"
"ds_read_b128 a[160:163], v2 offset:51200\n"
"v_mfma_f32_16x16x32_bf16 v[172:175], a[164:167], a[64:67], v[172:175]\n"
"v_mfma_f32_16x16x32_bf16 v[176:179], a[168:171], a[60:63], v[176:179]\n"
"ds_read_b128 a[164:167], v2 offset:52224\n"
"buffer_load_dwordx4 a[128:131], v4, s[16:19], s89 offen offset:1024 sc1\n"
"v_mfma_f32_16x16x32_bf16 v[180:183], a[168:171], a[64:67], v[180:183]\n"
"s_waitcnt lgkmcnt(4)\n"
"v_mfma_f32_16x16x32_bf16 v[112:115], a[172:175], a[68:71], v[112:115]\n"
"ds_read_b128 a[168:171], v2 offset:53248\n"
"v_mfma_f32_16x16x32_bf16 v[116:119], a[172:175], a[72:75], v[116:119]\n"
"v_mfma_f32_16x16x32_bf16 v[120:123], a[144:147], a[68:71], v[120:123]\n"
"ds_read_b128 a[172:175], v2 offset:54272\n"
"buffer_load_dwordx4 a[132:135], v4, s[16:19], s90 offen offset:0 sc1\n"
"v_mfma_f32_16x16x32_bf16 v[124:127], a[144:147], a[72:75], v[124:127]\n"
"v_mfma_f32_16x16x32_bf16 v[128:131], a[148:151], a[68:71], v[128:131]\n"
"ds_read_b128 a[144:147], v2 offset:55296\n"
"v_mfma_f32_16x16x32_bf16 v[132:135], a[148:151], a[72:75], v[132:135]\n"
"s_waitcnt lgkmcnt(4)\n"
"v_mfma_f32_16x16x32_bf16 v[136:139], a[152:155], a[68:71], v[136:139]\n"
".Lmk_PCHERE_8:\n"
"s_add_u32 s0, s0, .Lmk_PCHERE_8-.Lmk_PCHERE_7\n"
"s_addc_u32 s1, s1, 0\n"
"s_sub_u32 s2, s2, .Lmk_PCHERE_8-.Lmk_PCHERE_7\n"
"s_max_i32 s2, s2, 0\n"
"buffer_load_dword a255, off, s[0:3], 0\n"
"ds_read_b128 a[148:151], v2 offset:56320\n"
"buffer_load_dwordx4 a[136:139], v4, s[16:19], s90 offen offset:1024 sc1\n"
"v_mfma_f32_16x16x32_bf16 v[140:143], a[152:155], a[72:75], v[140:143]\n"
"v_mfma_f32_16x16x32_bf16 v[144:147], a[156:159], a[68:71], v[144:147]\n"
"ds_read_b128 a[152:155], v2 offset:57344\n"
"v_mfma_f32_16x16x32_bf16 v[148:151], a[156:159], a[72:75], v[148:151]\n"
"v_mfma_f32_16x16x32_bf16 v[152:155], a[160:163], a[68:71], v[152:155]\n"
"ds_read_b128 a[156:159], v2 offset:58368\n"
"buffer_load_dwordx4 a[140:143], v4, s[16:19], s91 offen offset:0 sc1\n"
"v_mfma_f32_16x16x32_bf16 v[156:159], a[160:163], a[72:75], v[156:159]\n"
"s_waitcnt lgkmcnt(4)\n"
"v_mfma_f32_16x16x32_bf16 v[160:163], a[164:167], a[68:71], v[160:163]\n"
"ds_read_b128 a[160:163], v2 offset:59392\n"
"v_mfma_f32_16x16x32_bf16 v[164:167], a[164:167], a[72:75], v[164:167]\n"
"buffer_load_dwordx4 a[176:179], v4, s[16:19], s91 offen offset:1024 sc1\n"
"v_mfma_f32_16x16x32_bf16 v[168:171], a[168:171], a[68:71], v[168:171]\n"
"ds_read_b128 a[164:167], v2 offset:60416\n"
"v_mfma_f32_16x16x32_bf16 v[172:175], a[168:171], a[72:75], v[172:175]\n"
"v_mfma_f32_16x16x32_bf16 v[176:179], a[172:175], a[68:71], v[176:179]\n"
"ds_read_b128 a[168:171], v2 offset:61440\n"
"buffer_load_dwordx4 a[180:183], v4, s[16:19], s92 offen offset:0 sc1\n"
"v_mfma_f32_16x16x32_bf16 v[180:183], a[172:175], a[72:75], v[180:183]\n"
"s_waitcnt vmcnt(18) lgkmcnt(4)\n"
"v_mfma_f32_16x16x32_bf16 v[112:115], a[144:147], a[76:79], v[112:115]\n"
"ds_read_b128 a[172:175], v2 offset:62464\n"
"v_mfma_f32_16x16x32_bf16 v[116:119], a[144:147], a[80:83], v[116:119]\n"
"v_mfma_f32_16x16x32_bf16 v[120:123], a[148:151], a[76:79], v[120:123]\n"
"ds_read_b128 a[144:147], v2 offset:63488\n"
"buffer_load_dwordx4 a[184:187], v4, s[16:19], s92 offen offset:1024 sc1\n"
"v_mfma_f32_16x16x32_bf16 v[124:127], a[148:151], a[80:83], v[124:127]\n"
"buffer_load_dwordx2 v[224:225], v25, s[8:11], s20 offen offset:0\n"
"v_mfma_f32_16x16x32_bf16 v[128:131], a[152:155], a[76:79], v[128:131]\n"
"ds_read_b128 a[148:151], v2 offset:64512\n"
"v_mfma_f32_16x16x32_bf16 v[132:135], a[152:155], a[80:83], v[132:135]\n"
"s_waitcnt lgkmcnt(4)\n"
"v_mfma_f32_16x16x32_bf16 v[136:139], a[156:159], a[76:79], v[136:139]\n"
"ds_read_b128 a[152:155], v5 offset:0\n"
"v_mfma_f32_16x16x32_bf16 v[140:143], a[156:159], a[80:83], v[140:143]\n"
"buffer_load_dwordx2 v[226:227], v25, s[8:11], s20 offen offset:128\n"
"v_mfma_f32_16x16x32_bf16 v[144:147], a[160:163], a[76:79], v[144:147]\n"
"ds_read_b128 a[156:159], v5 offset:1024\n"
"v_mfma_f32_16x16x32_bf16 v[148:151], a[160:163], a[80:83], v[148:151]\n"
"v_mfma_f32_16x16x32_bf16 v[152:155], a[164:167], a[76:79], v[152:155]\n"
"ds_read_b128 a[160:163], v5 offset:2048\n"
"v_mfma_f32_16x16x32_bf16 v[156:159], a[164:167], a[80:83], v[156:159]\n"
"buffer_load_dwordx2 v[228:229], v25, s[8:11], s21 offen offset:0\n"
"s_waitcnt lgkmcnt(4)\n"
"v_mfma_f32_16x16x32_bf16 v[160:163], a[168:171], a[76:79], v[160:163]\n"
"ds_read_b128 a[164:167], v5 offset:3072\n"
"v_mfma_f32_16x16x32_bf16 v[164:167], a[168:171], a[80:83], v[164:167]\n"
"v_mfma_f32_16x16x32_bf16 v[168:171], a[172:175], a[76:79], v[168:171]\n"
"ds_read_b128 a[168:171], v5 offset:4096\n"
"v_mfma_f32_16x16x32_bf16 v[172:175], a[172:175], a[80:83], v[172:175]\n"
"buffer_load_dwordx2 v[230:231], v25, s[8:11], s21 offen offset:128\n"
"v_mfma_f32_16x16x32_bf16 v[176:179], a[144:147], a[76:79], v[176:179]\n"
"ds_read_b128 a[172:175], v5 offset:5120\n"
"v_mfma_f32_16x16x32_bf16 v[180:183], a[144:147], a[80:83], v[180:183]\n"
"s_waitcnt vmcnt(21) lgkmcnt(4)\n"
"v_mfma_f32_16x16x32_bf16 v[112:115], a[148:151], a[84:87], v[112:115]\n"
"ds_read_b128 a[144:147], v5 offset:6144\n"
"v_mfma_f32_16x16x32_bf16 v[116:119], a[148:151], a[88:91], v[116:119]\n"
"buffer_load_ushort v232, v26, s[8:11], s22 offen offset:0\n"
"v_mfma_f32_16x16x32_bf16 v[120:123], a[152:155], a[84:87], v[120:123]\n"
"ds_read_b128 a[148:151], v5 offset:7168\n"
"v_mfma_f32_16x16x32_bf16 v[124:127], a[152:155], a[88:91], v[124:127]\n"
"v_mfma_f32_16x16x32_bf16 v[128:131], a[156:159], a[84:87], v[128:131]\n"
"v_mfma_f32_16x16x32_bf16 v[132:135], a[156:159], a[88:91], v[132:135]\n"
"buffer_load_ushort v233, v26, s[8:11], s22 offen offset:8\n"
"s_waitcnt lgkmcnt(3)\n"
"v_mfma_f32_16x16x32_bf16 v[136:139], a[160:163], a[84:87], v[136:139]\n"
"v_mfma_f32_16x16x32_bf16 v[140:143], a[160:163], a[88:91], v[140:143]\n"
"s_waitcnt lgkmcnt(0)\n"
"s_barrier\n"
"v_mfma_f32_16x16x32_bf16 v[144:147], a[164:167], a[84:87], v[144:147]\n"
"ds_read_b128 a[152:155], v5 offset:45056\n"
"v_mfma_f32_16x16x32_bf16 v[148:151], a[164:167], a[88:91], v[148:151]\n"
"ds_read_b128 a[156:159], v5 offset:54272\n"
"v_mfma_f32_16x16x32_bf16 v[152:155], a[168:171], a[84:87], v[152:155]\n"
"ds_read_b128 a[160:163], v5 offset:63488\n"
"v_mfma_f32_16x16x32_bf16 v[156:159], a[168:171], a[88:91], v[156:159]\n"
"ds_read_b128 a[164:167], v0 offset:7168\n"
"v_mfma_f32_16x16x32_bf16 v[160:163], a[172:175], a[84:87], v[160:163]\n"
"ds_read_b128 a[168:171], v5 offset:8192\n"
"v_mfma_f32_16x16x32_bf16 v[164:167], a[172:175], a[88:91], v[164:167]\n"
"v_mfma_f32_16x16x32_bf16 v[168:171], a[144:147], a[84:87], v[168:171]\n"
"ds_read_b128 a[172:175], v5 offset:17408\n"
"v_mfma_f32_16x16x32_bf16 v[172:175], a[144:147], a[88:91], v[172:175]\n"
"v_mfma_f32_16x16x32_bf16 v[176:179], a[148:151], a[84:87], v[176:179]\n"
"ds_read_b128 a[144:147], v5 offset:26624\n"
"v_mfma_f32_16x16x32_bf16 v[180:183], a[148:151], a[88:91], v[180:183]\n"
"s_waitcnt vmcnt(17) lgkmcnt(4)\n"
"v_mfma_f32_16x16x32_bf16 v[112:115], a[152:155], a[92:95], v[112:115]\n"
"ds_read_b128 a[148:151], v5 offset:35840\n"
"v_mfma_f32_16x16x32_bf16 v[116:119], a[152:155], a[96:99], v[116:119]\n"
"v_mfma_f32_16x16x32_bf16 v[112:115], a[156:159], a[100:103], v[112:115]\n"
"ds_read_b128 a[152:155], v5 offset:46080\n"
"v_mfma_f32_16x16x32_bf16 v[116:119], a[156:159], a[104:107], v[116:119]\n"
"v_mfma_f32_16x16x32_bf16 v[112:115], a[160:163], a[108:111], v[112:115]\n"
"ds_read_b128 a[156:159], v5 offset:55296\n"
"v_mfma_f32_16x16x32_bf16 v[116:119], a[160:163], a[112:115], v[116:119]\n"
"s_waitcnt vmcnt(10) lgkmcnt(4)\n"
"v_mfma_f32_16x16x32_bf16 v[112:115], a[164:167], a[116:119], v[112:115]\n"
"ds_read_b128 a[160:163], v5 offset:64512\n"
"v_mfma_f32_16x16x32_bf16 v[116:119], a[164:167], a[120:123], v[116:119]\n"
"v_mfma_f32_16x16x32_bf16 v[112:115], a[168:171], a[124:127], v[112:115]\n"
"ds_read_b128 a[164:167], v0 offset:8192\n"
"v_mfma_f32_16x16x32_bf16 v[116:119], a[168:171], a[128:131], v[116:119]\n"
"v_mfma_f32_16x16x32_bf16 v[112:115], a[172:175], a[132:135], v[112:115]\n"
"ds_read_b128 a[168:171], v5 offset:9216\n"
"v_mfma_f32_16x16x32_bf16 v[116:119], a[172:175], a[136:139], v[116:119]\n"
"s_waitcnt vmcnt(6) lgkmcnt(4)\n"
"v_mfma_f32_16x16x32_bf16 v[112:115], a[144:147], a[140:143], v[112:115]\n"
"ds_read_b128 a[172:175], v5 offset:18432\n"
"v_mfma_f32_16x16x32_bf16 v[116:119], a[144:147], a[176:179], v[116:119]\n"
"v_mfma_f32_16x16x32_bf16 v[112:115], a[148:151], a[180:183], v[112:115]\n"
"ds_read_b128 a[144:147], v5 offset:27648\n"
"v_mfma_f32_16x16x32_bf16 v[116:119], a[148:151], a[184:187], v[116:119]\n"
"v_mfma_f32_16x16x32_bf16 v[120:123], a[152:155], a[92:95], v[120:123]\n"
"ds_read_b128 a[148:151], v5 offset:36864\n"
"v_mfma_f32_16x16x32_bf16 v[124:127], a[152:155], a[96:99], v[124:127]\n"
"s_waitcnt lgkmcnt(4)\n"
"v_mfma_f32_16x16x32_bf16 v[120:123], a[156:159], a[100:103], v[120:123]\n"
"ds_read_b128 a[152:155], v5 offset:47104\n"
"v_mfma_f32_16x16x32_bf16 v[124:127], a[156:159], a[104:107], v[124:127]\n"
"v_mfma_f32_16x16x32_bf16 v[120:123], a[160:163], a[108:111], v[120:123]\n"
"ds_read_b128 a[156:159], v5 offset:56320\n"
"v_mfma_f32_16x16x32_bf16 v[124:127], a[160:163], a[112:115], v[124:127]\n"
"v_mfma_f32_16x16x32_bf16 v[120:123], a[164:167], a[116:119], v[120:123]\n"
"ds_read_b128 a[160:163], v0 offset:0\n"
"v_mfma_f32_16x16x32_bf16 v[124:127], a[164:167], a[120:123], v[124:127]\n"
"s_waitcnt lgkmcnt(4)\n"
"v_mfma_f32_16x16x32_bf16 v[120:123], a[168:171], a[124:127], v[120:123]\n"
"ds_read_b128 a[164:167], v0 offset:9216\n"
"v_mfma_f32_16x16x32_bf16 v[124:127], a[168:171], a[128:131], v[124:127]\n"
"v_mfma_f32_16x16x32_bf16 v[120:123], a[172:175], a[132:135], v[120:123]\n"
"ds_read_b128 a[168:171], v5 offset:10240\n"
"v_mfma_f32_16x16x32_bf16 v[124:127], a[172:175], a[136:139], v[124:127]\n"
"s_waitcnt vmcnt(5)\n"
"v_lshlrev_b32 v236, 16, v224\n"
"v_and_b32 v237, s77, v224\n"
"v_lshlrev_b32 v238, 16, v225\n"
"v_mfma_f32_16x16x32_bf16 v[120:123], a[144:147], a[140:143], v[120:123]\n"
"ds_read_b128 a[172:175], v5 offset:19456\n"
"v_and_b32 v239, s77, v225\n"
"v_mfma_f32_16x16x32_bf16 v[124:127], a[144:147], a[176:179], v[124:127]\n"
"s_waitcnt vmcnt(4)\n"
"v_lshlrev_b32 v240, 16, v226\n"
"s_waitcnt lgkmcnt(4)\n"
"v_mfma_f32_16x16x32_bf16 v[120:123], a[148:151], a[180:183], v[120:123]\n"
"ds_read_b128 a[144:147], v5 offset:28672\n"
"v_and_b32 v241, s77, v226\n"
"v_mfma_f32_16x16x32_bf16 v[124:127], a[148:151], a[184:187], v[124:127]\n"
"v_lshlrev_b32 v242, 16, v227\n"
"v_and_b32 v243, s77, v227\n"
"s_waitcnt vmcnt(3)\n"
"v_lshlrev_b32 v244, 16, v228\n"
"v_mfma_f32_16x16x32_bf16 v[128:131], a[152:155], a[92:95], v[128:131]\n"
"ds_read_b128 a[148:151], v5 offset:37888\n"
"v_and_b32 v245, s77, v228\n"
"v_mfma_f32_16x16x32_bf16 v[132:135], a[152:155], a[96:99], v[132:135]\n"
"v_lshlrev_b32 v246, 16, v229\n"
"v_and_b32 v247, s77, v229\n"
"v_mfma_f32_16x16x32_bf16 v[128:131], a[156:159], a[100:103], v[128:131]\n"
"ds_read_b128 a[152:155], v5 offset:48128\n"
"s_waitcnt vmcnt(2)\n"
"v_lshlrev_b32 v248, 16, v230\n"
"v_mfma_f32_16x16x32_bf16 v[132:135], a[156:159], a[104:107], v[132:135]\n"
"v_and_b32 v249, s77, v230\n"
"v_lshlrev_b32 v250, 16, v231\n"
"s_waitcnt lgkmcnt(4)\n"
"v_mfma_f32_16x16x32_bf16 v[128:131], a[160:163], a[108:111], v[128:131]\n"
"ds_read_b128 a[156:159], v5 offset:57344\n"
"v_and_b32 v251, s77, v231\n"
"v_mfma_f32_16x16x32_bf16 v[132:135], a[160:163], a[112:115], v[132:135]\n"
"s_waitcnt vmcnt(1)\n"
"v_lshlrev_b32 v252, 16, v232\n"
"s_waitcnt vmcnt(0)\n"
"v_lshlrev_b32 v253, 16, v233\n"
"v_mfma_f32_16x16x32_bf16 v[128:131], a[164:167], a[116:119], v[128:131]\n"
"ds_read_b128 a[160:163], v0 offset:1024\n"
"v_mfma_f32_16x16x32_bf16 v[132:135], a[164:167], a[120:123], v[132:135]\n"
"v_mfma_f32_16x16x32_bf16 v[128:131], a[168:171], a[124:127], v[128:131]\n"
"ds_read_b128 a[164:167], v0 offset:10240\n"
"v_cvt_pk_bf16_f32 v40, v112, v120\n"
"v_mfma_f32_16x16x32_bf16 v[132:135], a[168:171], a[128:131], v[132:135]\n"
"v_cvt_pk_bf16_f32 v42, v113, v121\n"
"s_waitcnt lgkmcnt(4)\n"
"v_mfma_f32_16x16x32_bf16 v[128:131], a[172:175], a[132:135], v[128:131]\n"
"ds_read_b128 a[168:171], v5 offset:11264\n"
"v_cvt_pk_bf16_f32 v44, v114, v122\n"
"v_mfma_f32_16x16x32_bf16 v[132:135], a[172:175], a[136:139], v[132:135]\n"
"v_cvt_pk_bf16_f32 v46, v115, v123\n"
"v_cvt_pk_bf16_f32 v56, v116, v124\n"
"v_cvt_pk_bf16_f32 v58, v117, v125\n"
"v_mfma_f32_16x16x32_bf16 v[128:131], a[144:147], a[140:143], v[128:131]\n"
"ds_read_b128 a[172:175], v5 offset:20480\n"
"v_cvt_pk_bf16_f32 v60, v118, v126\n"
"v_mfma_f32_16x16x32_bf16 v[132:135], a[144:147], a[176:179], v[132:135]\n"
"v_cvt_pk_bf16_f32 v62, v119, v127\n"
"v_mfma_f32_16x16x32_bf16 v[128:131], a[148:151], a[180:183], v[128:131]\n"
"ds_read_b128 a[144:147], v5 offset:29696\n"
"v_mfma_f32_16x16x32_bf16 v[132:135], a[148:151], a[184:187], v[132:135]\n"
"s_waitcnt lgkmcnt(4)\n"
"v_mfma_f32_16x16x32_bf16 v[136:139], a[152:155], a[92:95], v[136:139]\n"
"ds_read_b128 a[148:151], v5 offset:38912\n"
"v_mfma_f32_16x16x32_bf16 v[140:143], a[152:155], a[96:99], v[140:143]\n"
"v_mfma_f32_16x16x32_bf16 v[136:139], a[156:159], a[100:103], v[136:139]\n"
"ds_read_b128 a[152:155], v5 offset:49152\n"
"v_mfma_f32_16x16x32_bf16 v[140:143], a[156:159], a[104:107], v[140:143]\n"
"v_mfma_f32_16x16x32_bf16 v[136:139], a[160:163], a[108:111], v[136:139]\n"
"ds_read_b128 a[156:159], v5 offset:58368\n"
"v_mfma_f32_16x16x32_bf16 v[140:143], a[160:163], a[112:115], v[140:143]\n"
"s_waitcnt lgkmcnt(4)\n"
"v_mfma_f32_16x16x32_bf16 v[136:139], a[164:167], a[116:119], v[136:139]\n"
"ds_read_b128 a[160:163], v0 offset:2048\n"
"v_mfma_f32_16x16x32_bf16 v[140:143], a[164:167], a[120:123], v[140:143]\n"
"v_mfma_f32_16x16x32_bf16 v[136:139], a[168:171], a[124:127], v[136:139]\n"
"ds_read_b128 a[164:167], v0 offset:11264\n"
"v_mfma_f32_16x16x32_bf16 v[140:143], a[168:171], a[128:131], v[140:143]\n"
"v_mfma_f32_16x16x32_bf16 v[136:139], a[172:175], a[132:135], v[136:139]\n"
"ds_read_b128 a[168:171], v5 offset:12288\n"
"v_mfma_f32_16x16x32_bf16 v[140:143], a[172:175], a[136:139], v[140:143]\n"
"s_waitcnt lgkmcnt(4)\n"
"v_mfma_f32_16x16x32_bf16 v[136:139], a[144:147], a[140:143], v[136:139]\n"
"ds_read_b128 a[172:175], v5 offset:21504\n"
"v_mfma_f32_16x16x32_bf16 v[140:143], a[144:147], a[176:179], v[140:143]\n"
"v_mfma_f32_16x16x32_bf16 v[136:139], a[148:151], a[180:183], v[136:139]\n"
"ds_read_b128 a[144:147], v5 offset:30720\n"
"v_mfma_f32_16x16x32_bf16 v[140:143], a[148:151], a[184:187], v[140:143]\n"
"v_mfma_f32_16x16x32_bf16 v[144:147], a[152:155], a[92:95], v[144:147]\n"
"ds_read_b128 a[148:151], v5 offset:39936\n"
"v_mfma_f32_16x16x32_bf16 v[148:151], a[152:155], a[96:99], v[148:151]\n"
"s_waitcnt lgkmcnt(4)\n"
"v_mfma_f32_16x16x32_bf16 v[144:147], a[156:159], a[100:103], v[144:147]\n"
"ds_read_b128 a[152:155], v5 offset:50176\n"
"v_cvt_pk_bf16_f32 v41, v128, v136\n"
"v_mfma_f32_16x16x32_bf16 v[148:151], a[156:159], a[104:107], v[148:151]\n"
"v_cvt_pk_bf16_f32 v43, v129, v137\n"
"v_cvt_pk_bf16_f32 v45, v130, v138\n"
"v_cvt_pk_bf16_f32 v47, v131, v139\n"
"v_mfma_f32_16x16x32_bf16 v[144:147], a[160:163], a[108:111], v[144:147]\n"
"ds_read_b128 a[156:159], v5 offset:59392\n"
"v_cvt_pk_bf16_f32 v57, v132, v140\n"
"v_mfma_f32_16x16x32_bf16 v[148:151], a[160:163], a[112:115], v[148:151]\n"
"v_cvt_pk_bf16_f32 v59, v133, v141\n"
"v_cvt_pk_bf16_f32 v61, v134, v142\n"
"v_mfma_f32_16x16x32_bf16 v[144:147], a[164:167], a[116:119], v[144:147]\n"
"ds_read_b128 a[160:163], v0 offset:3072\n"
"v_cvt_pk_bf16_f32 v63, v135, v143\n"
"v_mfma_f32_16x16x32_bf16 v[148:151], a[164:167], a[120:123], v[148:151]\n"
"buffer_store_dwordx4 v[40:43], v24, s[16:19], s23 offen offset:0 sc1\n"
"s_waitcnt lgkmcnt(4)\n"
"v_mfma_f32_16x16x32_bf16 v[144:147], a[168:171], a[124:127], v[144:147]\n"
"ds_read_b128 a[164:167], v0 offset:12288\n"
"v_mfma_f32_16x16x32_bf16 v[148:151], a[168:171], a[128:131], v[148:151]\n"
"v_mfma_f32_16x16x32_bf16 v[144:147], a[172:175], a[132:135], v[144:147]\n"
"ds_read_b128 a[168:171], v5 offset:13312\n"
"buffer_store_dwordx4 v[44:47], v24, s[16:19], s23 offen offset:1024 sc1\n"
"v_mfma_f32_16x16x32_bf16 v[148:151], a[172:175], a[136:139], v[148:151]\n"
"v_mfma_f32_16x16x32_bf16 v[144:147], a[144:147], a[140:143], v[144:147]\n"
"ds_read_b128 a[172:175], v5 offset:22528\n"
"v_mfma_f32_16x16x32_bf16 v[148:151], a[144:147], a[176:179], v[148:151]\n"
"buffer_store_dwordx4 v[56:59], v24, s[16:19], s24 offen offset:0 sc1\n"
"s_waitcnt lgkmcnt(4)\n"
"v_mfma_f32_16x16x32_bf16 v[144:147], a[148:151], a[180:183], v[144:147]\n"
"ds_read_b128 a[144:147], v5 offset:31744\n"
"v_mfma_f32_16x16x32_bf16 v[148:151], a[148:151], a[184:187], v[148:151]\n"
"v_mfma_f32_16x16x32_bf16 v[152:155], a[152:155], a[92:95], v[152:155]\n"
"ds_read_b128 a[148:151], v5 offset:40960\n"
"buffer_store_dwordx4 v[60:63], v24, s[16:19], s24 offen offset:1024 sc1\n"
"v_mfma_f32_16x16x32_bf16 v[156:159], a[152:155], a[96:99], v[156:159]\n"
"s_add_u32 s38, s36, 131072\n"
"s_add_u32 s39, s37, 131072\n"
"v_mfma_f32_16x16x32_bf16 v[152:155], a[156:159], a[100:103], v[152:155]\n"
"ds_read_b128 a[152:155], v5 offset:51200\n"
"v_mfma_f32_16x16x32_bf16 v[156:159], a[156:159], a[104:107], v[156:159]\n"
"s_waitcnt lgkmcnt(4)\n"
"v_mfma_f32_16x16x32_bf16 v[152:155], a[160:163], a[108:111], v[152:155]\n"
"ds_read_b128 a[156:159], v5 offset:60416\n"
"v_mfma_f32_16x16x32_bf16 v[156:159], a[160:163], a[112:115], v[156:159]\n"
"v_mfma_f32_16x16x32_bf16 v[152:155], a[164:167], a[116:119], v[152:155]\n"
"ds_read_b128 a[160:163], v0 offset:4096\n"
"v_mfma_f32_16x16x32_bf16 v[156:159], a[164:167], a[120:123], v[156:159]\n"
"v_mfma_f32_16x16x32_bf16 v[152:155], a[168:171], a[124:127], v[152:155]\n"
"ds_read_b128 a[164:167], v0 offset:13312\n"
"v_mfma_f32_16x16x32_bf16 v[156:159], a[168:171], a[128:131], v[156:159]\n"
"s_waitcnt lgkmcnt(4)\n"
"v_mfma_f32_16x16x32_bf16 v[152:155], a[172:175], a[132:135], v[152:155]\n"
"ds_read_b128 a[168:171], v5 offset:14336\n"
"v_mfma_f32_16x16x32_bf16 v[156:159], a[172:175], a[136:139], v[156:159]\n"
"v_mfma_f32_16x16x32_bf16 v[152:155], a[144:147], a[140:143], v[152:155]\n"
"ds_read_b128 a[172:175], v5 offset:23552\n"
"v_mfma_f32_16x16x32_bf16 v[156:159], a[144:147], a[176:179], v[156:159]\n"
"v_mfma_f32_16x16x32_bf16 v[152:155], a[148:151], a[180:183], v[152:155]\n"
"ds_read_b128 a[144:147], v5 offset:32768\n"
"v_mfma_f32_16x16x32_bf16 v[156:159], a[148:151], a[184:187], v[156:159]\n"
"s_waitcnt lgkmcnt(4)\n"
"v_mfma_f32_16x16x32_bf16 v[160:163], a[152:155], a[92:95], v[160:163]\n"
"ds_read_b128 a[148:151], v5 offset:41984\n"
"v_mfma_f32_16x16x32_bf16 v[164:167], a[152:155], a[96:99], v[164:167]\n"
"v_mfma_f32_16x16x32_bf16 v[160:163], a[156:159], a[100:103], v[160:163]\n"
"ds_read_b128 a[152:155], v5 offset:52224\n"
"v_cvt_pk_bf16_f32 v48, v144, v152\n"
"v_mfma_f32_16x16x32_bf16 v[164:167], a[156:159], a[104:107], v[164:167]\n"
"v_cvt_pk_bf16_f32 v50, v145, v153\n"
"v_cvt_pk_bf16_f32 v52, v146, v154\n"
"v_cvt_pk_bf16_f32 v54, v147, v155\n"
"v_mfma_f32_16x16x32_bf16 v[160:163], a[160:163], a[108:111], v[160:163]\n"
"ds_read_b128 a[156:159], v5 offset:61440\n"
"v_cvt_pk_bf16_f32 v64, v148, v156\n"
"v_mfma_f32_16x16x32_bf16 v[164:167], a[160:163], a[112:115], v[164:167]\n"
"v_cvt_pk_bf16_f32 v66, v149, v157\n"
"s_waitcnt lgkmcnt(4)\n"
"v_mfma_f32_16x16x32_bf16 v[160:163], a[164:167], a[116:119], v[160:163]\n"
"ds_read_b128 a[160:163], v0 offset:5120\n"
"v_cvt_pk_bf16_f32 v68, v150, v158\n"
"v_mfma_f32_16x16x32_bf16 v[164:167], a[164:167], a[120:123], v[164:167]\n"
"v_cvt_pk_bf16_f32 v70, v151, v159\n"
"v_mfma_f32_16x16x32_bf16 v[160:163], a[168:171], a[124:127], v[160:163]\n"
"ds_read_b128 a[164:167], v0 offset:14336\n"
"v_mfma_f32_16x16x32_bf16 v[164:167], a[168:171], a[128:131], v[164:167]\n"
"v_mfma_f32_16x16x32_bf16 v[160:163], a[172:175], a[132:135], v[160:163]\n"
"ds_read_b128 a[168:171], v5 offset:15360\n"
"v_mfma_f32_16x16x32_bf16 v[164:167], a[172:175], a[136:139], v[164:167]\n"
"s_waitcnt lgkmcnt(4)\n"
"v_mfma_f32_16x16x32_bf16 v[160:163], a[144:147], a[140:143], v[160:163]\n"
"ds_read_b128 a[172:175], v5 offset:24576\n"
"v_mfma_f32_16x16x32_bf16 v[164:167], a[144:147], a[176:179], v[164:167]\n"
"v_mfma_f32_16x16x32_bf16 v[160:163], a[148:151], a[180:183], v[160:163]\n"
"ds_read_b128 a[144:147], v5 offset:33792\n"
"v_mfma_f32_16x16x32_bf16 v[164:167], a[148:151], a[184:187], v[164:167]\n"
"v_mfma_f32_16x16x32_bf16 v[168:171], a[152:155], a[92:95], v[168:171]\n"
"ds_read_b128 a[148:151], v5 offset:43008\n"
"v_mfma_f32_16x16x32_bf16 v[172:175], a[152:155], a[96:99], v[172:175]\n"
"s_waitcnt lgkmcnt(4)\n"
"v_mfma_f32_16x16x32_bf16 v[168:171], a[156:159], a[100:103], v[168:171]\n"
"ds_read_b128 a[152:155], v5 offset:53248\n"
"v_mfma_f32_16x16x32_bf16 v[172:175], a[156:159], a[104:107], v[172:175]\n"
"v_mfma_f32_16x16x32_bf16 v[168:171], a[160:163], a[108:111], v[168:171]\n"
"ds_read_b128 a[156:159], v5 offset:62464\n"
"v_mfma_f32_16x16x32_bf16 v[172:175], a[160:163], a[112:115], v[172:175]\n"
"v_mfma_f32_16x16x32_bf16 v[168:171], a[164:167], a[116:119], v[168:171]\n"
"ds_read_b128 a[160:163], v0 offset:6144\n"
"v_mfma_f32_16x16x32_bf16 v[172:175], a[164:167], a[120:123], v[172:175]\n"
"s_waitcnt lgkmcnt(4)\n"
"v_mfma_f32_16x16x32_bf16 v[168:171], a[168:171], a[124:127], v[168:171]\n"
"ds_read_b128 a[164:167], v0 offset:15360\n"
"v_mfma_f32_16x16x32_bf16 v[172:175], a[168:171], a[128:131], v[172:175]\n"
"v_mfma_f32_16x16x32_bf16 v[168:171], a[172:175], a[132:135], v[168:171]\n"
"ds_read_b128 a[168:171], v5 offset:16384\n"
"v_mfma_f32_16x16x32_bf16 v[172:175], a[172:175], a[136:139], v[172:175]\n"
"v_mfma_f32_16x16x32_bf16 v[168:171], a[144:147], a[140:143], v[168:171]\n"
"ds_read_b128 a[172:175], v5 offset:25600\n"
"v_mfma_f32_16x16x32_bf16 v[172:175], a[144:147], a[176:179], v[172:175]\n"
"s_waitcnt lgkmcnt(4)\n"
"v_mfma_f32_16x16x32_bf16 v[168:171], a[148:151], a[180:183], v[168:171]\n"
"ds_read_b128 a[144:147], v5 offset:34816\n"
"v_mfma_f32_16x16x32_bf16 v[172:175], a[148:151], a[184:187], v[172:175]\n"
"v_mfma_f32_16x16x32_bf16 v[176:179], a[152:155], a[92:95], v[176:179]\n"
"ds_read_b128 a[148:151], v5 offset:44032\n"
"v_mfma_f32_16x16x32_bf16 v[180:183], a[152:155], a[96:99], v[180:183]\n"
"v_mfma_f32_16x16x32_bf16 v[176:179], a[156:159], a[100:103], v[176:179]\n"
"s_nop 1\n"
"v_cvt_pk_bf16_f32 v49, v160, v168\n"
"v_cvt_pk_bf16_f32 v51, v161, v169\n"
"v_mfma_f32_16x16x32_bf16 v[180:183], a[156:159], a[104:107], v[180:183]\n"
"s_waitcnt vmcnt(0)\n"
"v_cvt_pk_bf16_f32 v53, v162, v170\n"
"s_waitcnt lgkmcnt(3)\n"
"v_mfma_f32_16x16x32_bf16 v[176:179], a[160:163], a[108:111], v[176:179]\n"
"s_mov_b64 exec, 0xff\n"
"buffer_store_dwordx4 v[16:19], v2, s[16:19], s38 offen sc1\n"
"s_mov_b64 exec, -1\n"
"v_mfma_f32_16x16x32_bf16 v[180:183], a[160:163], a[112:115], v[180:183]\n"
"v_cvt_pk_bf16_f32 v55, v163, v171\n"
"v_cvt_pk_bf16_f32 v65, v164, v172\n"
"v_mfma_f32_16x16x32_bf16 v[176:179], a[164:167], a[116:119], v[176:179]\n"
"v_cvt_pk_bf16_f32 v67, v165, v173\n"
"v_cvt_pk_bf16_f32 v69, v166, v174\n"
"v_mfma_f32_16x16x32_bf16 v[180:183], a[164:167], a[120:123], v[180:183]\n"
"v_cvt_pk_bf16_f32 v71, v167, v175\n"
"v_mfma_f32_16x16x32_bf16 v[176:179], a[168:171], a[124:127], v[176:179]\n"
"buffer_store_dwordx4 v[48:51], v24, s[16:19], s23 offen offset:2048 sc1\n"
"v_mfma_f32_16x16x32_bf16 v[180:183], a[168:171], a[128:131], v[180:183]\n"
"buffer_store_dwordx4 v[52:55], v24, s[16:19], s23 offen offset:3072 sc1\n"
"s_waitcnt lgkmcnt(0)\n"
"v_mfma_f32_16x16x32_bf16 v[176:179], a[172:175], a[132:135], v[176:179]\n"
"buffer_store_dwordx4 v[64:67], v24, s[16:19], s24 offen offset:2048 sc1\n"
"v_mfma_f32_16x16x32_bf16 v[180:183], a[172:175], a[136:139], v[180:183]\n"
"buffer_store_dwordx4 v[68:71], v24, s[16:19], s24 offen offset:3072 sc1\n"
"v_mfma_f32_16x16x32_bf16 v[176:179], a[144:147], a[140:143], v[176:179]\n"
"v_mfma_f32_16x16x32_bf16 v[180:183], a[144:147], a[176:179], v[180:183]\n"
"v_mfma_f32_16x16x32_bf16 v[176:179], a[148:151], a[180:183], v[176:179]\n"
"v_mfma_f32_16x16x32_bf16 v[180:183], a[148:151], a[184:187], v[180:183]\n"
"s_nop 6\n"
"v_cvt_pk_bf16_f32 v72, v176, v177\n"
"v_cvt_pk_bf16_f32 v73, v178, v179\n"
"v_cvt_pk_bf16_f32 v74, v180, v181\n"
"v_cvt_pk_bf16_f32 v75, v182, v183\n"
"buffer_store_dwordx4 v[72:75], v24, s[16:19], s25 offen sc1\n"
"s_sleep 9\n"
"buffer_load_dword v35, v33, s[16:19], s39 offen sc1\n"
"s_waitcnt vmcnt(1)\n"
"s_mov_b64 exec, 0xff\n"
"buffer_store_dwordx4 v[16:19], v2, s[16:19], s36 offen sc1\n"
"s_mov_b64 exec, -1\n"
"s_waitcnt vmcnt(1)\n"
"v_cmp_ne_u32 vcc, s55, v35\n"
"s_cbranch_vccnz .Lmk_SLOW_9\n"
".Lmk_BACK_9:\n"
"buffer_load_dwordx4 v[76:79], v2, s[16:19], s26 offen offset:0 sc1\n"
"buffer_load_dwordx4 v[80:83], v2, s[16:19], s26 offen offset:1024 sc1\n"
"buffer_load_dwordx4 v[92:95], v3, s[16:19], s26 offen offset:0 sc1\n"
"buffer_load_dwordx4 v[96:99], v3, s[16:19], s26 offen offset:1024 sc1\n"
"s_sleep 6\n"
"buffer_load_dword v34, v33, s[16:19], s37 offen sc1\n"
"s_waitcnt vmcnt(0)\n"
"v_cmp_ne_u32 vcc, s55, v34\n"
"s_cbranch_vccnz .Lmk_SLOW_10\n"
".Lmk_BACK_10:\n"
"buffer_load_dwordx4 v[84:87], v2, s[16:19], s26 offen offset:2048 sc1\n"
"buffer_load_dwordx4 v[88:91], v2, s[16:19], s26 offen offset:3072 sc1\n"
"buffer_load_dwordx4 v[100:103], v3, s[16:19], s26 offen offset:2048 sc1\n"
"buffer_load_dwordx4 v[104:107], v3, s[16:19], s26 offen offset:3072 sc1\n"
"buffer_load_dwordx4 v[108:111], v4, s[16:19], s26 offen offset:0 sc1\n"
"v_mfma_f32_16x16x32_bf16 v[236:239], v[8:11], v[76:79], v[236:239]\n"
"v_mfma_f32_16x16x32_bf16 v[236:239], v[12:15], v[80:83], v[236:239]\n"
"v_mfma_f32_16x16x32_bf16 v[244:247], v[8:11], v[92:95], v[244:247]\n"
"v_mfma_f32_16x16x32_bf16 v[244:247], v[12:15], v[96:99], v[244:247]\n"
"s_nop 5\n"
"v_cvt_pk_bf16_f32 v40, v236, v237\n"
"v_cvt_pk_bf16_f32 v41, v238, v239\n"
"buffer_store_dwordx2 v[40:41], v25, s[44:47], s20 offen offset:0\n"
"v_cvt_pk_bf16_f32 v44, v244, v245\n"
"v_cvt_pk_bf16_f32 v45, v246, v247\n"
"buffer_store_dwordx2 v[44:45], v25, s[44:47], s21 offen offset:0\n"
"s_waitcnt vmcnt(6)\n"
"v_mfma_f32_16x16x32_bf16 v[240:243], v[8:11], v[84:87], v[240:243]\n"
"s_waitcnt vmcnt(5)\n"
"v_mfma_f32_16x16x32_bf16 v[240:243], v[12:15], v[88:91], v[240:243]\n"
"s_waitcnt vmcnt(4)\n"
"v_mfma_f32_16x16x32_bf16 v[248:251], v[8:11], v[100:103], v[248:251]\n"
"s_waitcnt vmcnt(3)\n"
"v_mfma_f32_16x16x32_bf16 v[248:251], v[12:15], v[104:107], v[248:251]\n"
"s_waitcnt vmcnt(2)\n"
"v_mfma_f32_16x16x32_bf16 v[252:255], v[28:31], v[108:111], v[252:255]\n"
"s_nop 1\n"
"v_cvt_pk_bf16_f32 v42, v240, v241\n"
"v_cvt_pk_bf16_f32 v43, v242, v243\n"
"buffer_store_dwordx2 v[42:43], v25, s[44:47], s20 offen offset:128\n"
"s_nop 0\n"
"v_cvt_pk_bf16_f32 v46, v248, v249\n"
"v_cvt_pk_bf16_f32 v47, v250, v251\n"
"buffer_store_dwordx2 v[46:47], v25, s[44:47], s21 offen offset:128\n"
"v_cvt_pk_bf16_f32 v48, v252, v253\n"
"buffer_store_short v48, v26, s[44:47], s22 offen\n"
"buffer_store_short_d16_hi v48, v26, s[44:47], s22 offen offset:8\n"
"s_endpgm\n"
".Lmk_SLOW_0:\n"
"s_sub_u32 s59, s59, 1\n"
"s_cbranch_scc1 .Lmk_GIVEUP_0\n"
"s_sleep 1\n"
"buffer_load_dword v34, v33, s[16:19], s71 offen sc1\n"
"buffer_load_dword v36, v35, s[16:19], s65 offen sc1\n"
"s_waitcnt vmcnt(0)\n"
"v_xor_b32 v34, s55, v34\n"
"s_nop 1\n"
"v_xor_b32 v36, s55, v36\n"
"s_nop 1\n"
"v_or_b32 v34, v34, v36\n"
"s_nop 1\n"
"v_cmp_ne_u32 vcc, 0, v34\n"
"s_nop 1\n"
"s_cbranch_vccnz .Lmk_SLOW_0\n"
"s_branch .Lmk_BACK_0\n"
".Lmk_GIVEUP_0:\n"
"s_mov_b32 s59, 0\n"
"s_branch .Lmk_BACK_0\n"
".Lmk_SLOW_1:\n"
"s_sub_u32 s59, s59, 1\n"
"s_cbranch_scc1 .Lmk_GIVEUP_1\n"
"s_sleep 1\n"
"buffer_load_dwordx4 v[184:187], v2, s[16:19], s69 offen offset:0 sc1\n"
"buffer_load_dwordx4 v[188:191], v2, s[16:19], s69 offen offset:1024 sc1\n"
"buffer_load_dwordx4 v[192:195], v2, s[16:19], s69 offen offset:2048 sc1\n"
"buffer_load_dwordx4 v[196:199], v2, s[16:19], s69 offen offset:3072 sc1\n"
"buffer_load_dwordx4 v[200:203], v3, s[16:19], s69 offen offset:0 sc1\n"
"buffer_load_dwordx4 v[204:207], v3, s[16:19], s69 offen offset:1024 sc1\n"
"buffer_load_dwordx4 v[208:211], v3, s[16:19], s69 offen offset:2048 sc1\n"
"buffer_load_dwordx4 v[212:215], v3, s[16:19], s69 offen offset:3072 sc1\n"
"s_waitcnt vmcnt(0)\n"
"v_and_b32 v216, v185, v187\n"
"s_nop 1\n"
"v_or3_b32 v217, v185, v187, v17\n"
"s_nop 1\n"
"v_bitop3_b32 v216, v216, v189, v191 bitop3:0x80\n"
"s_nop 1\n"
"v_or3_b32 v217, v217, v189, v191\n"
"s_nop 1\n"
"v_bitop3_b32 v216, v216, v193, v195 bitop3:0x80\n"
"s_nop 1\n"
"v_or3_b32 v217, v217, v193, v195\n"
"s_nop 1\n"
"v_bitop3_b32 v216, v216, v197, v199 bitop3:0x80\n"
"s_nop 1\n"
"v_or3_b32 v217, v217, v197, v199\n"
"s_nop 1\n"
"v_bitop3_b32 v216, v216, v201, v203 bitop3:0x80\n"
"s_nop 1\n"
"v_or3_b32 v217, v217, v201, v203\n"
"s_nop 1\n"
"v_bitop3_b32 v216, v216, v205, v207 bitop3:0x80\n"
"s_nop 1\n"
"v_or3_b32 v217, v217, v205, v207\n"
"s_nop 1\n"
"v_bitop3_b32 v216, v216, v209, v211 bitop3:0x80\n"
"s_nop 1\n"
"v_or3_b32 v217, v217, v209, v211\n"
"s_nop 1\n"
"v_bitop3_b32 v216, v216, v213, v215 bitop3:0x80\n"
"s_nop 1\n"
"v_or3_b32 v217, v217, v213, v215\n"
"s_nop 1\n"
"v_and_b32 v216, v216, v17\n"
"s_nop 1\n"
"v_cmp_ne_u32 vcc, v216, v217\n"
"s_nop 1\n"
"s_cbranch_vccnz .Lmk_SLOW_1\n"
"s_branch .Lmk_BACK_1\n"
".Lmk_GIVEUP_1:\n"
"s_mov_b32 s59, 0\n"
"s_branch .Lmk_BACK_1\n"
".Lmk_SLOW_2:\n"
"s_sub_u32 s59, s59, 1\n"
"s_cbranch_scc1 .Lmk_GIVEUP_2\n"
"s_sleep 1\n"
"buffer_load_dword v34, v33, s[16:19], s71 offen sc1\n"
"s_waitcnt vmcnt(0)\n"
"v_cmp_ne_u32 vcc, s55, v34\n"
"s_nop 1\n"
"s_cbranch_vccnz .Lmk_SLOW_2\n"
"s_branch .Lmk_BACK_2\n"
".Lmk_GIVEUP_2:\n"
"s_mov_b32 s59, 0\n"
"s_branch .Lmk_BACK_2\n"
".Lmk_SLOW_3:\n"
"s_sub_u32 s59, s59, 1\n"
"s_cbranch_scc1 .Lmk_GIVEUP_3\n"
"s_sleep 1\n"
"buffer_load_dwordx2 v[194:195], v192, s[16:19], s69 offen sc1\n"
"s_waitcnt vmcnt(0)\n"
"v_xor_b32 v194, s55, v194\n"
"s_nop 1\n"
"v_xor_b32 v195, s55, v195\n"
"s_nop 1\n"
"v_or_b32 v194, v194, v195\n"
"s_nop 1\n"
"v_cmp_ne_u32 vcc, 0, v194\n"
"s_nop 1\n"
"s_cbranch_vccnz .Lmk_SLOW_3\n"
"s_branch .Lmk_BACK_3\n"
".Lmk_GIVEUP_3:\n"
"s_mov_b32 s59, 0\n"
"s_branch .Lmk_BACK_3\n"
".Lmk_SLOW_4:\n"
"s_sub_u32 s59, s59, 1\n"
"s_cbranch_scc1 .Lmk_GIVEUP_4\n"
"s_sleep 1\n"
"buffer_load_dword v193, v192, s[16:19], s71 offen sc1\n"
"s_waitcnt vmcnt(0)\n"
"v_cmp_ne_u32 vcc, s55, v193\n"
"s_nop 1\n"
"s_cbranch_vccnz .Lmk_SLOW_4\n"
"s_branch .Lmk_BACK_4\n"
".Lmk_GIVEUP_4:\n"
"s_mov_b32 s59, 0\n"
"s_branch .Lmk_BACK_4\n"
".Lmk_SLOW_5:\n"
"s_sub_u32 s59, s59, 1\n"
"s_cbranch_scc1 .Lmk_GIVEUP_5\n"
"s_sleep 1\n"
"buffer_load_dwordx2 v[196:197], v224, s[16:19], s69 offen sc1\n"
"buffer_load_dwordx2 v[198:199], v225, s[16:19], s69 offen sc1\n"
"buffer_load_dwordx2 v[212:213], v226, s[16:19], s69 offen sc1\n"
"s_waitcnt vmcnt(0)\n"
"v_bitop3_b32 v192, v197, v199, v213 bitop3:0x80\n"
"s_nop 1\n"
"v_or3_b32 v193, v197, v199, v213\n"
"s_nop 1\n"
"v_and_b32 v192, v192, v17\n"
"s_nop 1\n"
"v_or_b32 v193, v193, v17\n"
"s_nop 1\n"
"v_cmp_ne_u32 vcc, v192, v193\n"
"s_nop 1\n"
"s_cbranch_vccnz .Lmk_SLOW_5\n"
"s_branch .Lmk_BACK_5\n"
".Lmk_GIVEUP_5:\n"
"s_mov_b32 s59, 0\n"
"s_branch .Lmk_BACK_5\n"
".Lmk_SLOW_6:\n"
"s_sub_u32 s59, s59, 1\n"
"s_cbranch_scc1 .Lmk_GIVEUP_6\n"
"s_sleep 1\n"
"buffer_load_dword v34, v33, s[16:19], s70 offen sc1\n"
"s_waitcnt vmcnt(0)\n"
"v_cmp_ne_u32 vcc, s55, v34\n"
"s_nop 1\n"
"s_cbranch_vccnz .Lmk_SLOW_6\n"
"s_branch .Lmk_BACK_6\n"
".Lmk_GIVEUP_6:\n"
"s_mov_b32 s59, 0\n"
"s_branch .Lmk_BACK_6\n"
".Lmk_SLOW_7:\n"
"s_sub_u32 s59, s59, 1\n"
"s_cbranch_scc1 .Lmk_GIVEUP_7\n"
"s_sleep 1\n"
"buffer_load_dword v35, v33, s[16:19], s72 offen sc1\n"
"s_waitcnt vmcnt(0)\n"
"v_cmp_ne_u32 vcc, s55, v35\n"
"s_nop 1\n"
"s_cbranch_vccnz .Lmk_SLOW_7\n"
"s_branch .Lmk_BACK_7\n"
".Lmk_GIVEUP_7:\n"
"s_mov_b32 s59, 0\n"
"s_branch .Lmk_BACK_7\n"
".Lmk_SLOW_8:\n"
"s_sub_u32 s59, s59, 1\n"
"s_cbranch_scc1 .Lmk_GIVEUP_8\n"
"s_sleep 1\n"
"buffer_load_dword v36, v33, s[16:19], s74 offen sc1\n"
"s_waitcnt vmcnt(0)\n"
"v_cmp_ne_u32 vcc, s55, v36\n"
"s_nop 1\n"
"s_cbranch_vccnz .Lmk_SLOW_8\n"
"s_branch .Lmk_BACK_8\n"
".Lmk_GIVEUP_8:\n"
"s_mov_b32 s59, 0\n"
"s_branch .Lmk_BACK_8\n"
".Lmk_SLOW_9:\n"
"s_sub_u32 s59, s59, 1\n"
"s_cbranch_scc1 .Lmk_GIVEUP_9\n"
"s_sleep 1\n"
"buffer_load_dword v35, v33, s[16:19], s39 offen sc1\n"
"s_waitcnt vmcnt(0)\n"
"v_cmp_ne_u32 vcc, s55, v35\n"
"s_nop 1\n"
"s_cbranch_vccnz .Lmk_SLOW_9\n"
"s_branch .Lmk_BACK_9\n"
".Lmk_GIVEUP_9:\n"
"s_mov_b32 s59, 0\n"
"s_branch .Lmk_BACK_9\n"
".Lmk_SLOW_10:\n"
"s_sub_u32 s59, s59, 1\n"
"s_cbranch_scc1 .Lmk_GIVEUP_10\n"
"s_sleep 1\n"
"buffer_load_dword v34, v33, s[16:19], s37 offen sc1\n"
"s_waitcnt vmcnt(0)\n"
"v_cmp_ne_u32 vcc, s55, v34\n"
"s_nop 1\n"
"s_cbranch_vccnz .Lmk_SLOW_10\n"
"s_branch .Lmk_BACK_10\n"
".Lmk_GIVEUP_10:\n"
"s_mov_b32 s59, 0\n"
"s_branch .Lmk_BACK_10\n"
".Lmk_CODE_END:\n"
    : "+{v0}"(tid)
    : [ka] "s"(__builtin_amdgcn_kernarg_segment_ptr()), [bid] "s"(blockIdx.x)
    : "v1","v2","v3","v4","v5","v6","v7","v8","v9","v10","v11","v12","v13","v14","v15","v16","v17","v18","v19","v20","v21","v22","v23","v24","v25","v26","v27","v28","v29","v30","v31","v32","v33","v34","v35","v36","v37","v38","v39","v40","v41","v42","v43","v44","v45","v46","v47","v48","v49","v50","v51","v52","v53","v54","v55","v56","v57","v58","v59","v60","v61","v62","v63","v64","v65","v66","v67","v68","v69","v70","v71","v72","v73","v74","v75","v76","v77","v78","v79","v80","v81","v82","v83","v84","v85","v86","v87","v88","v89","v90","v91","v92","v93","v94","v95","v96","v97","v98","v99","v100","v101","v102","v103","v104","v105","v106","v107","v108","v109","v110","v111","v112","v113","v114","v115","v116","v117","v118","v119","v120","v121","v122","v123","v124","v125","v126","v127","v128","v129","v130","v131","v132","v133","v134","v135","v136","v137","v138","v139","v140","v141","v142","v143","v144","v145","v146","v147","v148","v149","v150","v151","v152","v153","v154","v155","v156","v157","v158","v159","v160","v161","v162","v163","v164","v165","v166","v167","v168","v169","v170","v171","v172","v173","v174","v175","v176","v177","v178","v179","v180","v181","v182","v183","v184","v185","v186","v187","v188","v189","v190","v191","v192","v193","v194","v195","v196","v197","v198","v199","v200","v201","v202","v203","v204","v205","v206","v207","v208","v209","v210","v211","v212","v213","v214","v215","v216","v217","v218","v219","v220","v221","v222","v223","v224","v225","v226","v227","v228","v229","v230","v231","v232","v233","v234","v235","v236","v237","v238","v239","v240","v241","v242","v243","v244","v245","v246","v247","v248","v249","v250","v251","v252","v253","v254","v255","a0","a1","a2","a3","a4","a5","a6","a7","a8","a9","a10","a11","a12","a13","a14","a15","a16","a17","a18","a19","a20","a21","a22","a23","a24","a25","a26","a27","a28","a29","a30","a31","a32","a33","a34","a35","a36","a37","a38","a39","a40","a41","a42","a43","a44","a45","a46","a47","a48","a49","a50","a51","a52","a53","a54","a55","a56","a57","a58","a59","a60","a61","a62","a63","a64","a65","a66","a67","a68","a69","a70","a71","a72","a73","a74","a75","a76","a77","a78","a79","a80","a81","a82","a83","a84","a85","a86","a87","a88","a89","a90","a91","a92","a93","a94","a95","a96","a97","a98","a99","a100","a101","a102","a103","a104","a105","a106","a107","a108","a109","a110","a111","a112","a113","a114","a115","a116","a117","a118","a119","a120","a121","a122","a123","a124","a125","a126","a127","a128","a129","a130","a131","a132","a133","a134","a135","a136","a137","a138","a139","a140","a141","a142","a143","a144","a145","a146","a147","a148","a149","a150","a151","a152","a153","a154","a155","a156","a157","a158","a159","a160","a161","a162","a163","a164","a165","a166","a167","a168","a169","a170","a171","a172","a173","a174","a175","a176","a177","a178","a179","a180","a181","a182","a183","a184","a185","a186","a187","a188","a189","a190","a191","a192","a193","a194","a195","a196","a197","a198","a199","a200","a201","a202","a203","a204","a205","a206","a207","a208","a209","a210","a211","a212","a213","a214","a215","a216","a217","a218","a219","a220","a221","a222","a223","a224","a225","a226","a227","a228","a229","a230","a231","a232","a233","a234","a235","a236","a237","a238","a239","a240","a241","a242","a243","a244","a245","a246","a247","a248","a249","a250","a251","a252","a253","a254","a255","s4","s5","s6","s7","s8","s9","s10","s11","s12","s13","s14","s15","s16","s17","s18","s19","s20","s21","s22","s23","s24","s25","s26","s27","s28","s29","s30","s31","s33","s34","s35","s36","s37","s38","s39","s40","s41","s42","s43","s44","s45","s46","s47","s48","s49","s50","s51","s52","s53","s54","s55","s56","s57","s58","s59","s60","s61","s62","s63","s64","s65","s66","s67","s68","s69","s70","s71","s72","s73","s74","s75","s76","s77","s78","s79","s80","s81","s82","s83","s84","s85","s86","s87","s88","s89","s90","s91","s92","s93","s94","s95","s96","s97","s98","s99","vcc","memory");
}

extern "C" void kernel_launch(void* const* d_in, const int* in_sizes, int n_in,
                              void* d_out, int out_size, void* d_ws, size_t ws_size,
                              hipStream_t stream) {
  MegaArgs a;
  const void* p[10] = {d_in[0], d_in[5], d_ws, d_in[1], d_in[6], d_in[8], d_in[3], d_in[2], d_in[7], d_out};
  const unsigned n[10] = {2048u * 1152u, 576u * 9216u, 0x8000000u, 576u * 3456u, 2048u * 4096u, 2304u * 1152u,
                          576u * 1152u, 32u * 648u, 4096u, 2048u * 1152u};
  for (int i = 0; i < 10; ++i) { a.d[i].p = p[i]; a.d[i].bytes = n[i]; a.d[i].fmt = 0x20000u; }
  (void)hipFuncSetAttribute((const void*)parallel_sum__linear_kernel_hedgehog__mixer_gmlp__prompt_kernel,
                            hipFuncAttributeMaxDynamicSharedMemorySize, 163840);
  parallel_sum__linear_kernel_hedgehog__mixer_gmlp__prompt_kernel<<<256, 256, 163840, stream>>>(a);
}
```
